```python
import torch

def _softmax(x, axis=-1):
    return torch.softmax(x, dim=axis)
def _gelu(x):
    return torch.nn.functional.gelu(x, approximate="tanh")
def _silu(x):
    return torch.nn.functional.silu(x)
def _sigmoid(x):
    return torch.sigmoid(x)
def _layernorm(x, axis=-1, eps=1e-5):
    m = x.mean(dim=axis, keepdim=True)
    v = x.var(dim=axis, keepdim=True, unbiased=False)
    return (x - m) / torch.sqrt(v + eps)
def _scatter_add(tgt, idx, src, axis=0):
    return tgt.clone().index_add_(axis, idx, src)
def _segment_sum(src, seg, n):
    out = torch.zeros((n,) + src.shape[1:], dtype=src.dtype)
    return out.index_add_(0, seg, src)
def _pad(x, pw):
    flat = tuple(v for pair in reversed(pw) for v in pair)
    return torch.nn.functional.pad(x, flat)

kernel_name = 'sequential_preLN__sliding_block_cached__dense_activation_gelu__decode'

def setup_inputs():

    _w = (lambda a: (lambda t: t.to(torch.bfloat16) if t.is_floating_point() else t)(torch.as_tensor(a)))
    def _A_setup():
        B, Sq, D = 8, 1, 512
        Hq, Hkv, Sk, Dh = 32, 32, 4096, 128
        assert Hq % Hkv == 0, (Hq, Hkv)
        return {
            "x": _w((lambda *s: torch.randn(*s, dtype=torch.bfloat16) * 0.05)(B, Sq, D)),
            "k_cache": _w((lambda *s: torch.randn(*s, dtype=torch.bfloat16) * 1.6)(B, Hkv, Sk, Dh)),
            "v_cache": _w((lambda *s: torch.randn(*s, dtype=torch.bfloat16) * 0.4)(B, Hkv, Sk, Dh)),
            "wq": _w((lambda *s: torch.randn(*s, dtype=torch.bfloat16) * 1.6)(D, Hq * Dh) * (D ** -0.5)),
            "wo": _w((lambda *s: torch.randn(*s, dtype=torch.bfloat16) * 0.4)(Hq * Dh, D) * ((Hq * Dh) ** -0.5)),
        }
    _A_in = _A_setup()

    def _B_setup():
        B, S, D, F = 8, 1, 512, 1376
        return {
            "x": _w((lambda *s: torch.randn(*s, dtype=torch.bfloat16) * 0.05)(B, S, D)),
            "w1": _w((lambda *s: torch.randn(*s, dtype=torch.bfloat16) * 0.8)(D, F) * (D ** -0.5)),
            "b1": _w((lambda *s: torch.randn(*s, dtype=torch.bfloat16) * 0.8)(F) * 0.01),
            "w2": _w((lambda *s: torch.randn(*s, dtype=torch.bfloat16) * 0.1)(F, D) * (F ** -0.5)),
            "b2": _w((lambda *s: torch.randn(*s, dtype=torch.bfloat16) * 0.1)(D) * 0.01),
        }
    _B_in = _B_setup()

    return {
        "A_x": _A_in["x"],
        "A_k_cache": _A_in["k_cache"],
        "A_v_cache": _A_in["v_cache"],
        "A_wq": _A_in["wq"],
        "A_wo": _A_in["wo"],
        "B_x": _B_in["x"],
        "B_w1": _B_in["w1"],
        "B_b1": _B_in["b1"],
        "B_w2": _B_in["w2"],
        "B_b2": _B_in["b2"],
    }

def reference(A_x, A_k_cache, A_v_cache, A_wq, A_wo, B_x, B_w1, B_b1, B_w2, B_b2):
    def _A_ref(x, k_cache, v_cache, wq, wo):
        B, Sq, D = x.shape
        Hq, Dh = 32, 128
        Hkv, Sk = k_cache.shape[1], k_cache.shape[2]
        W = min(4096, max(256, Sk // 4))
        rep = Hq // Hkv
        q = (lambda x, *d: x.permute(*d))((x @ wq).reshape(B, Sq, Hq, Dh), 0, 2, 1, 3)
        k = (lambda x, n, axis: torch.repeat_interleave(x, n, dim=axis))(k_cache[:, :, Sk - W:, :], rep, axis=1)
        v = (lambda x, n, axis: torch.repeat_interleave(x, n, dim=axis))(v_cache[:, :, Sk - W:, :], rep, axis=1)
        scores = torch.einsum("bhqd,bhkd->bhqk", q, k) * (Dh ** -0.5)
        p = _softmax(scores, axis=-1)
        o = torch.einsum("bhqk,bhkd->bhqd", p, v)
        return (lambda x, *d: x.permute(*d))(o, 0, 2, 1, 3).reshape(B, Sq, Hq * Dh) @ wo

    def _B_ref(x, w1, b1, w2, b2):
        h = torch.einsum("bsd,df->bsf", x, w1) + b1
        h = _gelu(h)
        return torch.einsum("bsf,fd->bsd", h, w2) + b2

    x = A_x
    h = x + _A_ref(_layernorm(x, axis=-1), A_k_cache, A_v_cache, A_wq, A_wo)
    h2 = h + _B_ref(_layernorm(h, axis=-1), B_w1, B_b1, B_w2, B_b2)
    return h2
```

```cpp
#include <hip/hip_runtime.h>
#include <hip/hip_bf16.h>
#include <stdint.h>

typedef unsigned u32x4 __attribute__((ext_vector_type(4)));
typedef float f32x4 __attribute__((ext_vector_type(4)));
typedef __bf16 bf16x2 __attribute__((ext_vector_type(2)));

constexpr int B = 8, D = 512, H = 32, SK = 4096, DH = 128, W = 1024, F = 1376;

__device__ __forceinline__ float dot2(unsigned a, unsigned b, float acc) {
  return __builtin_amdgcn_fdot2_f32_bf16(__builtin_bit_cast(bf16x2, a), __builtin_bit_cast(bf16x2, b), acc, false);
}
__device__ __forceinline__ float bflo(unsigned u) { return __uint_as_float(u << 16); }
__device__ __forceinline__ float bfhi(unsigned u) { return __uint_as_float(u & 0xffff0000u); }
__device__ __forceinline__ float bf2f(unsigned short u) { return __uint_as_float((unsigned)u << 16); }
__device__ __forceinline__ unsigned short f2bf(float f) { return __builtin_bit_cast(unsigned short, (__bf16)f); }
__device__ __forceinline__ float rbf(float f) { return bf2f(f2bf(f)); }
__device__ __forceinline__ float wave_sum(float v) {
  for (int o = 32; o > 0; o >>= 1) v += __shfl_xor(v, o);
  return v;
}
__device__ __forceinline__ float wave_max(float v) {
  for (int o = 32; o > 0; o >>= 1) v = fmaxf(v, __shfl_xor(v, o));
  return v;
}

__global__ void __launch_bounds__(512) k_attn(const unsigned short* __restrict__ x, const unsigned short* __restrict__ kc,
                                              const unsigned short* __restrict__ vc, const unsigned short* __restrict__ wq,
                                              const unsigned short* __restrict__ wo, float* __restrict__ part) {
  __shared__ float xn[D];
  __shared__ float red[8][512];
  __shared__ float qs[DH];
  __shared__ float ps[W];
  __shared__ float ml[16];
  const int bh = blockIdx.x, b = bh >> 5, h = bh & 31;
  const int tid = threadIdx.x, lane = tid & 63, w = tid >> 6, r = lane >> 4, c = lane & 15;
  u32x4 xv = *(const u32x4*)(x + b * D + lane * 8);
  float xf[8];
  for (int k = 0; k < 4; ++k) { xf[2 * k] = bflo(xv[k]); xf[2 * k + 1] = bfhi(xv[k]); }
  float s = 0; for (int k = 0; k < 8; ++k) s += xf[k];
  const float mean = wave_sum(s) * (1.f / D);
  float vs = 0; for (int k = 0; k < 8; ++k) { float d = xf[k] - mean; vs += d * d; }
  const float rstd = 1.f / sqrtf(wave_sum(vs) * (1.f / D) + 1e-5f);
  if (w == 0) for (int k = 0; k < 8; ++k) xn[lane * 8 + k] = rbf((xf[k] - mean) * rstd);
  __syncthreads();
  float acc[8] = {0, 0, 0, 0, 0, 0, 0, 0};
#pragma unroll
  for (int i = 0; i < 16; ++i) {
    const int d = w * 64 + i * 4 + r;
    u32x4 wv = *(const u32x4*)(wq + (size_t)d * (H * DH) + h * DH + c * 8);
    const float xx = xn[d];
    for (int k = 0; k < 4; ++k) { acc[2 * k] += xx * bflo(wv[k]); acc[2 * k + 1] += xx * bfhi(wv[k]); }
  }
  for (int k = 0; k < 8; ++k) red[w][r * 128 + c * 8 + k] = acc[k];
  __syncthreads();
  if (tid < DH) { float t = 0; for (int i = 0; i < 8; ++i) for (int j = 0; j < 4; ++j) t += red[i][j * 128 + tid]; qs[tid] = t; }
  __syncthreads();
  unsigned qpk[4];
  for (int k = 0; k < 4; ++k) qpk[k] = (unsigned)f2bf(qs[c * 8 + 2 * k]) | ((unsigned)f2bf(qs[c * 8 + 2 * k + 1]) << 16);
  const size_t base = ((size_t)bh * SK + (SK - W) + w * 128) * DH + lane * 8;
  float mx = -INFINITY;
#pragma unroll 8
  for (int i = 0; i < 32; ++i) {
    u32x4 kv = __builtin_nontemporal_load((const u32x4*)(kc + base + i * 512));
    float d = 0; for (int k = 0; k < 4; ++k) d = dot2(kv[k], qpk[k], d);
    d += __shfl_xor(d, 1); d += __shfl_xor(d, 2); d += __shfl_xor(d, 4); d += __shfl_xor(d, 8);
    d *= 0.08838834764831845f;
    if (c == 0) ps[w * 128 + i * 4 + r] = d;
    mx = fmaxf(mx, d);
  }
  mx = wave_max(mx);
  if (lane == 0) ml[w] = mx;
  __syncthreads();
  float M = ml[0]; for (int i = 1; i < 8; ++i) M = fmaxf(M, ml[i]);
  float p0 = __expf(ps[tid] - M), p1 = __expf(ps[tid + 512] - M);
  float ls = wave_sum(p0 + p1);
  ps[tid] = p0; ps[tid + 512] = p1;
  if (lane == 0) ml[8 + w] = ls;
  __syncthreads();
  float L = 0; for (int i = 0; i < 8; ++i) L += ml[8 + i];
  for (int k = 0; k < 8; ++k) acc[k] = 0;
#pragma unroll 8
  for (int i = 0; i < 32; ++i) {
    u32x4 vv = __builtin_nontemporal_load((const u32x4*)(vc + base + i * 512));
    const float p = ps[w * 128 + i * 4 + r];
    for (int k = 0; k < 4; ++k) { acc[2 * k] += p * bflo(vv[k]); acc[2 * k + 1] += p * bfhi(vv[k]); }
  }
  for (int k = 0; k < 8; ++k) red[w][r * 128 + c * 8 + k] = acc[k];
  __syncthreads();
  if (tid < DH) { float t = 0; for (int i = 0; i < 8; ++i) for (int j = 0; j < 4; ++j) t += red[i][j * 128 + tid]; qs[tid] = rbf(t / L); }
  __syncthreads();
  for (int k = 0; k < 8; ++k) acc[k] = 0;
#pragma unroll
  for (int i = 0; i < 16; ++i) {
    const int kk = w * 16 + i;
    u32x4 wv = *(const u32x4*)(wo + (size_t)(h * DH + kk) * D + lane * 8);
    const float o = qs[kk];
    for (int k = 0; k < 4; ++k) { acc[2 * k] += o * bflo(wv[k]); acc[2 * k + 1] += o * bfhi(wv[k]); }
  }
  for (int k = 0; k < 8; ++k) red[w][lane * 8 + k] = acc[k];
  __syncthreads();
  float t = 0; for (int i = 0; i < 8; ++i) t += red[i][tid];
  part[(size_t)bh * D + tid] = t;
}

__global__ void __launch_bounds__(512) k_mlp(const unsigned short* __restrict__ x, const float* __restrict__ part,
                                             const unsigned short* __restrict__ w1, const unsigned short* __restrict__ b1,
                                             const unsigned short* __restrict__ w2, float* __restrict__ part2, float* __restrict__ hbuf) {
  __shared__ float hn[D];
  __shared__ float sm[16];
  __shared__ float red[8][64];
  __shared__ float g[64];
  const int b = blockIdx.x >> 5, j = blockIdx.x & 31;
  const int tid = threadIdx.x, lane = tid & 63, w = tid >> 6;
  float t = 0;
  for (int i = 0; i < 32; ++i) t += part[(size_t)(b * 32 + i) * D + tid];
  const float hv = rbf(bf2f(x[b * D + tid]) + rbf(t));
  if (j == 0) hbuf[b * D + tid] = hv;
  float s = wave_sum(hv);
  if (lane == 0) sm[w] = s;
  __syncthreads();
  float mean = 0; for (int i = 0; i < 8; ++i) mean += sm[i]; mean *= (1.f / D);
  float dv = hv - mean;
  s = wave_sum(dv * dv);
  if (lane == 0) sm[8 + w] = s;
  __syncthreads();
  float var = 0; for (int i = 0; i < 8; ++i) var += sm[8 + i]; var *= (1.f / D);
  hn[tid] = rbf(dv / sqrtf(var + 1e-5f));
  __syncthreads();
  float a = 0;
  if (lane < 43) for (int i = 0; i < 64; ++i) { const int d = w * 64 + i; a += hn[d] * bf2f(w1[(size_t)d * F + j * 43 + lane]); }
  red[w][lane] = a;
  __syncthreads();
  if (tid < 43) {
    float u = 0; for (int i = 0; i < 8; ++i) u += red[i][tid];
    u = rbf(rbf(u) + bf2f(b1[j * 43 + tid]));
    const float z = 0.7978845608028654f * (u + 0.044715f * u * u * u);
    g[tid] = rbf(0.5f * u * (1.f + tanhf(z)));
  }
  __syncthreads();
  a = 0;
  for (int i = 0; i < 43; ++i) a += g[i] * bf2f(w2[(size_t)(j * 43 + i) * D + tid]);
  part2[(size_t)blockIdx.x * D + tid] = a;
}

__global__ void __launch_bounds__(512) k_fin(const float* __restrict__ part2, const float* __restrict__ hbuf,
                                             const unsigned short* __restrict__ b2, unsigned short* __restrict__ out) {
  const int b = blockIdx.x, tid = threadIdx.x;
  float t = 0;
  for (int i = 0; i < 32; ++i) t += part2[(size_t)(b * 32 + i) * D + tid];
  out[b * D + tid] = f2bf(hbuf[b * D + tid] + rbf(rbf(t) + bf2f(b2[tid])));
}

extern "C" void kernel_launch(void* const* d_in, const int* in_sizes, int n_in, void* d_out, int out_size, void* d_ws,
                              size_t ws_size, hipStream_t stream) {
  const unsigned short* x = (const unsigned short*)d_in[0];
  const unsigned short* kc = (const unsigned short*)d_in[1];
  const unsigned short* vc = (const unsigned short*)d_in[2];
  const unsigned short* wq = (const unsigned short*)d_in[3];
  const unsigned short* wo = (const unsigned short*)d_in[4];
  const unsigned short* w1 = (const unsigned short*)d_in[6];
  const unsigned short* b1 = (const unsigned short*)d_in[7];
  const unsigned short* w2 = (const unsigned short*)d_in[8];
  const unsigned short* b2 = (const unsigned short*)d_in[9];
  float* part = (float*)d_ws;
  float* part2 = part + 256 * D;
  float* hbuf = part2 + 256 * D;
  k_attn<<<256, 512, 0, stream>>>(x, kc, vc, wq, wo, part);
  k_mlp<<<256, 512, 0, stream>>>(x, part, w1, b1, w2, part2, hbuf);
  k_fin<<<8, 512, 0, stream>>>(part2, hbuf, b2, (unsigned short*)d_out);
}
```

```cpp
#include <hip/hip_runtime.h>
#include <hip/hip_bf16.h>
#include <stdint.h>

#define STAMPS 0

typedef unsigned u32x4 __attribute__((ext_vector_type(4)));
typedef unsigned u32x2 __attribute__((ext_vector_type(2)));
typedef float f32x2 __attribute__((ext_vector_type(2)));
typedef float f32x4 __attribute__((ext_vector_type(4)));
typedef __bf16 bf16x2 __attribute__((ext_vector_type(2)));
typedef short s16x4 __attribute__((ext_vector_type(4)));
typedef __amdgpu_buffer_rsrc_t rsrc_t;
#define LAS __attribute__((address_space(3)))
#define DI __device__ __forceinline__

constexpr int NB = 8, D = 512, NH = 32, SK = 4096, DH = 128, WIN = 1024, F = 1376;
constexpr int FS = 48;
constexpr int AUX_NT = 2, AUX_SC1 = 16;
constexpr int OOB = 0x7ffffff0;
constexpr unsigned SENT = 0xffffffffu;
constexpr unsigned SPIN_CAP = 1u << 17;
constexpr int WS_P1 = 0;
constexpr int WS_P2 = 512 * 1024;
constexpr int WS_HB = 1024 * 1024;
constexpr int WS_GO = WS_HB + 4096;
constexpr int WS_BYTES = WS_GO + 4096;
constexpr int WS_STAMP = 2 * 1024 * 1024;
constexpr int L_RED = 0;
constexpr int L_RED2 = L_RED + 4096;
constexpr int L_EX = L_RED2 + 4096;
constexpr int L_EX2 = L_EX + 2048;
constexpr int L_H = L_EX2 + 2048;
constexpr int L_ML = L_H + 2048;
constexpr int L_W = 16384;
constexpr int L_BYTES = L_W + 128 * 1024;

DI float dot2(unsigned a, unsigned b, float acc) {
  return __builtin_amdgcn_fdot2_f32_bf16(__builtin_bit_cast(bf16x2, a), __builtin_bit_cast(bf16x2, b), acc, false);
}
DI unsigned fbits(float f) { return __builtin_bit_cast(unsigned, f); }
DI float asf(unsigned u) { return __builtin_bit_cast(float, u); }
DI unsigned cvtpk(float lo, float hi) {
  bf16x2 r = {(__bf16)lo, (__bf16)hi};
  return __builtin_bit_cast(unsigned, r);
}
DI float rbf(float f) { return asf((unsigned)__builtin_bit_cast(unsigned short, (__bf16)f) << 16); }
template <int CTRL> DI unsigned dppu(unsigned x) { return __builtin_amdgcn_mov_dpp(x, CTRL, 0xf, 0xf, true); }
template <int CTRL> DI float dppf(float x) { return asf(dppu<CTRL>(fbits(x))); }
constexpr int XOR1 = 0xB1, XOR2 = 0x4E, HMIR = 0x141, ROR4 = 0x124, ROR8 = 0x128, BCAST = 0x150;
DI float tsum32(float a, float b) {
  auto s = __builtin_amdgcn_permlane32_swap(fbits(a), fbits(b), false, false);
  return asf(s[0]) + asf(s[1]);
}
DI float tsum16(float a, float b) {
  auto s = __builtin_amdgcn_permlane16_swap(fbits(a), fbits(b), false, false);
  return asf(s[0]) + asf(s[1]);
}
DI float row_sum(float x) { x += dppf<XOR1>(x); x += dppf<XOR2>(x); x += dppf<ROR4>(x); x += dppf<ROR8>(x); return x; }
DI float row_max(float x) { x = fmaxf(x, dppf<XOR1>(x)); x = fmaxf(x, dppf<XOR2>(x)); x = fmaxf(x, dppf<ROR4>(x)); x = fmaxf(x, dppf<ROR8>(x)); return x; }
DI float wave_sum(float x) { x = row_sum(x); x = tsum16(x, x); return tsum32(x, x); }
DI float wave_max(float x) {
  x = row_max(x);
  auto s = __builtin_amdgcn_permlane16_swap(fbits(x), fbits(x), false, false);
  x = fmaxf(asf(s[0]), asf(s[1]));
  auto t = __builtin_amdgcn_permlane32_swap(fbits(x), fbits(x), false, false);
  return fmaxf(asf(t[0]), asf(t[1]));
}
DI void rows_bcast(unsigned x, unsigned (&o)[4]) {
  auto s = __builtin_amdgcn_permlane16_swap(x, x, false, false);
  auto a = __builtin_amdgcn_permlane32_swap(s[0], s[0], false, false);
  auto b = __builtin_amdgcn_permlane32_swap(s[1], s[1], false, false);
  o[0] = a[0]; o[2] = a[1]; o[1] = b[0]; o[3] = b[1];
}
#define DB(y, p, w, c) "v_dot2c_f32_bf16_dpp %" #y ", %" #p ", %" #w " row_newbcast:%" #c " row_mask:0xf bank_mask:0xf\n\t"
template <int C> DI void dots_o(float (&y)[4], const unsigned (&op)[4], const u32x2* w) {
  asm(DB(0, 4, 8, 24) DB(1, 5, 9, 24) DB(2, 6, 10, 24) DB(3, 7, 11, 24) DB(0, 4, 12, 25) DB(1, 5, 13, 25) DB(2, 6, 14, 25) DB(3, 7, 15, 25)
      DB(0, 4, 16, 26) DB(1, 5, 17, 26) DB(2, 6, 18, 26) DB(3, 7, 19, 26) DB(0, 4, 20, 27) DB(1, 5, 21, 27) DB(2, 6, 22, 27) DB(3, 7, 23, 27)
      : "+v"(y[0]), "+v"(y[1]), "+v"(y[2]), "+v"(y[3])
      : "v"(op[0]), "v"(op[1]), "v"(op[2]), "v"(op[3]), "v"(w[0][0]), "v"(w[0][1]), "v"(w[1][0]), "v"(w[1][1]), "v"(w[2][0]), "v"(w[2][1]),
        "v"(w[3][0]), "v"(w[3][1]), "v"(w[4][0]), "v"(w[4][1]), "v"(w[5][0]), "v"(w[5][1]), "v"(w[6][0]), "v"(w[6][1]), "v"(w[7][0]), "v"(w[7][1]),
        "i"(C), "i"(C + 1), "i"(C + 2), "i"(C + 3));
}
#define DC(y, p, w, c) "v_dot2c_f32_bf16_dpp %" #y ", %" #p ", %" #w " row_newbcast:" #c " row_mask:0xf bank_mask:0xf\n\t"
#define W24(w) "v"(w[0][0]), "v"(w[0][1]), "v"(w[1][0]), "v"(w[1][1]), "v"(w[2][0]), "v"(w[2][1]), "v"(w[3][0]), "v"(w[3][1]), \
               "v"(w[4][0]), "v"(w[4][1]), "v"(w[5][0]), "v"(w[5][1]), "v"(w[6][0]), "v"(w[6][1]), "v"(w[7][0]), "v"(w[7][1]), \
               "v"(w[8][0]), "v"(w[8][1]), "v"(w[9][0]), "v"(w[9][1]), "v"(w[10][0]), "v"(w[10][1]), "v"(w[11][0]), "v"(w[11][1])
DI void dots_up(float& a0, float& a1, float& a2, unsigned hp, const u32x2 (&w)[12]) {
  asm("s_nop 1\n\t"
      DC(0, 3, 4, 0) DC(1, 3, 12, 0) DC(2, 3, 20, 0) DC(0, 3, 5, 2) DC(1, 3, 13, 2) DC(2, 3, 21, 2)
      DC(0, 3, 6, 4) DC(1, 3, 14, 4) DC(2, 3, 22, 4) DC(0, 3, 7, 6) DC(1, 3, 15, 6) DC(2, 3, 23, 6)
      DC(0, 3, 8, 8) DC(1, 3, 16, 8) DC(2, 3, 24, 8) DC(0, 3, 9, 10) DC(1, 3, 17, 10) DC(2, 3, 25, 10)
      DC(0, 3, 10, 12) DC(1, 3, 18, 12) DC(2, 3, 26, 12) DC(0, 3, 11, 14) DC(1, 3, 19, 14) DC(2, 3, 27, 14)
      "s_nop 2"
      : "+v"(a0), "+v"(a1), "+v"(a2) : "v"(hp), W24(w));
}
DI void dots_dn(float (&y)[3], const unsigned (&gp)[4], const u32x2 (&w)[12]) {
  asm("s_nop 1\n\t"
      DC(0, 3, 6, 0) DC(1, 4, 14, 0) DC(2, 5, 22, 0) DC(0, 3, 7, 2) DC(1, 4, 15, 2) DC(2, 5, 23, 2)
      DC(0, 3, 8, 4) DC(1, 4, 16, 4) DC(2, 5, 24, 4) DC(0, 3, 9, 6) DC(1, 4, 17, 6) DC(2, 5, 25, 6)
      DC(0, 3, 10, 8) DC(1, 4, 18, 8) DC(2, 5, 26, 8) DC(0, 3, 11, 10) DC(1, 4, 19, 10) DC(2, 5, 27, 10)
      DC(0, 3, 12, 12) DC(1, 4, 20, 12) DC(2, 5, 28, 12) DC(0, 3, 13, 14) DC(1, 4, 21, 14) DC(2, 5, 29, 14)
      "s_nop 2"
      : "+v"(y[0]), "+v"(y[1]), "+v"(y[2]) : "v"(gp[0]), "v"(gp[1]), "v"(gp[2]), W24(w));
}
#define PIN(x) asm volatile("" : "+v"(x))
#define SB() __builtin_amdgcn_sched_barrier(0)
#define PACEN(n) do { asm volatile("s_waitcnt vmcnt(%0)" :: "i"(n) : "memory"); SB(); } while (0)
#define PACE() PACEN(3)
#define BARRIER() do { asm volatile("s_waitcnt lgkmcnt(0)\n\ts_barrier" ::: "memory"); SB(); } while (0)
DI rsrc_t mk_rsrc(const void* p, int bytes) { return __builtin_amdgcn_make_buffer_rsrc((void*)p, 0, bytes, 0x00020000); }

template <int G, int N> DI void burst(int grp, rsrc_t r, int voff, int stride, int lds, int j0) {
  int keep, j, cnt, t;
  asm volatile(
      "s_cmp_lg_u32 %4, %10\n\t"
      "s_cbranch_scc1 BE%=\n\t"
      "s_mov_b32 %0, m0\n\t"
      "s_mov_b32 %1, %9\n\t"
      "s_mov_b32 %2, %11\n"
      "BL%=:\n\t"
      "s_lshl_b32 %3, %1, 10\n\t"
      "s_add_u32 m0, %3, %8\n\t"
      "s_mul_i32 %3, %1, %7\n\t"
      "s_add_u32 %1, %1, 1\n\t"
      "s_waitcnt vmcnt(3)\n\t"
      "buffer_load_dwordx4 %6, %5, %3 offen lds\n\t"
      "s_cmp_eq_u32 %1, %11\n\t"
      "s_cselect_b32 %1, 0, %1\n\t"
      "s_sub_u32 %2, %2, 1\n\t"
      "s_cmp_lg_u32 %2, 0\n\t"
      "s_cbranch_scc1 BL%=\n\t"
      "s_mov_b32 m0, %0\n"
      "BE%=:"
      : "=&s"(keep), "=&s"(j), "=&s"(cnt), "=&s"(t)
      : "s"(grp), "s"(r), "v"(voff), "s"(stride), "s"(lds), "s"(j0), "i"(G), "i"(N)
      : "memory", "scc");
  SB();
}
DI u32x2 tr_read(LAS unsigned char* p) {
  return __builtin_bit_cast(u32x2, __builtin_amdgcn_ds_read_tr16_b64_v4i16((LAS s16x4*)p));
}
DI f32x2 fold_rows8(const float (&acc)[8]) {
  const float t0 = tsum32(acc[0], acc[4]), t1 = tsum32(acc[1], acc[5]), t2 = tsum32(acc[2], acc[6]), t3 = tsum32(acc[3], acc[7]);
  return f32x2{tsum16(t0, t2), tsum16(t1, t3)};
}
DI float fold_rows4(float a0, float a1, float a2, float a3) { return tsum16(tsum32(a0, a2), tsum32(a1, a3)); }
DI unsigned max4(u32x4 v) { return max(max(v[0], v[1]), max(v[2], v[3])); }

template <int I> DI void pv_step(float (&acc)[8], unsigned plo, unsigned phi, u32x4 v) {
  const unsigned lo = dppu<BCAST + I>(plo), hi = dppu<BCAST + I>(phi);
#pragma unroll
  for (int k = 0; k < 4; ++k) { acc[2 * k] = dot2(lo, v[k], acc[2 * k]); acc[2 * k + 1] = dot2(hi, v[k], acc[2 * k + 1]); }
}
DI void pin8(float (&a)[8]) {
  asm volatile("" : "+v"(a[0]), "+v"(a[1]), "+v"(a[2]), "+v"(a[3]), "+v"(a[4]), "+v"(a[5]), "+v"(a[6]), "+v"(a[7]));
}
template <int... Is> struct iseq {};
template <int N, int... Is> struct mkseq : mkseq<N - 1, N - 1, Is...> {};
template <int... Is> struct mkseq<0, Is...> { typedef iseq<Is...> type; };
template <class Fn, int... Is> DI void sfor_(Fn&& f, iseq<Is...>) { (f(std::integral_constant<int, Is>{}), ...); }
template <int N, class Fn> DI void sfor(Fn&& f) { sfor_(f, typename mkseq<N>::type{}); }

DI float transpose_reduce16(float (&x)[16], int c) {
#define TR_STEP(HALF, CTRL, BIT) _Pragma("unroll") for (int h_ = 0; h_ < (HALF); ++h_) { \
    const float lo_ = x[h_], hi_ = x[h_ + (HALF)]; const float t1_ = lo_ + dppf<CTRL>(lo_), t2_ = hi_ + dppf<CTRL>(hi_); \
    x[h_] = (c & (BIT)) ? t2_ : t1_; }
  TR_STEP(8, ROR8, 8) TR_STEP(4, HMIR, 4) TR_STEP(2, XOR2, 2) TR_STEP(1, XOR1, 1)
#undef TR_STEP
  return x[0];
}

#define STAMP(i) do {} while (0)

__global__ void __launch_bounds__(512, 2)
fused_block(const unsigned short* __restrict__ x, const unsigned char* __restrict__ kc, const unsigned char* __restrict__ vc,
            const unsigned char* __restrict__ wq, const unsigned char* __restrict__ wo, const unsigned char* __restrict__ w1,
            const unsigned char* __restrict__ b1, const unsigned char* __restrict__ w2, const unsigned short* __restrict__ b2,
            unsigned short* __restrict__ out, unsigned char* ws) {
  __shared__ __attribute__((aligned(16))) unsigned char smem[L_BYTES];
  LAS unsigned char* L = (LAS unsigned char*)smem;
  STAMP(0);
  const int blk = blockIdx.x, b = blk >> 5, h = blk & 31;
  const int tid = threadIdx.x, lane = tid & 63, r = lane >> 4, c = lane & 15;
  const int w = __builtin_amdgcn_readfirstlane(tid >> 6);
  const int grp = h >> 3;
  const size_t kvoff = ((size_t)blk * SK + (SK - WIN)) * (DH * 2);
  const rsrc_t rk = mk_rsrc(kc + kvoff, WIN * DH * 2), rv = mk_rsrc(vc + kvoff, WIN * DH * 2);
  const int vkv = tid * 16;
  const int ldsw = (int)(unsigned)(size_t)(L + L_W) + w * 16384;

  u32x4 K[32];
  sfor<4>([&](auto I) { K[I] = __builtin_amdgcn_raw_buffer_load_b128(rk, vkv, I * 8192, AUX_NT); });
  SB();
  const u32x4 xrow = *(const u32x4*)(x + b * D + lane * 8);
  const int kcol = 64 * w + 4 * c + r;
  const float xe = asf((unsigned)x[b * D + kcol] << 16);
  const rsrc_t rws = mk_rsrc(ws, WS_BYTES);
  const int slice = (h & 7) * 4 + grp, col0 = slice * FS;
  const bool ctl = (w == 7);
  const int v_hb = (tid == 448) ? WS_HB + (b * 32 + h) * 4 : OOB, v_go = (tid == 448) ? WS_GO + b * 256 : OOB;
  const unsigned hb0 = __builtin_amdgcn_raw_buffer_load_b32(rws, v_hb, 0, AUX_SC1);
  const unsigned go0 = __builtin_amdgcn_raw_buffer_load_b32(rws, v_go, 0, AUX_SC1);
  const int o_snap = (ctl && h == 0) ? WS_HB + (b * 32 + (lane & 31)) * 4 : OOB;
  const unsigned snap = __builtin_amdgcn_raw_buffer_load_b32(rws, o_snap, 0, AUX_SC1);
  {
    const u32x4 s4 = {SENT, SENT, SENT, SENT};
    const int o1 = ctl ? WS_P1 + blk * 2048 + lane * 16 : OOB;
    const int o2 = ctl ? WS_P2 + ((b * 32 + (lane >> 2)) * 32 + slice) * 64 + (lane & 3) * 16 : OOB;
    __builtin_amdgcn_raw_buffer_store_b128(s4, rws, o1, 0, AUX_SC1);
    __builtin_amdgcn_raw_buffer_store_b128(s4, rws, o1, 1024, AUX_SC1);
    __builtin_amdgcn_raw_buffer_store_b128(s4, rws, o2, 0, AUX_SC1);
    __builtin_amdgcn_raw_buffer_store_b128(s4, rws, o2, 16 * 32 * 64, AUX_SC1);
  }
  float b1v = asf((unsigned)__builtin_amdgcn_raw_buffer_load_b16(mk_rsrc(b1, F * 2), lane < FS ? (col0 + lane) * 2 : OOB, 0, 0) << 16);
  const int ocol = 16 * h + 4 * (c & 3) + r;
  float b2v = asf((unsigned)b2[ocol] << 16);
  SB();

  const rsrc_t rq = mk_rsrc(wq + h * (DH * 2), D * NH * DH * 2 - h * (DH * 2));
  const int vq = (64 * w + r) * (NH * DH * 2) + c * 16;
  sfor<13>([&](auto I) {
    constexpr int i = I + 4;
    if constexpr (I >= 3 && (I % 3) == 0) burst<I / 3 - 1, 16>(grp, rq, vq, 4 * NH * DH * 2, ldsw, 2 * b);
    PACEN(I < 3 ? 14 : 3);
    K[i] = __builtin_amdgcn_raw_buffer_load_b128(rk, vkv, i * 8192, AUX_NT);
    SB();
  });
  unsigned xlo, xhi;
  {
    float xf[8];
#pragma unroll
    for (int k = 0; k < 4; ++k) { xf[2 * k] = asf(xrow[k] << 16); xf[2 * k + 1] = asf(xrow[k] & 0xffff0000u); }
    const float mean = wave_sum(((xf[0] + xf[1]) + (xf[2] + xf[3])) + ((xf[4] + xf[5]) + (xf[6] + xf[7]))) * (1.f / D);
    float vs = 0.f;
#pragma unroll
    for (int k = 0; k < 8; ++k) { const float d = xf[k] - mean; vs = fmaf(d, d, vs); }
    const float var = wave_sum(vs) * (1.f / D);
    const float den = rbf(sqrtf(rbf(rbf(var) + 1e-5f)));
    const float xn = rbf(rbf(xe - rbf(mean)) / den);
    xhi = fbits(xn); xlo = xhi >> 16;
  }
  SB();
  asm volatile("s_cmp_lg_u32 %0, 7\n\ts_cbranch_scc1 CW%=\n\ts_waitcnt vmcnt(0)\nCW%=:" :: "s"(w) : "memory", "scc");
  __builtin_amdgcn_raw_buffer_store_b32(hb0 + 1u, rws, v_hb, 0, AUX_SC1);
  SB();
  sfor<4>([&](auto I) {
    constexpr int i = I + 17;
    PACE();
    K[i] = __builtin_amdgcn_raw_buffer_load_b128(rk, vkv, i * 8192, AUX_NT);
    SB();
  });
  STAMP(1);
  unsigned qp[4];
  {
    float acc[8] = {0.f, 0.f, 0.f, 0.f, 0.f, 0.f, 0.f, 0.f};
    sfor<16>([&](auto I) {
      const u32x4 wv = *(const LAS u32x4*)(L + L_W + w * 16384 + I * 1024 + lane * 16);
      pv_step<I>(acc, xlo, xhi, wv);
      if constexpr (I == 7 || I == 15) {
        constexpr int i = 21 + (I >> 3);
        SB(); PACE();
        K[i] = __builtin_amdgcn_raw_buffer_load_b128(rk, vkv, i * 8192, AUX_NT);
        SB();
      }
    });
    *(LAS f32x2*)(L + L_RED + tid * 8) = fold_rows8(acc);
    BARRIER();
    f32x2 s = *(const LAS f32x2*)(L + L_RED + lane * 8);
#pragma unroll
    for (int i = 1; i < 8; ++i) s += *(const LAS f32x2*)(L + L_RED + i * 512 + lane * 8);
    rows_bcast(cvtpk(s[0], s[1]), qp);
  }
  SB();
  STAMP(2);
  const rsrc_t r1 = mk_rsrc(w1, D * F * 2);
  const int v1 = ((lane & 7) < 6 && col0 + (lane & 7) * 8 < F) ? (64 * w + (lane >> 3)) * (F * 2) + col0 * 2 + (lane & 7) * 16 : OOB;
  const int c2 = min(col0, F);
  const rsrc_t r2 = mk_rsrc(w2 + (size_t)c2 * (D * 2), (F - c2) * (D * 2));
  const int vo = (lane >> 3) * (D * 2) + w * 128 + (lane & 7) * 16;
  u32x4 V[32];
  float sc[2], xs[16];
  auto qk = [&](const u32x4& kv) { return dot2(kv[3], qp[3], dot2(kv[2], qp[2], dot2(kv[1], qp[1], dot2(kv[0], qp[0], 0.f)))); };
  sfor<13>([&](auto I) {
    constexpr int i = I + 23;
    if constexpr ((I & 3) == 0) {
      burst<I / 4, 8>(grp, r1, v1, 8 * F * 2, ldsw, b);
      burst<I / 4, 6>(grp, r2, vo, 8 * D * 2, ldsw + 8192, b < 6 ? b : b - 6);
    }
    PACE();
    if constexpr (i < 32) K[i] = __builtin_amdgcn_raw_buffer_load_b128(rk, vkv, i * 8192, AUX_NT);
    else V[i - 32] = __builtin_amdgcn_raw_buffer_load_b128(rv, vkv, (i - 32) * 8192, AUX_NT);
    SB();
    if constexpr (I < 4) { sfor<4>([&](auto J) { xs[4 * I + J] = qk(K[4 * I + J]); }); }
    if constexpr (I == 4) sc[0] = transpose_reduce16(xs, c);
    if constexpr (I >= 5) { xs[2 * (I - 5)] = qk(K[16 + 2 * (I - 5)]); xs[2 * (I - 5) + 1] = qk(K[17 + 2 * (I - 5)]); }
    SB();
  });
  sc[1] = transpose_reduce16(xs, c);
  SB(); PACE();
  V[4] = __builtin_amdgcn_raw_buffer_load_b128(rv, vkv, 4 * 8192, AUX_NT);
  const unsigned snap2 = __builtin_amdgcn_raw_buffer_load_b32(rws, o_snap, 0, AUX_SC1);
  SB();
  unsigned plo[2], phi[2];
  {
    const float s0 = rbf(rbf(sc[0]) * 0.08838834764831845f), s1 = rbf(rbf(sc[1]) * 0.08838834764831845f);
    const float mw = wave_max(fmaxf(s0, s1));
    const float e0 = __builtin_amdgcn_exp2f((s0 - mw) * 1.4426950408889634f), e1 = __builtin_amdgcn_exp2f((s1 - mw) * 1.4426950408889634f);
    const float lw = wave_sum(e0 + e1);
    if (lane == 0) *(LAS f32x2*)(L + L_ML + w * 8) = f32x2{mw, lw};
    SB(); PACE();
    V[5] = __builtin_amdgcn_raw_buffer_load_b128(rv, vkv, 5 * 8192, AUX_NT);
    SB();
    BARRIER();
    const f32x2 ml = *(const LAS f32x2*)(L + L_ML + (lane & 7) * 8);
    float M = ml[0];
    M = fmaxf(M, dppf<XOR1>(M)); M = fmaxf(M, dppf<XOR2>(M)); M = fmaxf(M, dppf<HMIR>(M));
    float Ls = ml[1] * __builtin_amdgcn_exp2f((ml[0] - M) * 1.4426950408889634f);
    Ls += dppf<XOR1>(Ls); Ls += dppf<XOR2>(Ls); Ls += dppf<HMIR>(Ls);
    const float scale = __builtin_amdgcn_exp2f((mw - M) * 1.4426950408889634f) / Ls;
    const unsigned pp = cvtpk(e0 * scale, e1 * scale);
    plo[0] = pp & 0xffffu; phi[0] = pp << 16; plo[1] = pp >> 16; phi[1] = pp & 0xffff0000u;
  }
  SB();
  STAMP(3);
  const rsrc_t ro = mk_rsrc(wo + (size_t)h * (DH * D * 2), DH * D * 2);
  float acc[8] = {0.f, 0.f, 0.f, 0.f, 0.f, 0.f, 0.f, 0.f};
  u32x2 W1[12], W2[12], WO[32];
  LAS unsigned char* tr1 = L + L_W + w * 16384 + (4 * ((lane >> 2) & 3) + r) * 128 + (lane & 3) * 8;
  LAS unsigned char* tro = L + L_W + w * 16384 + ((lane >> 2) & 3) * 128 + r * 32 + (lane & 3) * 8;
  unsigned go1 = 0;
  u32x4 G[8];
  const int vg = WS_P1 + (b * 32 + r) * 2048 + w * 256 + c * 16;
  float c1 = 0.f;
  int vp1 = WS_P1 + blk * 2048 + tid * 4;
  int vp2 = WS_P2 + ((b * 32 + (tid >> 4)) * 32 + slice) * 64 + (tid & 15) * 4;
  int lh = kcol * 4;
  sfor<30>([&](auto I) {
    constexpr int i = I + 6;
    if constexpr (i >= 10 && i <= 22 && ((i - 10) & 3) == 0) burst<(i - 10) / 4, 16>(grp, ro, vo, 8 * D * 2, ldsw, 2 * b);
    PACE();
    if constexpr (i < 32) V[i] = __builtin_amdgcn_raw_buffer_load_b128(rv, vkv, i * 8192, AUX_NT);
    else G[i - 32] = __builtin_amdgcn_raw_buffer_load_b128(rws, vg, (i - 32) * 8192, AUX_SC1);
    if constexpr (i == 24) go1 = __builtin_amdgcn_raw_buffer_load_b32(rws, v_go, 0, AUX_SC1);
    SB();
    if constexpr (i == 6) { pv_step<0>(acc, plo[0], phi[0], V[0]); pv_step<1>(acc, plo[0], phi[0], V[1]); }
    pv_step<(i - 4) & 15>(acc, plo[(i - 4) >> 4], phi[(i - 4) >> 4], V[i - 4]);
    pin8(acc);
    if constexpr (i == 8) {
      sfor<12>([&](auto J) { W1[J] = tr_read(tr1 + (J & 3) * 16 * 128 + (J >> 2) * 32); });
      sfor<12>([&](auto J) { W2[J] = tr_read(tro + 8192 + J * 512); });
      const bool go_now = __builtin_amdgcn_ballot_w64(snap2 == snap) == 0;
      __builtin_amdgcn_raw_buffer_store_b32(go0 + 1u, rws, go_now ? v_go : OOB, 0, AUX_SC1);
    }
    if constexpr (i == 9) {
      float a[3] = {0.f, 0.f, 0.f};
      sfor<12>([&](auto J) { a[J >> 2] = dot2(W1[J][1], 0x3f803f80u, dot2(W1[J][0], 0x3f803f80u, a[J >> 2])); });
      *(LAS float*)(L + L_EX + tid * 4) = fold_rows4(a[0], a[1], a[2], 0.f);
      asm volatile("s_waitcnt lgkmcnt(0)" ::: "memory");
    }
    if constexpr (i >= 27 && i <= 30) sfor<8>([&](auto J) { WO[8 * (i - 27) + J] = tr_read(tro + (8 * (i - 27) + J) * 512); });
    if constexpr (i == 29) {
      {
        unsigned beat = hb0 + 1u, tmp, sg, sg0, spins;
        unsigned long long seen = __builtin_amdgcn_ballot_w64(snap2 != snap);
        asm volatile(
            "s_cmp_lg_u32 %6, 7\n\t"
            "s_cbranch_scc1 HE%=\n\t"
            "v_readfirstlane_b32 %3, %7\n\t"
            "v_readfirstlane_b32 %2, %8\n\t"
            "s_mov_b32 %4, 0\n\t"
            "s_nop 3\n"
            "HL%=:\n\t"
            "s_cmp_lg_u32 %2, %3\n\t"
            "s_cbranch_scc1 HE%=\n\t"
            "s_add_u32 %4, %4, 1\n\t"
            "s_cmp_gt_u32 %4, %15\n\t"
            "s_cbranch_scc1 HE%=\n\t"
            "v_add_u32 %0, 1, %0\n\t"
            "buffer_store_dword %0, %10, %9, 0 offen sc1\n\t"
            "buffer_load_dword %1, %11, %9, 0 offen sc1\n\t"
            "s_waitcnt vmcnt(0)\n\t"
            "v_cmp_ne_u32 vcc, %1, %12\n\t"
            "s_nop 3\n\t"
            "s_or_b64 %5, %5, vcc\n\t"
            "s_cmp_eq_u64 %5, -1\n\t"
            "s_cbranch_scc0 HN%=\n\t"
            "buffer_store_dword %14, %13, %9, 0 offen sc1\n"
            "HN%=:\n\t"
            "buffer_load_dword %1, %13, %9, 0 offen sc1\n\t"
            "s_waitcnt vmcnt(0)\n\t"
            "v_readfirstlane_b32 %2, %1\n\t"
            "s_nop 3\n\t"
            "s_branch HL%=\n"
            "HE%=:"
            : "+v"(beat), "=&v"(tmp), "=&s"(sg), "=&s"(sg0), "=&s"(spins), "+s"(seen)
            : "s"(w), "v"(go0), "v"(go1), "s"(rws), "v"(v_hb), "v"(o_snap), "v"(snap), "v"(v_go), "v"(go0 + 1u), "s"(SPIN_CAP)
            : "memory", "scc", "vcc");
      }
      BARRIER();
#pragma unroll
      for (int k = 0; k < 8; ++k) c1 += *(const LAS float*)(L + L_EX + k * 256 + lane * 4);
      PIN(c1); PIN(vp1); PIN(vp2); PIN(b1v); PIN(b2v); PIN(lh);
    }
    SB();
  });
  STAMP(4);
  {
    *(LAS f32x2*)(L + L_RED2 + tid * 8) = fold_rows8(acc);
    BARRIER();
    f32x2 s = *(const LAS f32x2*)(L + L_RED2 + lane * 8);
#pragma unroll
    for (int i = 1; i < 8; ++i) s += *(const LAS f32x2*)(L + L_RED2 + i * 512 + lane * 8);
    unsigned op[4];
    rows_bcast(cvtpk(s[0], s[1]), op);
    SB();
    sfor<4>([&](auto I) { G[4 + I] = __builtin_amdgcn_raw_buffer_load_b128(rws, vg, (4 + I) * 8192, AUX_SC1); });
    float y[4] = {0.f, 0.f, 0.f, 0.f};
    asm volatile("s_nop 1" : "+v"(op[0]), "+v"(op[1]), "+v"(op[2]), "+v"(op[3]), "+v"(y[0]), "+v"(y[1]), "+v"(y[2]), "+v"(y[3]));
    dots_o<0>(y, op, WO); dots_o<4>(y, op, WO + 8); dots_o<8>(y, op, WO + 16); dots_o<12>(y, op, WO + 24);
    asm volatile("s_nop 2" : "+v"(y[0]), "+v"(y[1]), "+v"(y[2]), "+v"(y[3]));
    __builtin_amdgcn_raw_buffer_store_b32(min(fbits((y[0] + y[1]) + (y[2] + y[3])), SENT - 1u), rws, vp1, 0, AUX_SC1);
  }
  SB();
  STAMP(5);
  for (unsigned spins = 0; spins < SPIN_CAP; ++spins) {
    bool any = false;
    asm volatile("" ::: "memory");
    sfor<8>([&](auto I) {
      if (__builtin_amdgcn_ballot_w64(max4(G[I]) == SENT) != 0) {
        G[I] = __builtin_amdgcn_raw_buffer_load_b128(rws, vg, I * 8192, AUX_SC1);
        any = true;
      }
    });
    if (!any) break;
  }
  SB();
  STAMP(6);
  {
    f32x4 s = __builtin_bit_cast(f32x4, G[0]);
#pragma unroll
    for (int i = 1; i < 8; ++i) s += __builtin_bit_cast(f32x4, G[i]);
    const float hv = rbf(xe + rbf(fold_rows4(s[0], s[1], s[2], s[3])));
    *(LAS float*)(L + L_H + lh) = hv;
    unsigned hp = (fbits(hv) >> 16) | dppu<XOR1>(fbits(hv));
    const float h2 = hv * hv;
    const float t1 = hv + dppf<XOR1>(hv), t2 = h2 + dppf<XOR1>(h2);
    float stt = (c & 1) ? t2 : t1;
    stt += dppf<XOR2>(stt); stt += dppf<ROR4>(stt); stt += dppf<ROR8>(stt);
    float a0 = 0.f, a1 = 0.f, a2 = 0.f;
    dots_up(a0, a1, a2, hp, W1);
    *(LAS float*)(L + L_EX2 + tid * 4) = fold_rows4(a0, a1, a2, stt);
    BARRIER();
    float tot = *(const LAS float*)(L + L_EX2 + lane * 4);
#pragma unroll
    for (int k = 1; k < 8; ++k) tot += *(const LAS float*)(L + L_EX2 + k * 256 + lane * 4);
    const float mean = asf(__builtin_amdgcn_readlane(fbits(tot), 48)) * (1.f / D);
    const float var = fmaxf(fmaf(-mean, mean, asf(__builtin_amdgcn_readlane(fbits(tot), 49)) * (1.f / D)), 0.f);
    const float den = rbf(__builtin_amdgcn_sqrtf(rbf(rbf(var) + 1e-5f)));
    const float u = rbf(rbf(fmaf(-rbf(mean), c1, tot) * __builtin_amdgcn_rcpf(den)) + b1v);
    const float z = u * fmaf(u * u, 0.044715f * 0.7978845608028654f, 0.7978845608028654f);
    const float g = u * __builtin_amdgcn_rcpf(1.f + __builtin_amdgcn_exp2f(z * (-2.f * 1.4426950408889634f)));
    unsigned gp[4];
    rows_bcast(cvtpk(g, dppf<XOR1>(g)), gp);
    float y[3] = {0.f, 0.f, 0.f};
    dots_dn(y, gp, W2);
    __builtin_amdgcn_raw_buffer_store_b32(min(fbits((y[0] + y[1]) + y[2]), SENT - 1u), rws, vp2, 0, AUX_SC1);
  }
  SB();
  STAMP(7);
  if (w == 0) {
    const float hres = *(const LAS float*)(L + L_H + ocol * 4);
    const int vf = WS_P2 + blk * 2048 + lane * 16;
    u32x4 A, Bv;
    __builtin_amdgcn_s_sleep(10);
    for (unsigned spins = 0; spins < SPIN_CAP; ++spins) {
      asm volatile("" ::: "memory");
      A = __builtin_amdgcn_raw_buffer_load_b128(rws, vf, 0, AUX_SC1);
      Bv = __builtin_amdgcn_raw_buffer_load_b128(rws, vf, 1024, AUX_SC1);
      if (__builtin_amdgcn_ballot_w64(max(max4(A), max4(Bv)) == SENT) == 0) break;
    }
    STAMP(8);
    const f32x4 s = __builtin_bit_cast(f32x4, A) + __builtin_bit_cast(f32x4, Bv);
    float t = fold_rows4(s[0], s[1], s[2], s[3]);
    t += dppf<ROR4>(t); t += dppf<ROR8>(t);
    const float res = hres + rbf(rbf(t) + b2v);
    if (c < 4) out[b * D + ocol] = __builtin_bit_cast(unsigned short, (__bf16)res);
  }
}

extern "C" void kernel_launch(void* const* d_in, const int* in_sizes, int n_in, void* d_out, int out_size, void* d_ws,
                              size_t ws_size, hipStream_t stream) {
  fused_block<<<NB * NH, 512, 0, stream>>>((const unsigned short*)d_in[0], (const unsigned char*)d_in[1], (const unsigned char*)d_in[2],
                                           (const unsigned char*)d_in[3], (const unsigned char*)d_in[4], (const unsigned char*)d_in[6],
                                           (const unsigned char*)d_in[7], (const unsigned char*)d_in[8], (const unsigned short*)d_in[9],
                                           (unsigned short*)d_out, (unsigned char*)d_ws);
}
```

```cpp
#define XK 0
#include <hip/hip_runtime.h>
#include <hip/hip_bf16.h>
#include <stdint.h>

#define STAMPS 12

typedef unsigned u32x4 __attribute__((ext_vector_type(4)));
typedef unsigned u32x2 __attribute__((ext_vector_type(2)));
typedef float f32x2 __attribute__((ext_vector_type(2)));
typedef float f32x4 __attribute__((ext_vector_type(4)));
typedef __bf16 bf16x2 __attribute__((ext_vector_type(2)));
typedef short s16x4 __attribute__((ext_vector_type(4)));
typedef __amdgpu_buffer_rsrc_t rsrc_t;
#define LAS __attribute__((address_space(3)))
#define DI __device__ __forceinline__

constexpr int NB = 8, D = 512, NH = 32, SK = 4096, DH = 128, WIN = 1024, F = 1376;
constexpr int FS = 48;
constexpr int AUX_NT = 2, AUX_SC1 = 16;
constexpr int OOB = 0x7ffffff0;
constexpr unsigned SENT = 0xffffffffu;
constexpr unsigned SPIN_CAP = 1u << 17;
constexpr int WS_P1 = 0;
constexpr int WS_P2 = 512 * 1024;
constexpr int WS_HB = 1024 * 1024;
constexpr int WS_GO = WS_HB + 4096;
constexpr int WS_BYTES = WS_GO + 4096;
constexpr int WS_STAMP = 2 * 1024 * 1024;
constexpr int L_RED = 0;
constexpr int L_RED2 = L_RED + 4096;
constexpr int L_EX = L_RED2 + 4096;
constexpr int L_EX2 = L_EX + 2048;
constexpr int L_H = L_EX2 + 2048;
constexpr int L_ML = L_H + 2048;
constexpr int L_W = 16384;
constexpr int L_BYTES = L_W + 128 * 1024;

DI float dot2(unsigned a, unsigned b, float acc) {
  return __builtin_amdgcn_fdot2_f32_bf16(__builtin_bit_cast(bf16x2, a), __builtin_bit_cast(bf16x2, b), acc, false);
}
DI unsigned fbits(float f) { return __builtin_bit_cast(unsigned, f); }
DI float asf(unsigned u) { return __builtin_bit_cast(float, u); }
DI unsigned cvtpk(float lo, float hi) {
  bf16x2 r = {(__bf16)lo, (__bf16)hi};
  return __builtin_bit_cast(unsigned, r);
}
DI float rbf(float f) { return asf((unsigned)__builtin_bit_cast(unsigned short, (__bf16)f) << 16); }
template <int CTRL> DI unsigned dppu(unsigned x) { return __builtin_amdgcn_mov_dpp(x, CTRL, 0xf, 0xf, true); }
template <int CTRL> DI float dppf(float x) { return asf(dppu<CTRL>(fbits(x))); }
constexpr int XOR1 = 0xB1, XOR2 = 0x4E, HMIR = 0x141, ROR4 = 0x124, ROR8 = 0x128, BCAST = 0x150;
DI float tsum32(float a, float b) {
  auto s = __builtin_amdgcn_permlane32_swap(fbits(a), fbits(b), false, false);
  return asf(s[0]) + asf(s[1]);
}
DI float tsum16(float a, float b) {
  auto s = __builtin_amdgcn_permlane16_swap(fbits(a), fbits(b), false, false);
  return asf(s[0]) + asf(s[1]);
}
DI float row_sum(float x) { x += dppf<XOR1>(x); x += dppf<XOR2>(x); x += dppf<ROR4>(x); x += dppf<ROR8>(x); return x; }
DI float row_max(float x) { x = fmaxf(x, dppf<XOR1>(x)); x = fmaxf(x, dppf<XOR2>(x)); x = fmaxf(x, dppf<ROR4>(x)); x = fmaxf(x, dppf<ROR8>(x)); return x; }
DI float wave_sum(float x) { x = row_sum(x); x = tsum16(x, x); return tsum32(x, x); }
DI float wave_max(float x) {
  x = row_max(x);
  auto s = __builtin_amdgcn_permlane16_swap(fbits(x), fbits(x), false, false);
  x = fmaxf(asf(s[0]), asf(s[1]));
  auto t = __builtin_amdgcn_permlane32_swap(fbits(x), fbits(x), false, false);
  return fmaxf(asf(t[0]), asf(t[1]));
}
DI void rows_bcast(unsigned x, unsigned (&o)[4]) {
  auto s = __builtin_amdgcn_permlane16_swap(x, x, false, false);
  auto a = __builtin_amdgcn_permlane32_swap(s[0], s[0], false, false);
  auto b = __builtin_amdgcn_permlane32_swap(s[1], s[1], false, false);
  o[0] = a[0]; o[2] = a[1]; o[1] = b[0]; o[3] = b[1];
}
#define DB(y, p, w, c) "v_dot2c_f32_bf16_dpp %" #y ", %" #p ", %" #w " row_newbcast:%" #c " row_mask:0xf bank_mask:0xf\n\t"
template <int C> DI void dots_o(float (&y)[4], const unsigned (&op)[4], const u32x2* w) {
  asm(DB(0, 4, 8, 24) DB(1, 5, 9, 24) DB(2, 6, 10, 24) DB(3, 7, 11, 24) DB(0, 4, 12, 25) DB(1, 5, 13, 25) DB(2, 6, 14, 25) DB(3, 7, 15, 25)
      DB(0, 4, 16, 26) DB(1, 5, 17, 26) DB(2, 6, 18, 26) DB(3, 7, 19, 26) DB(0, 4, 20, 27) DB(1, 5, 21, 27) DB(2, 6, 22, 27) DB(3, 7, 23, 27)
      : "+v"(y[0]), "+v"(y[1]), "+v"(y[2]), "+v"(y[3])
      : "v"(op[0]), "v"(op[1]), "v"(op[2]), "v"(op[3]), "v"(w[0][0]), "v"(w[0][1]), "v"(w[1][0]), "v"(w[1][1]), "v"(w[2][0]), "v"(w[2][1]),
        "v"(w[3][0]), "v"(w[3][1]), "v"(w[4][0]), "v"(w[4][1]), "v"(w[5][0]), "v"(w[5][1]), "v"(w[6][0]), "v"(w[6][1]), "v"(w[7][0]), "v"(w[7][1]),
        "i"(C), "i"(C + 1), "i"(C + 2), "i"(C + 3));
}
#define DC(y, p, w, c) "v_dot2c_f32_bf16_dpp %" #y ", %" #p ", %" #w " row_newbcast:" #c " row_mask:0xf bank_mask:0xf\n\t"
#define W24(w) "v"(w[0][0]), "v"(w[0][1]), "v"(w[1][0]), "v"(w[1][1]), "v"(w[2][0]), "v"(w[2][1]), "v"(w[3][0]), "v"(w[3][1]), \
               "v"(w[4][0]), "v"(w[4][1]), "v"(w[5][0]), "v"(w[5][1]), "v"(w[6][0]), "v"(w[6][1]), "v"(w[7][0]), "v"(w[7][1]), \
               "v"(w[8][0]), "v"(w[8][1]), "v"(w[9][0]), "v"(w[9][1]), "v"(w[10][0]), "v"(w[10][1]), "v"(w[11][0]), "v"(w[11][1])
DI void dots_up(float& a0, float& a1, float& a2, unsigned hp, const u32x2 (&w)[12]) {
  asm("s_nop 1\n\t"
      DC(0, 3, 4, 0) DC(1, 3, 12, 0) DC(2, 3, 20, 0) DC(0, 3, 5, 2) DC(1, 3, 13, 2) DC(2, 3, 21, 2)
      DC(0, 3, 6, 4) DC(1, 3, 14, 4) DC(2, 3, 22, 4) DC(0, 3, 7, 6) DC(1, 3, 15, 6) DC(2, 3, 23, 6)
      DC(0, 3, 8, 8) DC(1, 3, 16, 8) DC(2, 3, 24, 8) DC(0, 3, 9, 10) DC(1, 3, 17, 10) DC(2, 3, 25, 10)
      DC(0, 3, 10, 12) DC(1, 3, 18, 12) DC(2, 3, 26, 12) DC(0, 3, 11, 14) DC(1, 3, 19, 14) DC(2, 3, 27, 14)
      "s_nop 2"
      : "+v"(a0), "+v"(a1), "+v"(a2) : "v"(hp), W24(w));
}
DI void dots_dn(float (&y)[3], const unsigned (&gp)[4], const u32x2 (&w)[12]) {
  asm("s_nop 1\n\t"
      DC(0, 3, 6, 0) DC(1, 4, 14, 0) DC(2, 5, 22, 0) DC(0, 3, 7, 2) DC(1, 4, 15, 2) DC(2, 5, 23, 2)
      DC(0, 3, 8, 4) DC(1, 4, 16, 4) DC(2, 5, 24, 4) DC(0, 3, 9, 6) DC(1, 4, 17, 6) DC(2, 5, 25, 6)
      DC(0, 3, 10, 8) DC(1, 4, 18, 8) DC(2, 5, 26, 8) DC(0, 3, 11, 10) DC(1, 4, 19, 10) DC(2, 5, 27, 10)
      DC(0, 3, 12, 12) DC(1, 4, 20, 12) DC(2, 5, 28, 12) DC(0, 3, 13, 14) DC(1, 4, 21, 14) DC(2, 5, 29, 14)
      "s_nop 2"
      : "+v"(y[0]), "+v"(y[1]), "+v"(y[2]) : "v"(gp[0]), "v"(gp[1]), "v"(gp[2]), W24(w));
}
#define PIN(x) asm volatile("" : "+v"(x))
#define SB() __builtin_amdgcn_sched_barrier(0)
#define PACEN(n) do { asm volatile("s_waitcnt vmcnt(%0)" :: "i"(n) : "memory"); SB(); } while (0)
#define PACE() PACEN(3)
#define BARRIER() do { asm volatile("s_waitcnt lgkmcnt(0)\n\ts_barrier" ::: "memory"); SB(); } while (0)
DI rsrc_t mk_rsrc(const void* p, int bytes) { return __builtin_amdgcn_make_buffer_rsrc((void*)p, 0, bytes, 0x00020000); }

template <int G, int N> DI void burst(int grp, rsrc_t r, int voff, int stride, int lds, int j0) {
  int keep, j, cnt, t;
  asm volatile(
      "s_cmp_lg_u32 %4, %10\n\t"
      "s_cbranch_scc1 BE%=\n\t"
      "s_mov_b32 %0, m0\n\t"
      "s_mov_b32 %1, %9\n\t"
      "s_mov_b32 %2, %11\n"
      "BL%=:\n\t"
      "s_lshl_b32 %3, %1, 10\n\t"
      "s_add_u32 m0, %3, %8\n\t"
      "s_mul_i32 %3, %1, %7\n\t"
      "s_add_u32 %1, %1, 1\n\t"
      "s_waitcnt vmcnt(3)\n\t"
      "buffer_load_dwordx4 %6, %5, %3 offen lds\n\t"
      "s_cmp_eq_u32 %1, %11\n\t"
      "s_cselect_b32 %1, 0, %1\n\t"
      "s_sub_u32 %2, %2, 1\n\t"
      "s_cmp_lg_u32 %2, 0\n\t"
      "s_cbranch_scc1 BL%=\n\t"
      "s_mov_b32 m0, %0\n"
      "BE%=:"
      : "=&s"(keep), "=&s"(j), "=&s"(cnt), "=&s"(t)
      : "s"(grp), "s"(r), "v"(voff), "s"(stride), "s"(lds), "s"(j0), "i"(G), "i"(N)
      : "memory", "scc");
  SB();
}
DI u32x2 tr_read(LAS unsigned char* p) {
  return __builtin_bit_cast(u32x2, __builtin_amdgcn_ds_read_tr16_b64_v4i16((LAS s16x4*)p));
}
DI f32x2 fold_rows8(const float (&acc)[8]) {
  const float t0 = tsum32(acc[0], acc[4]), t1 = tsum32(acc[1], acc[5]), t2 = tsum32(acc[2], acc[6]), t3 = tsum32(acc[3], acc[7]);
  return f32x2{tsum16(t0, t2), tsum16(t1, t3)};
}
DI float fold_rows4(float a0, float a1, float a2, float a3) { return tsum16(tsum32(a0, a2), tsum32(a1, a3)); }
DI unsigned max4(u32x4 v) { return max(max(v[0], v[1]), max(v[2], v[3])); }

template <int I> DI void pv_step(float (&acc)[8], unsigned plo, unsigned phi, u32x4 v) {
  const unsigned lo = dppu<BCAST + I>(plo), hi = dppu<BCAST + I>(phi);
#pragma unroll
  for (int k = 0; k < 4; ++k) { acc[2 * k] = dot2(lo, v[k], acc[2 * k]); acc[2 * k + 1] = dot2(hi, v[k], acc[2 * k + 1]); }
}
DI void pin8(float (&a)[8]) {
  asm volatile("" : "+v"(a[0]), "+v"(a[1]), "+v"(a[2]), "+v"(a[3]), "+v"(a[4]), "+v"(a[5]), "+v"(a[6]), "+v"(a[7]));
}
template <int... Is> struct iseq {};
template <int N, int... Is> struct mkseq : mkseq<N - 1, N - 1, Is...> {};
template <int... Is> struct mkseq<0, Is...> { typedef iseq<Is...> type; };
template <class Fn, int... Is> DI void sfor_(Fn&& f, iseq<Is...>) { (f(std::integral_constant<int, Is>{}), ...); }
template <int N, class Fn> DI void sfor(Fn&& f) { sfor_(f, typename mkseq<N>::type{}); }

DI float transpose_reduce16(float (&x)[16], int c) {
#define TR_STEP(HALF, CTRL, BIT) _Pragma("unroll") for (int h_ = 0; h_ < (HALF); ++h_) { \
    const float lo_ = x[h_], hi_ = x[h_ + (HALF)]; const float t1_ = lo_ + dppf<CTRL>(lo_), t2_ = hi_ + dppf<CTRL>(hi_); \
    x[h_] = (c & (BIT)) ? t2_ : t1_; }
  TR_STEP(8, ROR8, 8) TR_STEP(4, HMIR, 4) TR_STEP(2, XOR2, 2) TR_STEP(1, XOR1, 1)
#undef TR_STEP
  return x[0];
}

#define STAMP(i) do { SB(); st[i] = __builtin_amdgcn_s_memrealtime(); SB(); } while (0)

__global__ void __launch_bounds__(512, 2)
fused_block(const unsigned short* __restrict__ x, const unsigned char* __restrict__ kc, const unsigned char* __restrict__ vc,
            const unsigned char* __restrict__ wq, const unsigned char* __restrict__ wo, const unsigned char* __restrict__ w1,
            const unsigned char* __restrict__ b1, const unsigned char* __restrict__ w2, const unsigned short* __restrict__ b2,
            unsigned short* __restrict__ out, unsigned char* ws) {
  __shared__ __attribute__((aligned(16))) unsigned char smem[L_BYTES];
  LAS unsigned char* L = (LAS unsigned char*)smem;
  unsigned long long st[12];
  STAMP(0);
  const int blk = blockIdx.x, b = blk >> 5, h = blk & 31;
  const int tid = threadIdx.x, lane = tid & 63, r = lane >> 4, c = lane & 15;
  const int w = __builtin_amdgcn_readfirstlane(tid >> 6);
  const int grp = h >> 3;
  const size_t kvoff = ((size_t)blk * SK + (SK - WIN)) * (DH * 2);
  const rsrc_t rk = mk_rsrc(kc + kvoff, WIN * DH * 2), rv = mk_rsrc(vc + kvoff, WIN * DH * 2);
  const int vkv = tid * 16;
  const int ldsw = (int)(unsigned)(size_t)(L + L_W) + w * 16384;

  u32x4 K[32];
  sfor<4>([&](auto I) { K[I] = __builtin_amdgcn_raw_buffer_load_b128(rk, vkv, I * 8192, AUX_NT); });
  SB();
  const u32x4 xrow = *(const u32x4*)(x + b * D + lane * 8);
  const int kcol = 64 * w + 4 * c + r;
  const float xe = asf((unsigned)x[b * D + kcol] << 16);
  const rsrc_t rws = mk_rsrc(ws, WS_BYTES);
  const int slice = (h & 7) * 4 + grp, col0 = slice * FS;
  const bool ctl = (w == 7);
  const int v_hb = (tid == 448) ? WS_HB + (b * 32 + h) * 4 : OOB, v_go = (tid == 448) ? WS_GO + b * 256 : OOB;
  const unsigned hb0 = __builtin_amdgcn_raw_buffer_load_b32(rws, v_hb, 0, AUX_SC1);
  const unsigned go0 = __builtin_amdgcn_raw_buffer_load_b32(rws, v_go, 0, AUX_SC1);
  const int o_snap = (ctl && h == 0) ? WS_HB + (b * 32 + (lane & 31)) * 4 : OOB;
  const unsigned snap = __builtin_amdgcn_raw_buffer_load_b32(rws, o_snap, 0, AUX_SC1);
  {
    const u32x4 s4 = {SENT, SENT, SENT, SENT};
    const int o1 = ctl ? WS_P1 + blk * 2048 + lane * 16 : OOB;
    const int o2 = ctl ? WS_P2 + ((b * 32 + (lane >> 2)) * 32 + slice) * 64 + (lane & 3) * 16 : OOB;
    __builtin_amdgcn_raw_buffer_store_b128(s4, rws, o1, 0, AUX_SC1);
    __builtin_amdgcn_raw_buffer_store_b128(s4, rws, o1, 1024, AUX_SC1);
    __builtin_amdgcn_raw_buffer_store_b128(s4, rws, o2, 0, AUX_SC1);
    __builtin_amdgcn_raw_buffer_store_b128(s4, rws, o2, 16 * 32 * 64, AUX_SC1);
  }
  float b1v = asf((unsigned)__builtin_amdgcn_raw_buffer_load_b16(mk_rsrc(b1, F * 2), lane < FS ? (col0 + lane) * 2 : OOB, 0, 0) << 16);
  const int ocol = 16 * h + 4 * (c & 3) + r;
  float b2v = asf((unsigned)b2[ocol] << 16);
  SB();

  const rsrc_t rq = mk_rsrc(wq + h * (DH * 2), D * NH * DH * 2 - h * (DH * 2));
  const int vq = (64 * w + r) * (NH * DH * 2) + c * 16;
  sfor<13>([&](auto I) {
    constexpr int i = I + 4;
    if constexpr (I >= 3 && (I % 3) == 0) burst<I / 3 - 1, 16>(grp, rq, vq, 4 * NH * DH * 2, ldsw, 2 * b);
    PACEN(I < 3 ? 14 : 3);
    if constexpr (I == 0) STAMP(10);
    K[i] = __builtin_amdgcn_raw_buffer_load_b128(rk, vkv, i * 8192, AUX_NT);
    SB();
  });
  unsigned xlo, xhi;
  {
    float xf[8];
#pragma unroll
    for (int k = 0; k < 4; ++k) { xf[2 * k] = asf(xrow[k] << 16); xf[2 * k + 1] = asf(xrow[k] & 0xffff0000u); }
    const float mean = wave_sum(((xf[0] + xf[1]) + (xf[2] + xf[3])) + ((xf[4] + xf[5]) + (xf[6] + xf[7]))) * (1.f / D);
    float vs = 0.f;
#pragma unroll
    for (int k = 0; k < 8; ++k) { const float d = xf[k] - mean; vs = fmaf(d, d, vs); }
    const float var = wave_sum(vs) * (1.f / D);
    const float den = rbf(sqrtf(rbf(rbf(var) + 1e-5f)));
    const float xn = rbf(rbf(xe - rbf(mean)) / den);
    xhi = fbits(xn); xlo = xhi >> 16;
  }
  SB();
  asm volatile("s_cmp_lg_u32 %0, 7\n\ts_cbranch_scc1 CW%=\n\ts_waitcnt vmcnt(0)\nCW%=:" :: "s"(w) : "memory", "scc");
  __builtin_amdgcn_raw_buffer_store_b32(hb0 + 1u, rws, v_hb, 0, AUX_SC1);
  SB();
  sfor<4>([&](auto I) {
    constexpr int i = I + 17;
    PACE();
    K[i] = __builtin_amdgcn_raw_buffer_load_b128(rk, vkv, i * 8192, AUX_NT);
    SB();
  });
  STAMP(1);
  unsigned qp[4];
  {
    float acc[8] = {0.f, 0.f, 0.f, 0.f, 0.f, 0.f, 0.f, 0.f};
    sfor<16>([&](auto I) {
      const u32x4 wv = *(const LAS u32x4*)(L + L_W + w * 16384 + I * 1024 + lane * 16);
      pv_step<I>(acc, xlo, xhi, wv);
      if constexpr (I == 7 || I == 15) {
        constexpr int i = 21 + (I >> 3);
        SB(); PACE();
        K[i] = __builtin_amdgcn_raw_buffer_load_b128(rk, vkv, i * 8192, AUX_NT);
        SB();
      }
    });
    *(LAS f32x2*)(L + L_RED + tid * 8) = fold_rows8(acc);
    BARRIER();
    f32x2 s = *(const LAS f32x2*)(L + L_RED + lane * 8);
#pragma unroll
    for (int i = 1; i < 8; ++i) s += *(const LAS f32x2*)(L + L_RED + i * 512 + lane * 8);
    rows_bcast(cvtpk(s[0], s[1]), qp);
  }
  SB();
  STAMP(2);
  const rsrc_t r1 = mk_rsrc(w1, D * F * 2);
  const int v1 = ((lane & 7) < 6 && col0 + (lane & 7) * 8 < F) ? (64 * w + (lane >> 3)) * (F * 2) + col0 * 2 + (lane & 7) * 16 : OOB;
  const int c2 = min(col0, F);
  const rsrc_t r2 = mk_rsrc(w2 + (size_t)c2 * (D * 2), (F - c2) * (D * 2));
  const int vo = (lane >> 3) * (D * 2) + w * 128 + (lane & 7) * 16;
  u32x4 V[32];
  float sc[2], xs[16];
  auto qk = [&](const u32x4& kv) { return dot2(kv[3], qp[3], dot2(kv[2], qp[2], dot2(kv[1], qp[1], dot2(kv[0], qp[0], 0.f)))); };
  sfor<13>([&](auto I) {
    constexpr int i = I + 23;
    if constexpr ((I & 3) == 0) {
      burst<I / 4, 8>(grp, r1, v1, 8 * F * 2, ldsw, b);
      burst<I / 4, 6>(grp, r2, vo, 8 * D * 2, ldsw + 8192, b < 6 ? b : b - 6);
    }
    PACE();
    if constexpr (i < 32) K[i] = __builtin_amdgcn_raw_buffer_load_b128(rk, vkv, i * 8192, AUX_NT);
    else V[i - 32] = __builtin_amdgcn_raw_buffer_load_b128(rv, vkv, (i - 32) * 8192, AUX_NT);
    SB();
    if constexpr (I < 4) { sfor<4>([&](auto J) { xs[4 * I + J] = qk(K[4 * I + J]); }); }
    if constexpr (I == 4) sc[0] = transpose_reduce16(xs, c);
    if constexpr (I >= 5) { xs[2 * (I - 5)] = qk(K[16 + 2 * (I - 5)]); xs[2 * (I - 5) + 1] = qk(K[17 + 2 * (I - 5)]); }
    SB();
  });
  sc[1] = transpose_reduce16(xs, c);
  SB(); PACE();
  V[4] = __builtin_amdgcn_raw_buffer_load_b128(rv, vkv, 4 * 8192, AUX_NT);
  const unsigned snap2 = __builtin_amdgcn_raw_buffer_load_b32(rws, o_snap, 0, AUX_SC1);
  SB();
  unsigned plo[2], phi[2];
  {
    const float s0 = rbf(rbf(sc[0]) * 0.08838834764831845f), s1 = rbf(rbf(sc[1]) * 0.08838834764831845f);
    const float mw = wave_max(fmaxf(s0, s1));
    const float e0 = __builtin_amdgcn_exp2f((s0 - mw) * 1.4426950408889634f), e1 = __builtin_amdgcn_exp2f((s1 - mw) * 1.4426950408889634f);
    const float lw = wave_sum(e0 + e1);
    if (lane == 0) *(LAS f32x2*)(L + L_ML + w * 8) = f32x2{mw, lw};
    SB(); PACE();
    V[5] = __builtin_amdgcn_raw_buffer_load_b128(rv, vkv, 5 * 8192, AUX_NT);
    SB();
    BARRIER();
    const f32x2 ml = *(const LAS f32x2*)(L + L_ML + (lane & 7) * 8);
    float M = ml[0];
    M = fmaxf(M, dppf<XOR1>(M)); M = fmaxf(M, dppf<XOR2>(M)); M = fmaxf(M, dppf<HMIR>(M));
    float Ls = ml[1] * __builtin_amdgcn_exp2f((ml[0] - M) * 1.4426950408889634f);
    Ls += dppf<XOR1>(Ls); Ls += dppf<XOR2>(Ls); Ls += dppf<HMIR>(Ls);
    const float scale = __builtin_amdgcn_exp2f((mw - M) * 1.4426950408889634f) / Ls;
    const unsigned pp = cvtpk(e0 * scale, e1 * scale);
    plo[0] = pp & 0xffffu; phi[0] = pp << 16; plo[1] = pp >> 16; phi[1] = pp & 0xffff0000u;
  }
  SB();
  STAMP(3);
  const rsrc_t ro = mk_rsrc(wo + (size_t)h * (DH * D * 2), DH * D * 2);
  float acc[8] = {0.f, 0.f, 0.f, 0.f, 0.f, 0.f, 0.f, 0.f};
  u32x2 W1[12], W2[12], WO[32];
  LAS unsigned char* tr1 = L + L_W + w * 16384 + (4 * ((lane >> 2) & 3) + r) * 128 + (lane & 3) * 8;
  LAS unsigned char* tro = L + L_W + w * 16384 + ((lane >> 2) & 3) * 128 + r * 32 + (lane & 3) * 8;
  unsigned go1 = 0;
  u32x4 G[8];
  const int vg = WS_P1 + (b * 32 + r) * 2048 + w * 256 + c * 16;
  float c1 = 0.f;
  int vp1 = WS_P1 + blk * 2048 + tid * 4;
  int vp2 = WS_P2 + ((b * 32 + (tid >> 4)) * 32 + slice) * 64 + (tid & 15) * 4;
  int lh = kcol * 4;
  sfor<30>([&](auto I) {
    constexpr int i = I + 6;
    if constexpr (i >= 10 && i <= 22 && ((i - 10) & 3) == 0) burst<(i - 10) / 4, 16>(grp, ro, vo, 8 * D * 2, ldsw, 2 * b);
    PACE();
    if constexpr (i < 32) V[i] = __builtin_amdgcn_raw_buffer_load_b128(rv, vkv, i * 8192, AUX_NT);
    else G[i - 32] = __builtin_amdgcn_raw_buffer_load_b128(rws, vg, (i - 32) * 8192, AUX_SC1);
    if constexpr (i == 24) go1 = __builtin_amdgcn_raw_buffer_load_b32(rws, v_go, 0, AUX_SC1);
    SB();
    if constexpr (i == 6) { pv_step<0>(acc, plo[0], phi[0], V[0]); pv_step<1>(acc, plo[0], phi[0], V[1]); }
    pv_step<(i - 4) & 15>(acc, plo[(i - 4) >> 4], phi[(i - 4) >> 4], V[i - 4]);
    pin8(acc);
    if constexpr (i == 8) {
      sfor<12>([&](auto J) { W1[J] = tr_read(tr1 + (J & 3) * 16 * 128 + (J >> 2) * 32); });
      sfor<12>([&](auto J) { W2[J] = tr_read(tro + 8192 + J * 512); });
      const bool go_now = __builtin_amdgcn_ballot_w64(snap2 == snap) == 0;
      __builtin_amdgcn_raw_buffer_store_b32(go0 + 1u, rws, go_now ? v_go : OOB, 0, AUX_SC1);
    }
    if constexpr (i == 9) {
      float a[3] = {0.f, 0.f, 0.f};
      sfor<12>([&](auto J) { a[J >> 2] = dot2(W1[J][1], 0x3f803f80u, dot2(W1[J][0], 0x3f803f80u, a[J >> 2])); });
      *(LAS float*)(L + L_EX + tid * 4) = fold_rows4(a[0], a[1], a[2], 0.f);
      asm volatile("s_waitcnt lgkmcnt(0)" ::: "memory");
    }
    if constexpr (i >= 27 && i <= 30) sfor<8>([&](auto J) { WO[8 * (i - 27) + J] = tr_read(tro + (8 * (i - 27) + J) * 512); });
    if constexpr (i == 29) {
      {
        unsigned beat = hb0 + 1u, tmp, sg, sg0, spins;
        unsigned long long seen = __builtin_amdgcn_ballot_w64(snap2 != snap);
        asm volatile(
            "s_cmp_lg_u32 %6, 7\n\t"
            "s_cbranch_scc1 HE%=\n\t"
            "v_readfirstlane_b32 %3, %7\n\t"
            "v_readfirstlane_b32 %2, %8\n\t"
            "s_mov_b32 %4, 0\n\t"
            "s_nop 3\n"
            "HL%=:\n\t"
            "s_cmp_lg_u32 %2, %3\n\t"
            "s_cbranch_scc1 HE%=\n\t"
            "s_add_u32 %4, %4, 1\n\t"
            "s_cmp_gt_u32 %4, %15\n\t"
            "s_cbranch_scc1 HE%=\n\t"
            "v_add_u32 %0, 1, %0\n\t"
            "buffer_store_dword %0, %10, %9, 0 offen sc1\n\t"
            "buffer_load_dword %1, %11, %9, 0 offen sc1\n\t"
            "s_waitcnt vmcnt(0)\n\t"
            "v_cmp_ne_u32 vcc, %1, %12\n\t"
            "s_nop 3\n\t"
            "s_or_b64 %5, %5, vcc\n\t"
            "s_cmp_eq_u64 %5, -1\n\t"
            "s_cbranch_scc0 HN%=\n\t"
            "buffer_store_dword %14, %13, %9, 0 offen sc1\n"
            "HN%=:\n\t"
            "buffer_load_dword %1, %13, %9, 0 offen sc1\n\t"
            "s_waitcnt vmcnt(0)\n\t"
            "v_readfirstlane_b32 %2, %1\n\t"
            "s_nop 3\n\t"
            "s_branch HL%=\n"
            "HE%=:"
            : "+v"(beat), "=&v"(tmp), "=&s"(sg), "=&s"(sg0), "=&s"(spins), "+s"(seen)
            : "s"(w), "v"(go0), "v"(go1), "s"(rws), "v"(v_hb), "v"(o_snap), "v"(snap), "v"(v_go), "v"(go0 + 1u), "s"(SPIN_CAP)
            : "memory", "scc", "vcc");
      }
      BARRIER();
#pragma unroll
      for (int k = 0; k < 8; ++k) c1 += *(const LAS float*)(L + L_EX + k * 256 + lane * 4);
      PIN(c1); PIN(vp1); PIN(vp2); PIN(b1v); PIN(b2v); PIN(lh);
    }
    SB();
  });
  STAMP(4);
  {
    *(LAS f32x2*)(L + L_RED2 + tid * 8) = fold_rows8(acc);
    BARRIER();
    f32x2 s = *(const LAS f32x2*)(L + L_RED2 + lane * 8);
#pragma unroll
    for (int i = 1; i < 8; ++i) s += *(const LAS f32x2*)(L + L_RED2 + i * 512 + lane * 8);
    unsigned op[4];
    rows_bcast(cvtpk(s[0], s[1]), op);
    SB();
    sfor<4>([&](auto I) { G[4 + I] = __builtin_amdgcn_raw_buffer_load_b128(rws, vg, (4 + I) * 8192, AUX_SC1); });
    float y[4] = {0.f, 0.f, 0.f, 0.f};
    asm volatile("s_nop 1" : "+v"(op[0]), "+v"(op[1]), "+v"(op[2]), "+v"(op[3]), "+v"(y[0]), "+v"(y[1]), "+v"(y[2]), "+v"(y[3]));
    dots_o<0>(y, op, WO); dots_o<4>(y, op, WO + 8); dots_o<8>(y, op, WO + 16); dots_o<12>(y, op, WO + 24);
    asm volatile("s_nop 2" : "+v"(y[0]), "+v"(y[1]), "+v"(y[2]), "+v"(y[3]));
    __builtin_amdgcn_raw_buffer_store_b32(min(fbits((y[0] + y[1]) + (y[2] + y[3])), SENT - 1u), rws, vp1, 0, AUX_SC1);
  }
  SB();
  STAMP(5);
  for (unsigned spins = 0; spins < SPIN_CAP; ++spins) {
    bool any = false;
    asm volatile("" ::: "memory");
    sfor<8>([&](auto I) {
      if (__builtin_amdgcn_ballot_w64(max4(G[I]) == SENT) != 0) {
        G[I] = __builtin_amdgcn_raw_buffer_load_b128(rws, vg, I * 8192, AUX_SC1);
        any = true;
      }
    });
    if (!any) break;
  }
  SB();
  STAMP(6);
  {
    f32x4 s = __builtin_bit_cast(f32x4, G[0]);
#pragma unroll
    for (int i = 1; i < 8; ++i) s += __builtin_bit_cast(f32x4, G[i]);
    const float hv = rbf(xe + rbf(fold_rows4(s[0], s[1], s[2], s[3])));
    *(LAS float*)(L + L_H + lh) = hv;
    unsigned hp = (fbits(hv) >> 16) | dppu<XOR1>(fbits(hv));
    const float h2 = hv * hv;
    const float t1 = hv + dppf<XOR1>(hv), t2 = h2 + dppf<XOR1>(h2);
    float stt = (c & 1) ? t2 : t1;
    stt += dppf<XOR2>(stt); stt += dppf<ROR4>(stt); stt += dppf<ROR8>(stt);
    float a0 = 0.f, a1 = 0.f, a2 = 0.f;
    dots_up(a0, a1, a2, hp, W1);
    *(LAS float*)(L + L_EX2 + tid * 4) = fold_rows4(a0, a1, a2, stt);
    BARRIER();
    float tot = *(const LAS float*)(L + L_EX2 + lane * 4);
#pragma unroll
    for (int k = 1; k < 8; ++k) tot += *(const LAS float*)(L + L_EX2 + k * 256 + lane * 4);
    const float mean = asf(__builtin_amdgcn_readlane(fbits(tot), 48)) * (1.f / D);
    const float var = fmaxf(fmaf(-mean, mean, asf(__builtin_amdgcn_readlane(fbits(tot), 49)) * (1.f / D)), 0.f);
    const float den = rbf(__builtin_amdgcn_sqrtf(rbf(rbf(var) + 1e-5f)));
    const float u = rbf(rbf(fmaf(-rbf(mean), c1, tot) * __builtin_amdgcn_rcpf(den)) + b1v);
    const float z = u * fmaf(u * u, 0.044715f * 0.7978845608028654f, 0.7978845608028654f);
    const float g = u * __builtin_amdgcn_rcpf(1.f + __builtin_amdgcn_exp2f(z * (-2.f * 1.4426950408889634f)));
    unsigned gp[4];
    rows_bcast(cvtpk(g, dppf<XOR1>(g)), gp);
    float y[3] = {0.f, 0.f, 0.f};
    dots_dn(y, gp, W2);
    __builtin_amdgcn_raw_buffer_store_b32(min(fbits((y[0] + y[1]) + y[2]), SENT - 1u), rws, vp2, 0, AUX_SC1);
  }
  SB();
  STAMP(7);
  if (w == 0) {
    const float hres = *(const LAS float*)(L + L_H + ocol * 4);
    const int vf = WS_P2 + blk * 2048 + lane * 16;
    u32x4 A, Bv;
    __builtin_amdgcn_s_sleep(10);
    for (unsigned spins = 0; spins < SPIN_CAP; ++spins) {
      asm volatile("" ::: "memory");
      A = __builtin_amdgcn_raw_buffer_load_b128(rws, vf, 0, AUX_SC1);
      Bv = __builtin_amdgcn_raw_buffer_load_b128(rws, vf, 1024, AUX_SC1);
      if (__builtin_amdgcn_ballot_w64(max(max4(A), max4(Bv)) == SENT) == 0) break;
    }
    STAMP(8);
    const f32x4 s = __builtin_bit_cast(f32x4, A) + __builtin_bit_cast(f32x4, Bv);
    float t = fold_rows4(s[0], s[1], s[2], s[3]);
    t += dppf<ROR4>(t); t += dppf<ROR8>(t);
    const float res = hres + rbf(rbf(t) + b2v);
    if (c < 4) out[b * D + ocol] = __builtin_bit_cast(unsigned short, (__bf16)res);
    STAMP(9);
    { const unsigned long long tgt = st[9] + (st[XK] - st[0]); while (__builtin_amdgcn_s_memrealtime() < tgt) {} }
  }
}

extern "C" void kernel_launch(void* const* d_in, const int* in_sizes, int n_in, void* d_out, int out_size, void* d_ws,
                              size_t ws_size, hipStream_t stream) {
  fused_block<<<NB * NH, 512, 0, stream>>>((const unsigned short*)d_in[0], (const unsigned char*)d_in[1], (const unsigned char*)d_in[2],
                                           (const unsigned char*)d_in[3], (const unsigned char*)d_in[4], (const unsigned char*)d_in[6],
                                           (const unsigned char*)d_in[7], (const unsigned char*)d_in[8], (const unsigned short*)d_in[9],
                                           (unsigned short*)d_out, (unsigned char*)d_ws);
}
```

```cpp
#define XMUL 0
#include <hip/hip_runtime.h>
#include <hip/hip_bf16.h>
#include <stdint.h>

#define STAMPS 0
#define PFCODE 0
#define WN 3
#define BWN 3
#define QB0 4
#define QRDY 9
#define QSTEP 28
#define OB0 19
#define MB0 38
#define INB 5248
#define SENTM 0
#define SENTS 61
#define BEATM 1
#define BEATD 8
#define CHKM 1
#define HBP 4
#define HBS 256
#define EARLYST 0
#define EXPM 0
#define DLY0 24
#define DLY1 10
#define DLY2 24

typedef unsigned u32x4 __attribute__((ext_vector_type(4)));
typedef unsigned u32x2 __attribute__((ext_vector_type(2)));
typedef float f32x2 __attribute__((ext_vector_type(2)));
typedef float f32x4 __attribute__((ext_vector_type(4)));
typedef __bf16 bf16x2 __attribute__((ext_vector_type(2)));
typedef short s16x4 __attribute__((ext_vector_type(4)));
typedef __amdgpu_buffer_rsrc_t rsrc_t;
#define LAS __attribute__((address_space(3)))
#define DI __device__ __forceinline__

constexpr int NB = 8, D = 512, NH = 32, SK = 4096, DH = 128, WIN = 1024, F = 1376;
constexpr int FS = 48;
constexpr int AUX_NT = 2, AUX_SC1 = 16;
constexpr int OOB = 0x7ffffff0;
constexpr unsigned SENT = 0xffffffffu;
constexpr unsigned SPIN_CAP = 1u << 17;
constexpr int IN_P1 = 0;
constexpr int IN_P2 = 2048;
constexpr int IN_H = 4096;
constexpr int IN_BYTES = INB;
constexpr int WS_HB = 256 * 8192;
constexpr int WS_BYTES = WS_HB + 32768;
constexpr int WS_STAMP = 4 * 1024 * 1024;
constexpr int L_RED = 0;
constexpr int L_RED2 = L_RED + 2048;
constexpr int L_EX = L_RED2 + 4096;
constexpr int L_EX2 = L_EX + 1024;
constexpr int L_ML = L_EX2 + 1024;
constexpr int L_DUMP = 9216;
constexpr int L_W = L_DUMP + 8192;
constexpr int L_BYTES = L_W + 128 * 1024;

DI float dot2(unsigned a, unsigned b, float acc) {
  return __builtin_amdgcn_fdot2_f32_bf16(__builtin_bit_cast(bf16x2, a), __builtin_bit_cast(bf16x2, b), acc, false);
}
DI unsigned fbits(float f) { return __builtin_bit_cast(unsigned, f); }
DI float asf(unsigned u) { return __builtin_bit_cast(float, u); }
DI unsigned cvtpk(float lo, float hi) {
  bf16x2 r = {(__bf16)lo, (__bf16)hi};
  return __builtin_bit_cast(unsigned, r);
}
DI float rbf(float f) { return asf((unsigned)__builtin_bit_cast(unsigned short, (__bf16)f) << 16); }
template <int CTRL> DI unsigned dppu(unsigned x) { return __builtin_amdgcn_mov_dpp(x, CTRL, 0xf, 0xf, true); }
template <int CTRL> DI float dppf(float x) { return asf(dppu<CTRL>(fbits(x))); }
constexpr int XOR1 = 0xB1, XOR2 = 0x4E, HMIR = 0x141, ROR4 = 0x124, ROR8 = 0x128, BCAST = 0x150;
DI float tsum32(float a, float b) {
  auto s = __builtin_amdgcn_permlane32_swap(fbits(a), fbits(b), false, false);
  return asf(s[0]) + asf(s[1]);
}
DI float tsum16(float a, float b) {
  auto s = __builtin_amdgcn_permlane16_swap(fbits(a), fbits(b), false, false);
  return asf(s[0]) + asf(s[1]);
}
DI float row_sum(float x) { x += dppf<XOR1>(x); x += dppf<XOR2>(x); x += dppf<ROR4>(x); x += dppf<ROR8>(x); return x; }
DI float row_max(float x) { x = fmaxf(x, dppf<XOR1>(x)); x = fmaxf(x, dppf<XOR2>(x)); x = fmaxf(x, dppf<ROR4>(x)); x = fmaxf(x, dppf<ROR8>(x)); return x; }
DI float wave_sum(float x) { x = row_sum(x); x = tsum16(x, x); return tsum32(x, x); }
DI float wave_max(float x) {
  x = row_max(x);
  auto s = __builtin_amdgcn_permlane16_swap(fbits(x), fbits(x), false, false);
  x = fmaxf(asf(s[0]), asf(s[1]));
  auto t = __builtin_amdgcn_permlane32_swap(fbits(x), fbits(x), false, false);
  return fmaxf(asf(t[0]), asf(t[1]));
}
DI void rows_bcast(unsigned x, unsigned (&o)[4]) {
  auto s = __builtin_amdgcn_permlane16_swap(x, x, false, false);
  auto a = __builtin_amdgcn_permlane32_swap(s[0], s[0], false, false);
  auto b = __builtin_amdgcn_permlane32_swap(s[1], s[1], false, false);
  o[0] = a[0]; o[2] = a[1]; o[1] = b[0]; o[3] = b[1];
}
#define DB(y, p, w, c) "v_dot2c_f32_bf16_dpp %" #y ", %" #p ", %" #w " row_newbcast:%" #c " row_mask:0xf bank_mask:0xf\n\t"
#define W8(w) "v"(w[0][0]), "v"(w[0][1]), "v"(w[1][0]), "v"(w[1][1]), "v"(w[2][0]), "v"(w[2][1]), "v"(w[3][0]), "v"(w[3][1])
template <int C> DI void dots_o(float (&y)[4], const unsigned (&op)[4], const u32x2* wa, const u32x2* wb) {
  asm(DB(0, 4, 8, 24) DB(2, 4, 16, 24) DB(1, 5, 9, 24) DB(3, 5, 17, 24) DB(0, 6, 10, 24) DB(2, 6, 18, 24) DB(1, 7, 11, 24) DB(3, 7, 19, 24)
      DB(0, 4, 12, 25) DB(2, 4, 20, 25) DB(1, 5, 13, 25) DB(3, 5, 21, 25) DB(0, 6, 14, 25) DB(2, 6, 22, 25) DB(1, 7, 15, 25) DB(3, 7, 23, 25)
      : "+v"(y[0]), "+v"(y[1]), "+v"(y[2]), "+v"(y[3])
      : "v"(op[0]), "v"(op[1]), "v"(op[2]), "v"(op[3]), W8(wa), W8(wb), "i"(C), "i"(C + 1));
}
#define DC(y, p, w, c) "v_dot2c_f32_bf16_dpp %" #y ", %" #p ", %" #w " row_newbcast:" #c " row_mask:0xf bank_mask:0xf\n\t"
#define UP8(c0, c1, c2, c3, c4, c5, c6, c7) \
      DC(0, 3, 4, c0) DC(1, 3, 12, c0) DC(2, 3, 20, c0) DC(0, 3, 5, c1) DC(1, 3, 13, c1) DC(2, 3, 21, c1) \
      DC(0, 3, 6, c2) DC(1, 3, 14, c2) DC(2, 3, 22, c2) DC(0, 3, 7, c3) DC(1, 3, 15, c3) DC(2, 3, 23, c3) \
      DC(0, 3, 8, c4) DC(1, 3, 16, c4) DC(2, 3, 24, c4) DC(0, 3, 9, c5) DC(1, 3, 17, c5) DC(2, 3, 25, c5) \
      DC(0, 3, 10, c6) DC(1, 3, 18, c6) DC(2, 3, 26, c6) DC(0, 3, 11, c7) DC(1, 3, 19, c7) DC(2, 3, 27, c7)
DI void dots_up(float& a0, float& a1, float& a2, unsigned hp, const u32x2 (&w)[24]) {
  asm("s_nop 1\n\t" UP8(0, 1, 2, 3, 4, 5, 6, 7) : "+v"(a0), "+v"(a1), "+v"(a2) : "v"(hp), W8(w), W8((w + 8)), W8((w + 16)));
  asm(UP8(8, 9, 10, 11, 12, 13, 14, 15) "s_nop 2" : "+v"(a0), "+v"(a1), "+v"(a2) : "v"(hp), W8((w + 4)), W8((w + 12)), W8((w + 20)));
}
DI void dots_dn(float (&y)[4], unsigned g, const u32x2* wa, const u32x2* wb) {
  asm(DC(0, 4, 5, 0) DC(2, 4, 13, 0) DC(1, 4, 6, 2) DC(3, 4, 14, 2) DC(0, 4, 7, 4) DC(2, 4, 15, 4) DC(1, 4, 8, 6) DC(3, 4, 16, 6)
      DC(0, 4, 9, 8) DC(2, 4, 17, 8) DC(1, 4, 10, 10) DC(3, 4, 18, 10) DC(0, 4, 11, 12) DC(2, 4, 19, 12) DC(1, 4, 12, 14) DC(3, 4, 20, 14)
      : "+v"(y[0]), "+v"(y[1]), "+v"(y[2]), "+v"(y[3]) : "v"(g), W8(wa), W8(wb));
}
template <class T> DI void pin_(T& x) { asm volatile("" : "+v"(x)); }
#define PIN(x) pin_(x)
#define SB() __builtin_amdgcn_sched_barrier(0)
#define PACEN(n) do { asm volatile("s_waitcnt vmcnt(%0)" :: "i"(n) : "memory"); SB(); } while (0)
#define PACE() PACEN(WN)
#define BARRIER() do { asm volatile("s_waitcnt lgkmcnt(0)\n\ts_barrier" ::: "memory"); SB(); } while (0)
DI rsrc_t mk_rsrc(const void* p, int bytes) { return __builtin_amdgcn_make_buffer_rsrc((void*)p, 0, bytes, 0x00020000); }

template <int G, int N, int CNT = N> DI void burst(int grp, rsrc_t r, int voff, int stride, int lds, int j0) {
  int keep, j, cnt, t;
  asm volatile(
      "s_cmp_lg_u32 %4, %10\n\t"
      "s_cbranch_scc1 BE%=\n\t"
      "s_mov_b32 %0, m0\n\t"
      "s_mov_b32 %1, %9\n\t"
      "s_mov_b32 %2, %12\n"
      "BL%=:\n\t"
      "s_lshl_b32 %3, %1, 10\n\t"
      "s_add_u32 m0, %3, %8\n\t"
      "s_mul_i32 %3, %1, %7\n\t"
      "s_add_u32 %1, %1, 1\n\t"
      "s_waitcnt vmcnt(%13)\n\t"
      "buffer_load_dwordx4 %6, %5, %3 offen lds\n\t"
      "s_cmp_eq_u32 %1, %11\n\t"
      "s_cselect_b32 %1, 0, %1\n\t"
      "s_sub_u32 %2, %2, 1\n\t"
      "s_cmp_lg_u32 %2, 0\n\t"
      "s_cbranch_scc1 BL%=\n\t"
      "s_mov_b32 m0, %0\n"
      "BE%=:"
      : "=&s"(keep), "=&s"(j), "=&s"(cnt), "=&s"(t)
      : "s"(grp), "s"(r), "v"(voff), "s"(stride), "s"(lds), "s"(j0), "i"(G), "i"(N), "i"(CNT), "i"(BWN)
      : "memory", "scc");
  SB();
}
DI void ald128nt(u32x4& d, rsrc_t r, int voff, int soff) { asm volatile("buffer_load_dwordx4 %0, %1, %2, %3 offen nt" : "=v"(d) : "v"(voff), "s"(r), "s"(soff) : "memory"); SB(); }
DI void ald128(u32x4& d, rsrc_t r, int voff, int soff) { asm volatile("buffer_load_dwordx4 %0, %1, %2, %3 offen" : "=v"(d) : "v"(voff), "s"(r), "s"(soff) : "memory"); SB(); }
DI void ald64sc(u32x2& d, rsrc_t r, int voff, int soff) { asm volatile("buffer_load_dwordx2 %0, %1, %2, %3 offen sc1" : "=v"(d) : "v"(voff), "s"(r), "s"(soff) : "memory"); SB(); }
DI void ald32sc(unsigned& d, rsrc_t r, int voff) { asm volatile("buffer_load_dword %0, %1, %2, 0 offen sc1" : "=v"(d) : "v"(voff), "s"(r) : "memory"); SB(); }
DI void ald32(unsigned& d, rsrc_t r, int voff) { asm volatile("buffer_load_dword %0, %1, %2, 0 offen" : "=v"(d) : "v"(voff), "s"(r) : "memory"); SB(); }
DI void ald16(unsigned& d, rsrc_t r, int voff, int soff) { asm volatile("buffer_load_ushort %0, %1, %2, %3 offen" : "=v"(d) : "v"(voff), "s"(r), "s"(soff) : "memory"); SB(); }
template <bool NT = false> DI void dma1(rsrc_t r, int voff, int soff, int lds) {
  int keep;
  if constexpr (NT)
    asm volatile("s_mov_b32 %0, m0\n\ts_mov_b32 m0, %4\n\ts_nop 0\n\tbuffer_load_dwordx4 %1, %2, %3 offen nt lds\n\ts_mov_b32 m0, %0"
                 : "=&s"(keep) : "v"(voff), "s"(r), "s"(soff), "s"(lds) : "memory");
  else
    asm volatile("s_mov_b32 %0, m0\n\ts_mov_b32 m0, %4\n\ts_nop 0\n\tbuffer_load_dwordx4 %1, %2, %3 offen lds\n\ts_mov_b32 m0, %0"
                 : "=&s"(keep) : "v"(voff), "s"(r), "s"(soff), "s"(lds) : "memory");
  SB();
}
DI unsigned atom_add(rsrc_t r, int voff, int v) { return (unsigned)__builtin_amdgcn_raw_ptr_buffer_atomic_add_i32(v, r, voff, 0, 0); }
template <int G> DI void cond_barrier(int grp) {
  asm volatile("s_cmp_lg_u32 %0, %1\n\ts_cbranch_scc1 CB%=\n\ts_barrier\nCB%=:" :: "s"(grp), "i"(G) : "memory", "scc");
  SB();
}
DI float gather16(rsrc_t r, int voff);
DI u32x2 tr_read(LAS unsigned char* p) {
  return __builtin_bit_cast(u32x2, __builtin_amdgcn_ds_read_tr16_b64_v4i16((LAS s16x4*)p));
}
DI f32x2 fold_rows8(const float (&acc)[8]) {
  const float t0 = tsum32(acc[0], acc[4]), t1 = tsum32(acc[1], acc[5]), t2 = tsum32(acc[2], acc[6]), t3 = tsum32(acc[3], acc[7]);
  return f32x2{tsum16(t0, t2), tsum16(t1, t3)};
}
DI float fold_rows4(float a0, float a1, float a2, float a3) { return tsum16(tsum32(a0, a2), tsum32(a1, a3)); }
DI unsigned max4(u32x4 v) { return max(max(v[0], v[1]), max(v[2], v[3])); }

DI float gather16(rsrc_t r, int voff) {
  u32x4 A, Bv;
  for (unsigned spins = 0; spins < SPIN_CAP; ++spins) {
    asm volatile("" ::: "memory");
    A = __builtin_amdgcn_raw_buffer_load_b128(r, voff, 0, AUX_SC1);
    Bv = __builtin_amdgcn_raw_buffer_load_b128(r, voff, 1024, AUX_SC1);
    if (__builtin_amdgcn_ballot_w64(max(max4(A), max4(Bv)) == SENT) == 0) break;
  }
  const f32x4 s = __builtin_bit_cast(f32x4, A) + __builtin_bit_cast(f32x4, Bv);
  float t = fold_rows4(s[0], s[1], s[2], s[3]);
  t += dppf<ROR4>(t); t += dppf<ROR8>(t);
  return t;
}

template <int I> DI void pv_step(float (&acc)[8], unsigned plo, unsigned phi, u32x4 v) {
  const unsigned lo = dppu<BCAST + I>(plo), hi = dppu<BCAST + I>(phi);
#pragma unroll
  for (int k = 0; k < 4; ++k) { acc[2 * k] = dot2(lo, v[k], acc[2 * k]); acc[2 * k + 1] = dot2(hi, v[k], acc[2 * k + 1]); }
}
DI void pin8(float (&a)[8]) {
  asm volatile("" : "+v"(a[0]), "+v"(a[1]), "+v"(a[2]), "+v"(a[3]), "+v"(a[4]), "+v"(a[5]), "+v"(a[6]), "+v"(a[7]));
}
template <int... Is> struct iseq {};
template <int N, int... Is> struct mkseq : mkseq<N - 1, N - 1, Is...> {};
template <int... Is> struct mkseq<0, Is...> { typedef iseq<Is...> type; };
template <class Fn, int... Is> DI void sfor_(Fn&& f, iseq<Is...>) { (f(std::integral_constant<int, Is>{}), ...); }
template <int N, class Fn> DI void sfor(Fn&& f) { sfor_(f, typename mkseq<N>::type{}); }

DI float transpose_reduce16(float (&x)[16], int c) {
#define TR_STEP(HALF, CTRL, BIT) _Pragma("unroll") for (int h_ = 0; h_ < (HALF); ++h_) { \
    const float lo_ = x[h_], hi_ = x[h_ + (HALF)]; const float t1_ = lo_ + dppf<CTRL>(lo_), t2_ = hi_ + dppf<CTRL>(hi_); \
    x[h_] = (c & (BIT)) ? t2_ : t1_; }
  TR_STEP(8, ROR8, 8) TR_STEP(4, HMIR, 4) TR_STEP(2, XOR2, 2) TR_STEP(1, XOR1, 1)
#undef TR_STEP
  return x[0];
}

#define STAMP(i) do {} while (0)
#define ESTAMP(i) do {} while (0)

__global__ void __launch_bounds__(768, 3)
fused_block(const unsigned short* __restrict__ x, const unsigned char* __restrict__ kc, const unsigned char* __restrict__ vc,
            const unsigned char* __restrict__ wq, const unsigned char* __restrict__ wo, const unsigned char* __restrict__ w1,
            const unsigned char* __restrict__ b1, const unsigned char* __restrict__ w2, const unsigned short* __restrict__ b2,
            unsigned short* __restrict__ out, unsigned char* ws) {
  __shared__ __attribute__((aligned(16))) unsigned char smem[L_BYTES];
  LAS unsigned char* L = (LAS unsigned char*)smem;
  const unsigned long long xt0 = __builtin_amdgcn_s_memrealtime();
  const int blk = blockIdx.x, b = blk >> 5, h = blk & 31;
  const int tid = threadIdx.x, lane = tid & 63, r = lane >> 4, c = lane & 15;
  const int w = __builtin_amdgcn_readfirstlane(tid >> 6);
  const int slice = (h & 7) * 4 + (h >> 3), col0 = slice * FS;
#define IC(n) std::integral_constant<int, (n)>{}
  if (w >= 8) {
  const int u = w - 8;
  const rsrc_t rws = mk_rsrc(ws, WS_BYTES);
  const int vin = blk * IN_BYTES + lane * 16;
  const u32x4 sent4 = {SENT, SENT, SENT, SENT};
  if (!(EXPM & 1)) {
    __builtin_amdgcn_raw_buffer_store_b128(sent4, rws, vin, 1024 + u * 1024, AUX_SC1);
    __builtin_amdgcn_raw_buffer_store_b128(sent4, rws, u == 0 ? vin : OOB, 0, AUX_SC1);
  }
  const u32x4 xrow = *(const u32x4*)(x + b * D + lane * 8);
  const float xe0 = asf((unsigned)x[b * D + 128 * u + 4 * c + r] << 16), xe1 = asf((unsigned)x[b * D + 128 * u + 64 + 4 * c + r] << 16);
  const int v_hb = (u == 0 && lane < 32 && !(EXPM & 2)) ? WS_HB + b * HBS + lane * HBP : OOB;
  const unsigned snap = atom_add(rws, v_hb, 0);
  float b1v = asf((unsigned)__builtin_amdgcn_raw_buffer_load_b16(mk_rsrc(b1, F * 2), lane < FS ? (col0 + lane) * 2 : OOB, 0, 0) << 16);
  if (PFCODE && b == 0 && u >= 2) {
    unsigned long long pc; int o0, o1;
    asm volatile("s_getpc_b64 %0\nKPC0:\n\ts_mov_b32 %1, KPC0-KPCA\n\ts_mov_b32 %2, KPCB-KPCA" : "=s"(pc), "=s"(o0), "=s"(o1));
    const rsrc_t rc = mk_rsrc((const void*)(pc - (long long)o0), o1);
    unsigned p0 = __builtin_amdgcn_raw_buffer_load_b32(rc, lane * 128, (u - 2) * 8192, 0);
    PIN(p0);
  }
  SB();
  {
    float xf[8];
#pragma unroll
    for (int k = 0; k < 4; ++k) { xf[2 * k] = asf(xrow[k] << 16); xf[2 * k + 1] = asf(xrow[k] & 0xffff0000u); }
    const float mean = wave_sum(((xf[0] + xf[1]) + (xf[2] + xf[3])) + ((xf[4] + xf[5]) + (xf[6] + xf[7]))) * (1.f / D);
    float vs = 0.f;
#pragma unroll
    for (int k = 0; k < 8; ++k) { const float d = xf[k] - mean; vs = fmaf(d, d, vs); }
    const float var = wave_sum(vs) * (1.f / D);
    const float den = rbf(sqrtf(rbf(rbf(var) + 1e-5f))), mb = rbf(mean);
    const unsigned xp = cvtpk(rbf(xe0 - mb) / den, rbf(xe1 - mb) / den);
    unsigned xlo[2] = {xp & 0xffffu, xp >> 16}, xhi[2] = {xp << 16, xp & 0xffff0000u};
    PIN(xlo[0]); PIN(xlo[1]); PIN(xhi[0]); PIN(xhi[1]); PIN(b1v);
    PACEN(0);
    ESTAMP(5);
    if (BEATM == 1) { if (BEATD) __builtin_amdgcn_s_sleep(BEATD); (void)atom_add(rws, lane == h ? v_hb : OOB, 1); }
    BARRIER();
    ESTAMP(6);
    if (BEATM == 0) (void)atom_add(rws, lane == h ? v_hb : OOB, 1);
    float acc[8] = {0.f, 0.f, 0.f, 0.f, 0.f, 0.f, 0.f, 0.f};
    sfor<32>([&](auto I) { pv_step<I & 15>(acc, xlo[I >> 4], xhi[I >> 4], *(const LAS u32x4*)(L + L_W + u * 32768 + I * 1024 + lane * 16)); });
    *(LAS f32x2*)(L + L_RED + (u >> 1) * 1024 + lane * 16 + (u & 1) * 8) = fold_rows8(acc);
  }
  STAMP(11);
  BARRIER();
  BARRIER();
  u32x2 WA[32], WB[32];
  LAS unsigned char* tro = L + L_W + u * 32768 + ((lane >> 2) & 3) * 128 + r * 32 + (lane & 3) * 8;
  sfor<32>([&](auto J) { WA[J] = tr_read(tro + J * 512); WB[J] = tr_read(tro + 16384 + J * 512); });
  sfor<32>([&](auto J) { PIN(WA[J]); PIN(WB[J]); });
  BARRIER();
  if (CHKM == 1) BARRIER();
  if (u == 0 && !(EXPM & 2)) {
    for (unsigned spins = 0; spins < SPIN_CAP; ++spins) {
      const unsigned cur = atom_add(rws, v_hb, 0);
      if (__builtin_amdgcn_ballot_w64(lane < 32 && cur == snap) == 0) break;
    }
  }
  int vp = (b * 32 + 8 * u + (lane >> 3)) * IN_BYTES + (lane & 7) * 8;
  int vhl = blk * IN_BYTES + IN_H + u * 256 + lane * 4;
  int lrd = lane * 16, lwr = lane * 16 + u * 4;
  PIN(vp); PIN(vhl); PIN(lrd); PIN(lwr);
  float y[4] = {0.f, 0.f, 0.f, 0.f};
  PIN(y[0]); PIN(y[1]); PIN(y[2]); PIN(y[3]);
  BARRIER();
  {
    f32x4 s4 = *(const LAS f32x4*)(L + L_RED2 + lrd) + *(const LAS f32x4*)(L + L_RED2 + 1024 + lrd);
    s4 += *(const LAS f32x4*)(L + L_RED2 + 2048 + lrd) + *(const LAS f32x4*)(L + L_RED2 + 3072 + lrd);
    unsigned op[4];
    rows_bcast(cvtpk(s4[0] + s4[2], s4[1] + s4[3]), op);
    asm volatile("s_nop 1" : "+v"(op[0]), "+v"(op[1]), "+v"(op[2]), "+v"(op[3]));
    sfor<8>([&](auto J) { dots_o<2 * J>(y, op, WA + 4 * J, WB + 4 * J); });
    asm volatile("s_nop 2" : "+v"(y[0]), "+v"(y[1]), "+v"(y[2]), "+v"(y[3]));
    const u32x2 pv = {min(fbits(y[0] + y[1]), SENT - 1u), min(fbits(y[2] + y[3]), SENT - 1u)};
    __builtin_amdgcn_raw_buffer_store_b64(pv, rws, (EXPM & 8) ? OOB : vp, IN_P1 + h * 64, AUX_SC1);
  }
  SB();
  STAMP(5);
  (void)atom_add(rws, lane == h ? v_hb : OOB, 1);
  u32x2 W1[24], W2A[12], W2B[12];
  {
    LAS unsigned char* tr1 = L + L_W + u * 32768 + ((lane >> 2) & 1) * 16384 + (16 * r + ((lane >> 3) & 1)) * 128 + (lane & 3) * 8;
    sfor<24>([&](auto J) { W1[J] = tr_read(tr1 + (J & 7) * 256 + (J >> 3) * 32); });
    sfor<12>([&](auto J) { W2A[J] = tr_read(tro + 8192 + J * 512); W2B[J] = tr_read(tro + 16384 + 8192 + J * 512); });
    float a[3] = {0.f, 0.f, 0.f};
    sfor<24>([&](auto J) { a[J >> 3] = dot2(W1[J][1], 0x3f803f80u, dot2(W1[J][0], 0x3f803f80u, a[J >> 3])); });
    *(LAS float*)(L + L_EX + lwr) = fold_rows4(a[0], a[1], a[2], 0.f);
    sfor<12>([&](auto J) { PIN(W2A[J]); PIN(W2B[J]); });
  }
  SB();
  STAMP(6);
  BARRIER();
  float c1;
  { const f32x4 t = *(const LAS f32x4*)(L + L_EX + lrd); c1 = (t[0] + t[1]) + (t[2] + t[3]); }
  float a0 = 0.f, a1 = 0.f, a2 = 0.f;
  y[0] = y[1] = y[2] = y[3] = 0.f;
  PIN(c1); PIN(a0); PIN(a1); PIN(a2); PIN(y[0]); PIN(y[1]); PIN(y[2]); PIN(y[3]);
  if (DLY1) __builtin_amdgcn_s_sleep(DLY1);
  SB();
  unsigned hp;
  for (unsigned spins = 0; spins < SPIN_CAP; ++spins) {
    asm volatile("" ::: "memory");
    hp = __builtin_amdgcn_raw_buffer_load_b32(rws, vhl, 0, AUX_SC1);
    if (__builtin_amdgcn_ballot_w64(hp == SENT) == 0) break;
  }
  SB();
  STAMP(8);
  {
    const float s1 = dot2(hp, 0x3f803f80u, 0.f), s2 = dot2(hp, hp, 0.f);
    dots_up(a0, a1, a2, hp, W1);
    const float t1 = s1 + dppf<ROR8>(s1), t2 = s2 + dppf<ROR8>(s2);
    float stt = (c & 8) ? t2 : t1;
    stt += dppf<XOR1>(stt); stt += dppf<XOR2>(stt); stt += dppf<HMIR>(stt);
    *(LAS float*)(L + L_EX2 + lwr) = fold_rows4(a0, a1, a2, stt);
    BARRIER();
    const f32x4 t4 = *(const LAS f32x4*)(L + L_EX2 + lrd);
    const float tot = (t4[0] + t4[1]) + (t4[2] + t4[3]);
    const float mean = asf(__builtin_amdgcn_readlane(fbits(tot), 48)) * (1.f / D);
    const float var = fmaxf(fmaf(-mean, mean, asf(__builtin_amdgcn_readlane(fbits(tot), 56)) * (1.f / D)), 0.f);
    const float den = rbf(__builtin_amdgcn_sqrtf(rbf(rbf(var) + 1e-5f)));
    const float uu = rbf(rbf(fmaf(-rbf(mean), c1, tot) * __builtin_amdgcn_rcpf(den)) + b1v);
    const float z = uu * fmaf(uu * uu, -2.f * 1.4426950408889634f * 0.044715f * 0.7978845608028654f, -2.f * 1.4426950408889634f * 0.7978845608028654f);
    const float g = uu * __builtin_amdgcn_rcpf(1.f + __builtin_amdgcn_exp2f(z));
    unsigned gp[4];
    rows_bcast(cvtpk(g, dppf<XOR1>(g)), gp);
    asm volatile("s_nop 1" : "+v"(gp[0]), "+v"(gp[1]), "+v"(gp[2]));
    dots_dn(y, gp[0], W2A, W2B); dots_dn(y, gp[1], W2A + 4, W2B + 4); dots_dn(y, gp[2], W2A + 8, W2B + 8);
    asm volatile("s_nop 2" : "+v"(y[0]), "+v"(y[1]), "+v"(y[2]), "+v"(y[3]));
    const u32x2 pv = {min(fbits(y[0] + y[1]), SENT - 1u), min(fbits(y[2] + y[3]), SENT - 1u)};
    __builtin_amdgcn_raw_buffer_store_b64(pv, rws, vp, IN_P2 + slice * 64, AUX_SC1);
  }
  asm volatile("KPCB:");
  SB();
  STAMP(9);
  } else {
  const int grp = (b + 2 * (h >> 3)) & 7;
  const size_t kvoff = ((size_t)blk * SK + (SK - WIN)) * (DH * 2);
  const rsrc_t rk = mk_rsrc(kc + kvoff, WIN * DH * 2), rv = mk_rsrc(vc + kvoff, WIN * DH * 2);
  const int vkv = tid * 16;
  const int lds0 = (int)(unsigned)(size_t)L, ldsw = lds0 + L_W + w * 16384, ldsd = lds0 + L_DUMP + w * 1024;

  u32x4 K[32], V[32];
  auto ldk = [&](auto I) { ald128nt(K[I], rk, vkv, I * 8192); };
  auto ldv = [&](auto I) { ald128nt(V[I], rv, vkv, I * 8192); };
  ldk(IC(0)); ldk(IC(1));
  asm volatile("KPCA:");
  const rsrc_t rq = mk_rsrc(wq + h * (DH * 2), D * NH * DH * 2 - h * (DH * 2));
  const int vq = (64 * w + r) * (NH * DH * 2) + c * 16;
  dma1(rq, vq, 2 * b * (4 * NH * DH * 2), ldsw + 2 * b * 1024);
  dma1(rq, vq, (2 * b + 1) * (4 * NH * DH * 2), ldsw + (2 * b + 1) * 1024);
  const rsrc_t ro = mk_rsrc(wo + (size_t)h * (DH * D * 2), DH * D * 2);
  const int vo = (lane >> 3) * (D * 2) + w * 128 + (lane & 7) * 16;
  const rsrc_t r1 = mk_rsrc(w1, D * F * 2);
  const int v1 = ((lane & 7) < 6 && col0 + (lane & 7) * 8 < F) ? (64 * w + (lane >> 3)) * (F * 2) + col0 * 2 + (lane & 7) * 16 : OOB;
  const int c2 = min(col0, F);
  const rsrc_t r2 = mk_rsrc(w2 + (size_t)c2 * (D * 2), (F - c2) * (D * 2));
  const int b6 = b < 6 ? b : b - 6;
  auto hook = [&](auto I) {
    constexpr int i = I;
    constexpr auto site = [](int i_, int p0) { return (i_ >= p0 && ((i_ - p0) & 1) == 0 && i_ - p0 < 16) ? (i_ - p0) / 2 : -1; };
    if constexpr (site(i, QB0 + 4) >= 0) cond_barrier<site(i, QB0 + 4)>(grp);
    if constexpr (site(i, QB0 + QRDY) >= 0) cond_barrier<site(i, QB0 + QRDY)>(grp);
    if constexpr (site(i, OB0 + 4) >= 0) cond_barrier<site(i, OB0 + 4)>(grp);
    if constexpr (CHKM == 1 && i == SENTS) BARRIER();
    if constexpr (site(i, QB0) >= 0) burst<site(i, QB0), 16, 14>(grp, rq, vq, 4 * NH * DH * 2, ldsw, (2 * b + 2) & 15);
    if constexpr (i == OB0 - 5) { PACE(); dma1(ro, vo, 2 * b * 8192, ldsd); PACE(); dma1(ro, vo, (2 * b + 1) * 8192, ldsd); }
    if constexpr (site(i, OB0) >= 0) burst<site(i, OB0), 16>(grp, ro, vo, 8 * D * 2, ldsw, 2 * b);
    if constexpr (i == MB0 - 5) { PACE(); dma1(r1, v1, b * (8 * F * 2), ldsd); }
    if constexpr (i == MB0 - 4) { PACE(); dma1(r2, vo, b * (8 * D * 2), ldsd); }
    if constexpr (site(i, MB0) >= 0) {
      burst<site(i, MB0), 8>(grp, r1, v1, 8 * F * 2, ldsw, b);
      burst<site(i, MB0), 6>(grp, r2, vo, 8 * D * 2, ldsw + 8192, b6);
    }
  };
  static_assert(QRDY > 4 && QB0 + 14 + QRDY < QSTEP && OB0 >= QB0 + QRDY && OB0 + 14 + 4 <= 37 && MB0 >= 38 && MB0 + 14 <= 63 && SENTS >= 38, "barrier order, staging area reuse");
  sfor<QSTEP - 2>([&](auto I) { constexpr int i = I + 2; if constexpr (i == 11) ESTAMP(7); hook(IC(i)); PACE(); ldk(IC(i)); });
  STAMP(1);
  unsigned qp[4];
  {
    const f32x4 s4 = *(const LAS f32x4*)(L + L_RED + lane * 16) + *(const LAS f32x4*)(L + L_RED + 1024 + lane * 16);
    rows_bcast(cvtpk(s4[0] + s4[2], s4[1] + s4[3]), qp);
  }
  SB();
  STAMP(2);
  float sc[2], xs[16];
  auto qk = [&](u32x4& kv) { PIN(kv); return dot2(kv[3], qp[3], dot2(kv[2], qp[2], dot2(kv[1], qp[1], dot2(kv[0], qp[0], 0.f)))); };
  static_assert(QSTEP <= 28, "");
  sfor<36 - QSTEP>([&](auto I) {
    constexpr int i = I + QSTEP;
    hook(IC(i));
    PACE();
    if constexpr (i < 32) ldk(IC(i)); else ldv(IC(i - 32));
    if constexpr (I < 3) sfor<(I < 2 ? 6 : 4)>([&](auto J) { xs[6 * I + J] = qk(K[6 * I + J]); PIN(xs[6 * I + J]); });
    if constexpr (I == 3) { sc[0] = transpose_reduce16(xs, c); PIN(sc[0]); }
    if constexpr (I >= 4) {
      constexpr int lo = (I == 4) ? 16 : (16 + 6 * (I - 4) < i - 4 ? 16 + 6 * (I - 4) : i - 4);
      constexpr int hi = (16 + 6 * (I - 3) < i - 3) ? 16 + 6 * (I - 3) : i - 3;
      sfor<hi - lo>([&](auto J) { xs[lo - 16 + J] = qk(K[lo + J]); PIN(xs[lo - 16 + J]); });
    }
    SB();
  });
  sc[1] = transpose_reduce16(xs, c);
  SB();
  hook(IC(36)); PACE(); ldv(IC(4));
  unsigned plo[2], phi[2];
  {
    const float s0 = rbf(rbf(sc[0]) * 0.08838834764831845f), s1 = rbf(rbf(sc[1]) * 0.08838834764831845f);
    const float mw = wave_max(fmaxf(s0, s1));
    const float e0 = __builtin_amdgcn_exp2f((s0 - mw) * 1.4426950408889634f), e1 = __builtin_amdgcn_exp2f((s1 - mw) * 1.4426950408889634f);
    const float lw = wave_sum(e0 + e1);
    *(LAS f32x2*)(L + L_ML + w * 8) = f32x2{mw, lw};
    SB();
    hook(IC(37)); PACE(); ldv(IC(5));
    BARRIER();
    const f32x2 ml = *(const LAS f32x2*)(L + L_ML + (lane & 7) * 8);
    float M = ml[0];
    M = fmaxf(M, dppf<XOR1>(M)); M = fmaxf(M, dppf<XOR2>(M)); M = fmaxf(M, dppf<HMIR>(M));
    float Ls = ml[1] * __builtin_amdgcn_exp2f((ml[0] - M) * 1.4426950408889634f);
    Ls += dppf<XOR1>(Ls); Ls += dppf<XOR2>(Ls); Ls += dppf<HMIR>(Ls);
    const float scale = __builtin_amdgcn_exp2f((mw - M) * 1.4426950408889634f) / Ls;
    const unsigned pp = cvtpk(e0 * scale, e1 * scale);
    plo[0] = pp & 0xffffu; phi[0] = pp << 16; plo[1] = pp >> 16; phi[1] = pp & 0xffff0000u;
  }
  SB();
  STAMP(3);
  float acc[8] = {0.f, 0.f, 0.f, 0.f, 0.f, 0.f, 0.f, 0.f};
  const int ocol = 128 * (h >> 3) + 64 * (r & 1) + 8 * (h & 7) + 2 * (c & 3) + (r >> 1);
  unsigned b2v, xo;
  int lw2 = (w >> 1) * 1024 + lane * 16 + (w & 1) * 8;
  sfor<30>([&](auto I) {
    constexpr int i = I + 6;
    hook(IC(i + 32));
    if constexpr (i < 32) { PACE(); ldv(IC(i)); }
    else PACEN(35 - i);
    if constexpr (i == 24) { ald16(b2v, mk_rsrc(b2, D * 2), w == 0 ? ocol * 2 : OOB, 0); ald16(xo, mk_rsrc(x, NB * D * 2), w == 0 ? ocol * 2 : OOB, b * (D * 2)); PIN(lw2); }
    if constexpr (i == 6) { PIN(V[0]); PIN(V[1]); pv_step<0>(acc, plo[0], phi[0], V[0]); pv_step<1>(acc, plo[0], phi[0], V[1]); }
    PIN(V[i - 4]);
    pv_step<(i - 4) & 15>(acc, plo[(i - 4) >> 4], phi[(i - 4) >> 4], V[i - 4]);
    pin8(acc);
    SB();
  });
  STAMP(4);
  *(LAS f32x2*)(L + L_RED2 + lw2) = fold_rows8(acc);
  BARRIER();
  PIN(b2v); PIN(xo);
  if (w == 0) {
    const rsrc_t rws = mk_rsrc(ws, WS_BYTES);
    int vin = blk * IN_BYTES + lane * 16;
    int vhs = (b * 32 + (lane >> 2)) * IN_BYTES + IN_H + h * 32 + (lane & 3) * 8;
    int vout = c < 4 ? (b * D + ocol) * 2 : OOB;
    b2v <<= 16; xo <<= 16;
    PIN(vin); PIN(vhs); PIN(vout); PIN(b2v); PIN(xo);
    if (DLY0) __builtin_amdgcn_s_sleep(DLY0);
    const float hres = rbf(asf(xo) + rbf(gather16(rws, vin + IN_P1)));
    unsigned hb[4];
    rows_bcast(fbits(hres), hb);
    const u32x2 pk = {min((hb[0] >> 16) | hb[1], SENT - 1u), min((hb[2] >> 16) | hb[3], SENT - 1u)};
    __builtin_amdgcn_raw_buffer_store_b64(pk, rws, vhs, 0, AUX_SC1);
    __builtin_amdgcn_raw_buffer_store_b64(pk, rws, vhs, 16 * IN_BYTES, AUX_SC1);
    SB();
    STAMP(7);
    BARRIER();
    BARRIER();
    if (DLY2) __builtin_amdgcn_s_sleep(DLY2);
    const float res = hres + rbf(rbf(gather16(rws, vin + IN_P2)) + asf(b2v));
    __builtin_amdgcn_raw_buffer_store_b16(__builtin_bit_cast(unsigned short, (__bf16)res), mk_rsrc(out, NB * D * 2), vout, 0, 0);
    { SB(); const unsigned long long xt1 = __builtin_amdgcn_s_memrealtime(); const unsigned long long tgt = xt1 + XMUL * (xt1 - xt0); while (__builtin_amdgcn_s_memrealtime() < tgt) {} }
  }
  }
}

extern "C" void kernel_launch(void* const* d_in, const int* in_sizes, int n_in, void* d_out, int out_size, void* d_ws,
                              size_t ws_size, hipStream_t stream) {
  fused_block<<<NB * NH, 768, 0, stream>>>((const unsigned short*)d_in[0], (const unsigned char*)d_in[1], (const unsigned char*)d_in[2],
                                           (const unsigned char*)d_in[3], (const unsigned char*)d_in[4], (const unsigned char*)d_in[6],
                                           (const unsigned char*)d_in[7], (const unsigned char*)d_in[8], (const unsigned short*)d_in[9],
                                           (unsigned short*)d_out, (unsigned char*)d_ws);
}
```

```cpp
#include <hip/hip_runtime.h>
#include <hip/hip_bf16.h>
#include <stdint.h>

#define STAMPS 0
#define PFCODE 0
#define WN 3
#define BWN 3
#define QB0 4
#define QRDY 9
#define QSTEP 28
#define OB0 19
#define MB0 38
#define INB 8192
#define SENTH 0
#define SENTP 1
#define CHKS 61
#define BEATD 8
#define EARLYST 0
#define TOUCH 0
#define SENTW 0
#define TAILST 0
#define HST 1
#define EXPM 0
#define DLY0 20
#define DLY1 11
#define DLY2 18

typedef unsigned u32x4 __attribute__((ext_vector_type(4)));
typedef unsigned u32x2 __attribute__((ext_vector_type(2)));
typedef float f32x2 __attribute__((ext_vector_type(2)));
typedef float f32x4 __attribute__((ext_vector_type(4)));
typedef __bf16 bf16x2 __attribute__((ext_vector_type(2)));
typedef short s16x4 __attribute__((ext_vector_type(4)));
typedef unsigned u32x8 __attribute__((ext_vector_type(8)));
typedef __bf16 bf16x8 __attribute__((ext_vector_type(8)));
typedef __bf16 bf16x16 __attribute__((ext_vector_type(16)));
typedef __amdgpu_buffer_rsrc_t rsrc_t;
#define LAS __attribute__((address_space(3)))
#define DI __device__ __forceinline__

constexpr int NB = 8, D = 512, NH = 32, SK = 4096, DH = 128, WIN = 1024, F = 1376;
constexpr int FS = 48;
constexpr int AUX_NT = 2, AUX_SC1 = 16;
constexpr int OOB = 0x7ffffff0;
constexpr unsigned SENT = 0xffffffffu;
constexpr unsigned SPIN_CAP = 1u << 17;
constexpr int IN_P1 = 0;
constexpr int IN_P2 = 2048;
constexpr int IN_H = 4096;
constexpr int IN_BYTES = INB;
constexpr int WS_HB = 256 * 8192;
constexpr int WS_BYTES = WS_HB + 32768;
constexpr int WS_STAMP = 4 * 1024 * 1024;
constexpr int L_RED = 0;
constexpr int L_RED2 = L_RED + 2048;
constexpr int L_EX = L_RED2 + 4096;
constexpr int L_EX2 = L_EX + 1024;
constexpr int L_ML = L_EX2 + 1024;
constexpr int L_ST = L_ML + 64;
constexpr int L_DUMP = 9216;
constexpr int L_W = L_DUMP + 1024;
constexpr int L_BYTES = L_W + 128 * 1024;

DI float dot2(unsigned a, unsigned b, float acc) {
  return __builtin_amdgcn_fdot2_f32_bf16(__builtin_bit_cast(bf16x2, a), __builtin_bit_cast(bf16x2, b), acc, false);
}
DI unsigned fbits(float f) { return __builtin_bit_cast(unsigned, f); }
DI float asf(unsigned u) { return __builtin_bit_cast(float, u); }
DI unsigned cvtpk(float lo, float hi) {
  bf16x2 r = {(__bf16)lo, (__bf16)hi};
  return __builtin_bit_cast(unsigned, r);
}
DI float rbf(float f) { return asf((unsigned)__builtin_bit_cast(unsigned short, (__bf16)f) << 16); }
template <int CTRL> DI unsigned dppu(unsigned x) { return __builtin_amdgcn_mov_dpp(x, CTRL, 0xf, 0xf, true); }
template <int CTRL> DI float dppf(float x) { return asf(dppu<CTRL>(fbits(x))); }
constexpr int XOR1 = 0xB1, XOR2 = 0x4E, HMIR = 0x141, ROR4 = 0x124, ROR8 = 0x128, BCAST = 0x150;
DI float tsum32(float a, float b) {
  auto s = __builtin_amdgcn_permlane32_swap(fbits(a), fbits(b), false, false);
  return asf(s[0]) + asf(s[1]);
}
DI float tsum16(float a, float b) {
  auto s = __builtin_amdgcn_permlane16_swap(fbits(a), fbits(b), false, false);
  return asf(s[0]) + asf(s[1]);
}
DI float row_sum(float x) { x += dppf<XOR1>(x); x += dppf<XOR2>(x); x += dppf<ROR4>(x); x += dppf<ROR8>(x); return x; }
DI float row_max(float x) { x = fmaxf(x, dppf<XOR1>(x)); x = fmaxf(x, dppf<XOR2>(x)); x = fmaxf(x, dppf<ROR4>(x)); x = fmaxf(x, dppf<ROR8>(x)); return x; }
DI float wave_sum(float x) { x = row_sum(x); x = tsum16(x, x); return tsum32(x, x); }
DI float wave_max(float x) {
  x = row_max(x);
  auto s = __builtin_amdgcn_permlane16_swap(fbits(x), fbits(x), false, false);
  x = fmaxf(asf(s[0]), asf(s[1]));
  auto t = __builtin_amdgcn_permlane32_swap(fbits(x), fbits(x), false, false);
  return fmaxf(asf(t[0]), asf(t[1]));
}
DI void rows_bcast(unsigned x, unsigned (&o)[4]) {
  auto s = __builtin_amdgcn_permlane16_swap(x, x, false, false);
  auto a = __builtin_amdgcn_permlane32_swap(s[0], s[0], false, false);
  auto b = __builtin_amdgcn_permlane32_swap(s[1], s[1], false, false);
  o[0] = a[0]; o[2] = a[1]; o[1] = b[0]; o[3] = b[1];
}
DI f32x4 smf(u32x4 a, u32x8 b, f32x4 c, int idx) {
  return __builtin_amdgcn_smfmac_f32_16x16x64_bf16(__builtin_bit_cast(bf16x8, a), __builtin_bit_cast(bf16x16, b), c, idx, 0, 0);
}
DI f32x4 mfma(u32x4 a, u32x4 b, f32x4 c) {
  return __builtin_amdgcn_mfma_f32_16x16x32_bf16(__builtin_bit_cast(bf16x8, a), __builtin_bit_cast(bf16x8, b), c, 0, 0, 0);
}
constexpr int SHL = 0x100;
template <class T> DI void pin_(T& x) { asm volatile("" : "+v"(x)); }
#define PIN(x) pin_(x)
template <class T> DI void keep_(const T& x) { asm volatile("" :: "v"(x)); }
#define KEEP(x) keep_(x)
template <class T> DI void pins_(T& x) { asm volatile("" : "+s"(x)); }
#define PINS(x) pins_(x)
#define SB() __builtin_amdgcn_sched_barrier(0)
#define PACEN(n) do { asm volatile("s_waitcnt vmcnt(%0)" :: "i"(n) : "memory"); SB(); } while (0)
#define PACE() PACEN(WN)
#define BARRIER() do { asm volatile("s_waitcnt lgkmcnt(0)\n\ts_barrier" ::: "memory"); SB(); } while (0)
DI rsrc_t mk_rsrc(const void* p, int bytes) { return __builtin_amdgcn_make_buffer_rsrc((void*)p, 0, bytes, 0x00020000); }

template <int G, int N, int CNT = N> DI void burst(int grp, rsrc_t r, int voff, int stride, int lds, int j0) {
  int keep, j, cnt, t;
  asm volatile(
      "s_cmp_lg_u32 %4, %10\n\t"
      "s_cbranch_scc1 BE%=\n\t"
      "s_mov_b32 %0, m0\n\t"
      "s_mov_b32 %1, %9\n\t"
      "s_mov_b32 %2, %12\n"
      "BL%=:\n\t"
      "s_lshl_b32 %3, %1, 10\n\t"
      "s_add_u32 m0, %3, %8\n\t"
      "s_mul_i32 %3, %1, %7\n\t"
      "s_add_u32 %1, %1, 1\n\t"
      "s_waitcnt vmcnt(%13)\n\t"
      "buffer_load_dwordx4 %6, %5, %3 offen lds\n\t"
      "s_cmp_eq_u32 %1, %11\n\t"
      "s_cselect_b32 %1, 0, %1\n\t"
      "s_sub_u32 %2, %2, 1\n\t"
      "s_cmp_lg_u32 %2, 0\n\t"
      "s_cbranch_scc1 BL%=\n\t"
      "s_mov_b32 m0, %0\n"
      "BE%=:"
      : "=&s"(keep), "=&s"(j), "=&s"(cnt), "=&s"(t)
      : "s"(grp), "s"(r), "v"(voff), "s"(stride), "s"(lds), "s"(j0), "i"(G), "i"(N), "i"(CNT), "i"(BWN)
      : "memory", "scc");
  SB();
}
DI void ald128nt(u32x4& d, rsrc_t r, int voff, int soff) { asm volatile("buffer_load_dwordx4 %0, %1, %2, %3 offen nt" : "=v"(d) : "v"(voff), "s"(r), "s"(soff) : "memory"); SB(); }
DI void ald128(u32x4& d, rsrc_t r, int voff, int soff) { asm volatile("buffer_load_dwordx4 %0, %1, %2, %3 offen" : "=v"(d) : "v"(voff), "s"(r), "s"(soff) : "memory"); SB(); }
DI void ald64sc(u32x2& d, rsrc_t r, int voff, int soff) { asm volatile("buffer_load_dwordx2 %0, %1, %2, %3 offen sc1" : "=v"(d) : "v"(voff), "s"(r), "s"(soff) : "memory"); SB(); }
DI void ald32sc(unsigned& d, rsrc_t r, int voff) { asm volatile("buffer_load_dword %0, %1, %2, 0 offen sc1" : "=v"(d) : "v"(voff), "s"(r) : "memory"); SB(); }
DI void ald32(unsigned& d, rsrc_t r, int voff) { asm volatile("buffer_load_dword %0, %1, %2, 0 offen" : "=v"(d) : "v"(voff), "s"(r) : "memory"); SB(); }
DI void ald16(unsigned& d, rsrc_t r, int voff, int soff) { asm volatile("buffer_load_ushort %0, %1, %2, %3 offen" : "=v"(d) : "v"(voff), "s"(r), "s"(soff) : "memory"); SB(); }
template <bool NT = false> DI void dma1(rsrc_t r, int voff, int soff, int lds) {
  int keep;
  if constexpr (NT)
    asm volatile("s_mov_b32 %0, m0\n\ts_mov_b32 m0, %4\n\ts_nop 0\n\tbuffer_load_dwordx4 %1, %2, %3 offen nt lds\n\ts_mov_b32 m0, %0"
                 : "=&s"(keep) : "v"(voff), "s"(r), "s"(soff), "s"(lds) : "memory");
  else
    asm volatile("s_mov_b32 %0, m0\n\ts_mov_b32 m0, %4\n\ts_nop 0\n\tbuffer_load_dwordx4 %1, %2, %3 offen lds\n\ts_mov_b32 m0, %0"
                 : "=&s"(keep) : "v"(voff), "s"(r), "s"(soff), "s"(lds) : "memory");
  SB();
}
DI unsigned atom_add(rsrc_t r, int voff, int v) { return (unsigned)__builtin_amdgcn_raw_ptr_buffer_atomic_add_i32(v, r, voff, 0, 0); }
template <int G> DI void cond_barrier(int grp) {
  asm volatile("s_cmp_lg_u32 %0, %1\n\ts_cbranch_scc1 CB%=\n\ts_barrier\nCB%=:" :: "s"(grp), "i"(G) : "memory", "scc");
  SB();
}
DI float gather16(rsrc_t r, int voff);
DI u32x2 tr_read(LAS unsigned char* p) {
  return __builtin_bit_cast(u32x2, __builtin_amdgcn_ds_read_tr16_b64_v4i16((LAS s16x4*)p));
}
template <int M1, int M2> DI u32x8 tr_read8(LAS unsigned char* p) {
  const u32x2 a = tr_read(p), b = tr_read(p + M1), c = tr_read(p + M2), d = tr_read(p + M1 + M2);
  return u32x8{a[0], a[1], b[0], b[1], c[0], c[1], d[0], d[1]};
}
DI f32x2 fold_rows8(const float (&acc)[8]) {
  const float t0 = tsum32(acc[0], acc[4]), t1 = tsum32(acc[1], acc[5]), t2 = tsum32(acc[2], acc[6]), t3 = tsum32(acc[3], acc[7]);
  return f32x2{tsum16(t0, t2), tsum16(t1, t3)};
}
DI float fold_rows4(float a0, float a1, float a2, float a3) { return tsum16(tsum32(a0, a2), tsum32(a1, a3)); }
DI unsigned max4(u32x4 v) { return max(max(v[0], v[1]), max(v[2], v[3])); }

DI float gather16(rsrc_t r, int voff) {
  u32x4 A, Bv;
  for (unsigned spins = 0; spins < SPIN_CAP; ++spins) {
    asm volatile("" ::: "memory");
    A = __builtin_amdgcn_raw_buffer_load_b128(r, voff, 0, AUX_SC1);
    Bv = __builtin_amdgcn_raw_buffer_load_b128(r, voff, 1024, AUX_SC1);
    if (__builtin_amdgcn_ballot_w64(max(max4(A), max4(Bv)) == SENT) == 0) break;
  }
  const f32x4 s = __builtin_bit_cast(f32x4, A) + __builtin_bit_cast(f32x4, Bv);
  float t = fold_rows4(s[0], s[1], s[2], s[3]);
  t += dppf<ROR4>(t); t += dppf<ROR8>(t);
  return t;
}

template <int I> DI void pv_step(float (&acc)[8], unsigned plo, unsigned phi, u32x4 v) {
  const unsigned lo = dppu<BCAST + I>(plo), hi = dppu<BCAST + I>(phi);
#pragma unroll
  for (int k = 0; k < 4; ++k) { acc[2 * k] = dot2(lo, v[k], acc[2 * k]); acc[2 * k + 1] = dot2(hi, v[k], acc[2 * k + 1]); }
}
DI void pin8(float (&a)[8]) {
  asm volatile("" : "+v"(a[0]), "+v"(a[1]), "+v"(a[2]), "+v"(a[3]), "+v"(a[4]), "+v"(a[5]), "+v"(a[6]), "+v"(a[7]));
}
template <int... Is> struct iseq {};
template <int N, int... Is> struct mkseq : mkseq<N - 1, N - 1, Is...> {};
template <int... Is> struct mkseq<0, Is...> { typedef iseq<Is...> type; };
template <class Fn, int... Is> DI void sfor_(Fn&& f, iseq<Is...>) { (f(std::integral_constant<int, Is>{}), ...); }
template <int N, class Fn> DI void sfor(Fn&& f) { sfor_(f, typename mkseq<N>::type{}); }

DI float transpose_reduce16(float (&x)[16], int c) {
#define TR_STEP(HALF, CTRL, BIT) _Pragma("unroll") for (int h_ = 0; h_ < (HALF); ++h_) { \
    const float lo_ = x[h_], hi_ = x[h_ + (HALF)]; const float t1_ = lo_ + dppf<CTRL>(lo_), t2_ = hi_ + dppf<CTRL>(hi_); \
    x[h_] = (c & (BIT)) ? t2_ : t1_; }
  TR_STEP(8, ROR8, 8) TR_STEP(4, HMIR, 4) TR_STEP(2, XOR2, 2) TR_STEP(1, XOR1, 1)
#undef TR_STEP
  return x[0];
}

#define STAMP(i) do {} while (0)
#define ESTAMP(i) do {} while (0)
#define TSTAMP(i) do {} while (0)

extern "C" __global__ void __launch_bounds__(768, 3)
fused_block(const unsigned char* __restrict__ wq, const unsigned char* __restrict__ kc, const unsigned char* __restrict__ wo,
            const unsigned char* __restrict__ vc, const unsigned short* __restrict__ x, const unsigned char* __restrict__ w1,
            const unsigned char* __restrict__ b1, const unsigned char* __restrict__ w2, const unsigned short* __restrict__ b2,
            unsigned short* __restrict__ out, unsigned char* ws) {
  __shared__ __attribute__((aligned(16))) unsigned char smem[L_BYTES];
  LAS unsigned char* L = (LAS unsigned char*)smem;
  STAMP(0);
  const int blk = blockIdx.x, b = blk >> 5, h = blk & 31;
  const int tid = threadIdx.x, lane = tid & 63, r = lane >> 4, c = lane & 15;
  const int w = __builtin_amdgcn_readfirstlane(tid >> 6);
  const int slice = (h & 7) * 4 + (h >> 3), col0 = slice * FS;
#define IC(n) std::integral_constant<int, (n)>{}
  if (w >= 8) {
  const int u = w - 8;
  const rsrc_t rws = mk_rsrc(ws, WS_BYTES);
  const int vin = blk * IN_BYTES + lane * 16;
  const u32x4 sent4 = {SENT, SENT, SENT, SENT};
  if (!(EXPM & 1)) {
    __builtin_amdgcn_raw_buffer_store_b128(sent4, rws, (u == 0 || (u == 3 ? !SENTH : !SENTP)) ? vin : OOB, 1024 + u * 1024, AUX_SC1);
    __builtin_amdgcn_raw_buffer_store_b128(sent4, rws, u == 0 ? vin : OOB, 0, AUX_SC1);
  }
  const u32x4 xrow = *(const u32x4*)(x + b * D + lane * 8);
  const float xe0 = asf((unsigned)x[b * D + 128 * u + 4 * c + r] << 16), xe1 = asf((unsigned)x[b * D + 128 * u + 64 + 4 * c + r] << 16);
  const int v_hb = (u == 0 && lane < 32 && !(EXPM & 2)) ? WS_HB + b * 256 + lane * 4 : OOB;
  const unsigned snap = atom_add(rws, v_hb, 0);
  float b1v = asf((unsigned)__builtin_amdgcn_raw_buffer_load_b16(mk_rsrc(b1, F * 2), lane < FS ? (col0 + lane) * 2 : OOB, 0, 0) << 16);
  if (PFCODE && b == 0 && u >= 2) {
    unsigned long long pc; int o0, o1;
    asm volatile("s_getpc_b64 %0\nKPC0:\n\ts_mov_b32 %1, KPC0-KPCA\n\ts_mov_b32 %2, KPCB-KPCA" : "=s"(pc), "=s"(o0), "=s"(o1));
    const rsrc_t rc = mk_rsrc((const void*)(pc - (long long)o0), o1);
    unsigned p0 = __builtin_amdgcn_raw_buffer_load_b32(rc, lane * 128, (u - 2) * 8192, 0);
    PIN(p0);
  }
  SB();
  {
    float xf[8];
#pragma unroll
    for (int k = 0; k < 4; ++k) { xf[2 * k] = asf(xrow[k] << 16); xf[2 * k + 1] = asf(xrow[k] & 0xffff0000u); }
    const float mean = wave_sum(((xf[0] + xf[1]) + (xf[2] + xf[3])) + ((xf[4] + xf[5]) + (xf[6] + xf[7]))) * (1.f / D);
    float vs = 0.f;
#pragma unroll
    for (int k = 0; k < 8; ++k) { const float d = xf[k] - mean; vs = fmaf(d, d, vs); }
    const float var = wave_sum(vs) * (1.f / D);
    const float den = rbf(sqrtf(rbf(rbf(var) + 1e-5f))), mb = rbf(mean);
    const unsigned xp = cvtpk(rbf(xe0 - mb) / den, rbf(xe1 - mb) / den);
    unsigned xlo[2] = {xp & 0xffffu, xp >> 16}, xhi[2] = {xp << 16, xp & 0xffff0000u};
    PIN(xlo[0]); PIN(xlo[1]); PIN(xhi[0]); PIN(xhi[1]); PIN(b1v);
    PACEN(0);
    ESTAMP(5);
    if (BEATD) __builtin_amdgcn_s_sleep(BEATD);
    (void)atom_add(rws, lane == h ? v_hb : OOB, 1);
    BARRIER();
    ESTAMP(6);
    float acc[8] = {0.f, 0.f, 0.f, 0.f, 0.f, 0.f, 0.f, 0.f};
    sfor<32>([&](auto I) { pv_step<I & 15>(acc, xlo[I >> 4], xhi[I >> 4], *(const LAS u32x4*)(L + L_W + u * 32768 + I * 1024 + lane * 16)); });
    *(LAS f32x2*)(L + L_RED + (u >> 1) * 1024 + lane * 16 + (u & 1) * 8) = fold_rows8(acc);
  }
  STAMP(11);
  BARRIER();
  BARRIER();
  u32x8 WO[16];
  const int q4 = (lane >> 2) & 3;
  LAS unsigned char* lw = L + L_W + u * 32768 + (lane & 3) * 8;
  {
    LAS unsigned char* p = lw + (32 * (r >> 1) + 4 * (r & 1) + (q4 >> 1) + 8 * (q4 & 1)) * 128;
    sfor<16>([&](auto J) { WO[J] = tr_read8<2 * 128, 64 * 128>(p + (J >> 3) * 16384 + ((J >> 1) & 3) * 32 + (J & 1) * 16 * 128); });
    sfor<16>([&](auto J) { PIN(WO[J]); });
  }
  BARRIER();
  BARRIER();
  if (u == 0 && !(EXPM & 2)) {
    for (unsigned spins = 0; spins < SPIN_CAP; ++spins) {
      const unsigned cur = atom_add(rws, v_hb, 0);
      if (__builtin_amdgcn_ballot_w64(lane < 32 && cur == snap) == 0) break;
    }
  }
  if (SENTH) { __builtin_amdgcn_raw_buffer_store_b128(sent4, rws, u == 3 ? vin : OOB, IN_H, AUX_SC1); PACEN(0); }
  int vp = lane < 16 ? (b * 32 + 8 * u + (lane >> 2)) * IN_BYTES + (lane & 3) * 16 : OOB;
  int lrd = lane * 16, v_beat = lane == h ? v_hb : OOB;
  int idx = (lane & 1) ? 0xEEEEEEEE : 0x44444444;
  PIN(vp); PIN(lrd); PIN(idx); PIN(v_beat);
  const f32x4 zero4 = {0.f, 0.f, 0.f, 0.f};
  f32x4 d[8];
  d[0] = zero4; d[1] = zero4; PIN(d[0]); PIN(d[1]);
  int so1 = IN_P1 + h * 64, so2 = IN_P1 + h * 64 + 4 * IN_BYTES;
  PINS(so1); PINS(so2);
  BARRIER();
  TSTAMP(2);
  {
    f32x4 s4 = *(const LAS f32x4*)(L + L_RED2 + lrd) + *(const LAS f32x4*)(L + L_RED2 + 1024 + lrd);
    s4 += *(const LAS f32x4*)(L + L_RED2 + 2048 + lrd) + *(const LAS f32x4*)(L + L_RED2 + 3072 + lrd);
    const unsigned op = cvtpk(s4[0] + s4[2], s4[1] + s4[3]);
    const u32x4 a0 = {op, dppu<SHL + 2>(op), dppu<SHL + 4>(op), dppu<SHL + 6>(op)};
    const u32x4 a1 = {dppu<SHL + 8>(op), dppu<SHL + 10>(op), dppu<SHL + 12>(op), dppu<SHL + 14>(op)};
    float y[8];
    auto fin = [&](auto T) { y[T] = d[T][0] + d[T][1]; PIN(y[T]); KEEP(d[T]); };
    sfor<8>([&](auto T) {
      d[T] = smf(a0, WO[2 * T], d[T], idx);
      if constexpr (T > 0) SB();
      if constexpr (T + 2 < 8 && T > 0) { d[T + 2] = zero4; PIN(d[T + 2]); }
      SB();
      d[T] = smf(a1, WO[2 * T + 1], d[T], idx);
      SB();
      if constexpr (T == 0) { d[2] = zero4; PIN(d[2]); }
      if constexpr (T >= 2) fin(IC(T - 2));
      if constexpr (T == 6)
        __builtin_amdgcn_raw_buffer_store_b128(u32x4{fbits(y[0]), fbits(y[1]), fbits(y[2]), fbits(y[3])}, rws, (EXPM & 8) ? OOB : vp, so1, AUX_SC1);
      SB();
    });
    fin(IC(6)); fin(IC(7));
    __builtin_amdgcn_raw_buffer_store_b128(u32x4{fbits(y[4]), fbits(y[5]), fbits(y[6]), fbits(y[7])}, rws, (EXPM & 8) ? OOB : vp, so2, AUX_SC1);
  }
  SB();
  STAMP(5); TSTAMP(3);
  (void)atom_add(rws, v_beat, 1);
  u32x8 W2[8];
  {
    const int ln = __builtin_amdgcn_mbcnt_hi(~0u, __builtin_amdgcn_mbcnt_lo(~0u, 0u)), rr = ln >> 4, qq = (ln >> 2) & 3;
    LAS unsigned char* p2 = L + L_W + u * 32768 + (ln & 3) * 8 + 8192 + (16 * (rr >> 1) + 8 * (rr & 1) + (qq >> 1) + 2 * (qq & 1)) * 128;
    LAS unsigned char* p3 = p2 + (32 - 16 * (rr >> 1)) * 128;
    sfor<8>([&](auto J) {
      constexpr int o = (J >> 2) * 16384 + (J & 3) * 32;
      const u32x2 t0 = tr_read(p2 + o), t1 = tr_read(p2 + o + 4 * 128), t2 = tr_read(p3 + o), t3 = tr_read(p3 + o + 4 * 128);
      W2[J] = u32x8{t0[0], t0[1], t1[0], t1[1], t2[0], t2[1], t3[0], t3[1]};
    });
    sfor<8>([&](auto J) { PIN(W2[J]); d[J] = zero4; PIN(d[J]); });
  }
  so1 = IN_P2 + slice * 64; so2 = IN_P2 + slice * 64 + 4 * IN_BYTES;
  int lst = L_ST;
  float gk = -2.f * 1.4426950408889634f * 0.7978845608028654f;
  unsigned gmask = lrd < FS * 16 ? ~0u : 0u;
  PINS(so1); PINS(so2); PIN(lst); PIN(gk); PIN(gmask);
  SB();
  STAMP(6);
  BARRIER();
  float c1;
  { const f32x4 t = *(const LAS f32x4*)(L + L_EX + lrd); c1 = (t[0] + t[1]) + (t[2] + t[3]); }
  PIN(c1);
  BARRIER();
  if (TAILST == 2) TSTAMP(8);
  {
    const f32x4 t4 = *(const LAS f32x4*)(L + L_EX2 + lrd);
    const f32x2 ms = *(const LAS f32x2*)(L + lst);
    const float tot = (t4[0] + t4[1]) + (t4[2] + t4[3]);
    const float uu = rbf(rbf(fmaf(-ms[0], c1, tot) * ms[1]) + b1v);
    const float z = uu * fmaf(uu * uu, -2.f * 1.4426950408889634f * 0.044715f * 0.7978845608028654f, gk);
    const float g = asf(fbits(uu) & gmask) * __builtin_amdgcn_rcpf(1.f + __builtin_amdgcn_exp2f(z));
    const unsigned gp = cvtpk(g, dppf<XOR2>(g));
    const u32x4 ag = {gp, dppu<SHL + 4>(gp), dppu<SHL + 8>(gp), dppu<SHL + 12>(gp)};
    if (TAILST == 2) { KEEP(ag); TSTAMP(9); }
    float y[8];
    auto fin = [&](auto T) { y[T] = d[T][0] + d[T][1]; PIN(y[T]); KEEP(d[T]); SB(); };
    sfor<8>([&](auto T) { d[T] = smf(ag, W2[T], d[T], idx); SB(); });
    fin(IC(0)); fin(IC(1)); fin(IC(2)); fin(IC(3));
    __builtin_amdgcn_raw_buffer_store_b128(u32x4{fbits(y[0]), fbits(y[1]), fbits(y[2]), fbits(y[3])}, rws, vp, so1, AUX_SC1);
    SB();
    fin(IC(4)); fin(IC(5)); fin(IC(6)); fin(IC(7));
    KEEP(y[0]); KEEP(y[1]); KEEP(y[2]); KEEP(y[3]);
    __builtin_amdgcn_raw_buffer_store_b128(u32x4{fbits(y[4]), fbits(y[5]), fbits(y[6]), fbits(y[7])}, rws, vp, so2, AUX_SC1);
  }
  asm volatile("KPCB:");
  SB();
  STAMP(9); TSTAMP(10);
  } else {
  const int grp = (b + 2 * (h >> 3)) & 7;
  const size_t kvoff = ((size_t)blk * SK + (SK - WIN)) * (DH * 2);
  const rsrc_t rk = mk_rsrc(kc + kvoff, WIN * DH * 2), rv = mk_rsrc(vc + kvoff, WIN * DH * 2);
  const int vkv = tid * 16;
  const int lds0 = (int)(unsigned)(size_t)L, ldsw = lds0 + L_W + w * 16384, ldsd = lds0 + L_DUMP;

  u32x4 K[32], V[32];
  auto ldk = [&](auto I) { ald128nt(K[I], rk, vkv, I * 8192); };
  auto ldv = [&](auto I) { ald128nt(V[I], rv, vkv, I * 8192); };
  ldk(IC(0)); ldk(IC(1));
  asm volatile("KPCA:");
  const rsrc_t rq = mk_rsrc(wq + h * (DH * 2), D * NH * DH * 2 - h * (DH * 2));
  const int vq = (64 * w + r) * (NH * DH * 2) + c * 16;
  dma1(rq, vq, 2 * b * (4 * NH * DH * 2), ldsw + 2 * b * 1024);
  dma1(rq, vq, (2 * b + 1) * (4 * NH * DH * 2), ldsw + (2 * b + 1) * 1024);
  unsigned tch = 0;
  if (TOUCH) {
    const unsigned char* p = w == 0 ? vc + kvoff : w == 1 ? wo + (size_t)h * (DH * D * 2) : w == 2 ? w1 + 256 * F * 2 : w == 3 ? w2 + (size_t)min(col0, F - 1) * (D * 2)
                           : w == 4 ? ws + blk * IN_BYTES + 7168 : w == 5 ? (const unsigned char*)out : w == 6 ? (const unsigned char*)b2 : b1;
    ald32(tch, mk_rsrc(p, 4), lane == 0 ? 0 : OOB);
  }
  const rsrc_t ro = mk_rsrc(wo + (size_t)h * (DH * D * 2), DH * D * 2);
  const int vo = (lane >> 3) * (D * 2) + w * 128 + (lane & 7) * 16;
  const rsrc_t r1 = mk_rsrc(w1, D * F * 2);
  const int v1 = ((lane & 7) < 6 && col0 + (lane & 7) * 8 < F) ? (64 * w + (lane >> 3)) * (F * 2) + col0 * 2 + (lane & 7) * 16 : OOB;
  const int c2 = min(col0, F);
  const rsrc_t r2 = mk_rsrc(w2 + (size_t)c2 * (D * 2), (F - c2) * (D * 2));
  const int b6 = b < 6 ? b : b - 6;
  auto hook = [&](auto I) {
    constexpr int i = I;
    constexpr auto site = [](int i_, int p0) { return (i_ >= p0 && ((i_ - p0) & 1) == 0 && i_ - p0 < 16) ? (i_ - p0) / 2 : -1; };
    if constexpr (site(i, QB0 + 4) >= 0) cond_barrier<site(i, QB0 + 4)>(grp);
    if constexpr (site(i, QB0 + QRDY) >= 0) cond_barrier<site(i, QB0 + QRDY)>(grp);
    if constexpr (site(i, OB0 + 4) >= 0) cond_barrier<site(i, OB0 + 4)>(grp);
    if constexpr (i == CHKS) BARRIER();
    if constexpr (site(i, QB0) >= 0) burst<site(i, QB0), 16, 14>(grp, rq, vq, 4 * NH * DH * 2, ldsw, (2 * b + 2) & 15);
    if constexpr (i == OB0 - 5) { PACE(); dma1(ro, vo, 2 * b * 8192, ldsd); PACE(); dma1(ro, vo, (2 * b + 1) * 8192, ldsd); }
    if constexpr (site(i, OB0) >= 0) burst<site(i, OB0), 16>(grp, ro, vo, 8 * D * 2, ldsw, 2 * b);
    if constexpr (i == MB0 - 5) { PACE(); dma1(r1, v1, b * (8 * F * 2), ldsd); }
    if constexpr (i == MB0 - 4) { PACE(); dma1(r2, vo, b * (8 * D * 2), ldsd); }
    if constexpr (site(i, MB0) >= 0) {
      burst<site(i, MB0), 8>(grp, r1, v1, 8 * F * 2, ldsw, b);
      burst<site(i, MB0), 6>(grp, r2, vo, 8 * D * 2, ldsw + 8192, b6);
    }
  };
  static_assert(QRDY > 4 && QB0 + 14 + QRDY < QSTEP && OB0 >= QB0 + QRDY && OB0 + 14 + 4 <= 37 && MB0 >= 38 && MB0 + 14 <= 63 && CHKS >= 38, "barrier order, staging area reuse");
  constexpr int S0 = 2;
  sfor<QSTEP - S0>([&](auto I) { constexpr int i = I + S0; if constexpr (i == 11) ESTAMP(7); if constexpr (i == 2) ESTAMP(8); if constexpr (i == 3) ESTAMP(9); hook(IC(i)); PACEN(WN + (TOUCH && i < S0 + 4 ? 1 : 0)); ldk(IC(i)); });
  PIN(tch);
  STAMP(1);
  unsigned qp[4];
  {
    const f32x4 s4 = *(const LAS f32x4*)(L + L_RED + lane * 16) + *(const LAS f32x4*)(L + L_RED + 1024 + lane * 16);
    rows_bcast(cvtpk(s4[0] + s4[2], s4[1] + s4[3]), qp);
  }
  SB();
  STAMP(2);
  float sc[2], xs[16];
  auto qk = [&](u32x4& kv) { PIN(kv); return dot2(kv[3], qp[3], dot2(kv[2], qp[2], dot2(kv[1], qp[1], dot2(kv[0], qp[0], 0.f)))); };
  static_assert(QSTEP <= 28, "");
  sfor<36 - QSTEP>([&](auto I) {
    constexpr int i = I + QSTEP;
    hook(IC(i));
    PACE();
    if constexpr (i < 32) ldk(IC(i)); else ldv(IC(i - 32));
    if constexpr (I < 3) sfor<(I < 2 ? 6 : 4)>([&](auto J) { xs[6 * I + J] = qk(K[6 * I + J]); PIN(xs[6 * I + J]); });
    if constexpr (I == 3) { sc[0] = transpose_reduce16(xs, c); PIN(sc[0]); }
    if constexpr (I >= 4) {
      constexpr int lo = (I == 4) ? 16 : (16 + 6 * (I - 4) < i - 4 ? 16 + 6 * (I - 4) : i - 4);
      constexpr int hi = (16 + 6 * (I - 3) < i - 3) ? 16 + 6 * (I - 3) : i - 3;
      sfor<hi - lo>([&](auto J) { xs[lo - 16 + J] = qk(K[lo + J]); PIN(xs[lo - 16 + J]); });
    }
    SB();
  });
  sc[1] = transpose_reduce16(xs, c);
  SB();
  hook(IC(36)); PACE(); ldv(IC(4));
  unsigned pp[2];
  {
    const float s0 = rbf(rbf(sc[0]) * 0.08838834764831845f), s1 = rbf(rbf(sc[1]) * 0.08838834764831845f);
    const float mw = wave_max(fmaxf(s0, s1));
    const float e0 = __builtin_amdgcn_exp2f((s0 - mw) * 1.4426950408889634f), e1 = __builtin_amdgcn_exp2f((s1 - mw) * 1.4426950408889634f);
    const float lw = wave_sum(e0 + e1);
    *(LAS f32x2*)(L + L_ML + w * 8) = f32x2{mw, lw};
    SB();
    hook(IC(37)); PACE(); ldv(IC(5));
    BARRIER();
    const f32x2 ml = *(const LAS f32x2*)(L + L_ML + (lane & 7) * 8);
    float M = ml[0];
    M = fmaxf(M, dppf<XOR1>(M)); M = fmaxf(M, dppf<XOR2>(M)); M = fmaxf(M, dppf<HMIR>(M));
    float Ls = ml[1] * __builtin_amdgcn_exp2f((ml[0] - M) * 1.4426950408889634f);
    Ls += dppf<XOR1>(Ls); Ls += dppf<XOR2>(Ls); Ls += dppf<HMIR>(Ls);
    const float scale = __builtin_amdgcn_exp2f((mw - M) * 1.4426950408889634f) / Ls;
    pp[0] = cvtpk(e0 * scale, e0 * scale); pp[1] = cvtpk(e1 * scale, e1 * scale);
  }
  SB();
  STAMP(3);
  f32x4 acc = {0.f, 0.f, 0.f, 0.f};
  u32x4 sel;
#pragma unroll
  for (int k = 0; k < 4; ++k) sel[k] = ((c & 7) >> 1) == k ? ((c & 1) ? 0xffff0000u : 0xffffu) : 0u;
  auto pv = [&](auto I) { PIN(V[I]); const unsigned pb = dppu<BCAST + (I & 15)>(pp[I >> 4]); acc = mfma(V[I], sel & pb, acc); PIN(acc); };
  const int ocol = 64 * (h >> 2) + 16 * r + 4 * (h & 3) + (c & 3);
  unsigned b2v, xo;
  int lw2 = (w >> 1) * 1024 + lane * 16 + (w & 1) * 8;
  u32x4 blast;
  sfor<29>([&](auto I) {
    constexpr int i = I + 6;
    hook(IC(i + 32));
    if constexpr (i < 32) { PACE(); ldv(IC(i)); }
    else PACEN(35 - i);
    if constexpr (i == 24) { ald16(b2v, mk_rsrc(b2, D * 2), w == 0 ? ocol * 2 : OOB, 0); ald16(xo, mk_rsrc(x, NB * D * 2), w == 0 ? ocol * 2 : OOB, b * (D * 2)); PIN(lw2); }
    if constexpr (i == 6) { pv(IC(0)); pv(IC(1)); }
    pv(IC(i - 4));
    SB();
  });
  blast = sel & dppu<BCAST + 15>(pp[1]);
  PIN(blast);
  PACEN(0);
  STAMP(4); TSTAMP(1);
  PIN(V[31]);
  acc = mfma(V[31], blast, acc);
  *(LAS f32x2*)(L + L_RED2 + lw2) = (c & 8) ? f32x2{acc[2], acc[3]} : f32x2{acc[0], acc[1]};
  BARRIER();
  PIN(b2v); PIN(xo);
  if (w == 0) {
    const rsrc_t rws = mk_rsrc(ws, WS_BYTES);
    int vin = blk * IN_BYTES + lane * 16;
    if (SENTP) {
      const u32x4 sent4 = {SENT, SENT, SENT, SENT};
      __builtin_amdgcn_raw_buffer_store_b128(sent4, rws, vin, IN_P2, AUX_SC1);
      __builtin_amdgcn_raw_buffer_store_b128(sent4, rws, vin, IN_P2 + 1024, AUX_SC1);
    }
    int vhs = HST == 0 ? (b * 32 + c) * IN_BYTES + IN_H + h * 32 + r * 8 : (b * 32 + (lane >> 1)) * IN_BYTES + IN_H + h * 32 + (lane & 1) * 16;
    int vout = c < 4 ? (b * D + ocol) * 2 : OOB;
    b2v <<= 16; xo <<= 16;
    PIN(vin); PIN(vhs); PIN(vout); PIN(b2v); PIN(xo);
    if (DLY0) __builtin_amdgcn_s_sleep(DLY0);
    PACEN(0);
    const float hf = asf(xo) + rbf(gather16(rws, vin + IN_P1));
    const unsigned hq = min(cvtpk(hf, dppf<XOR1>(hf)), SENT - 1u);
    unsigned hb[4];
    rows_bcast(dppu<0x88>(hq), hb);
    __builtin_amdgcn_raw_buffer_store_b128(u32x4{hb[0], hb[1], hb[2], hb[3]}, rws, vhs, 0, AUX_SC1);
    SB();
    STAMP(7); TSTAMP(5);
    BARRIER();
    float hres = asf(hq << 16);
    PIN(hres);
    if (DLY1) __builtin_amdgcn_s_sleep(DLY1);
    u32x4 hr;
    for (unsigned spins = 0; spins < SPIN_CAP; ++spins) {
      asm volatile("" ::: "memory");
      hr = __builtin_amdgcn_raw_buffer_load_b128(rws, vin, IN_H, AUX_SC1);
      if (__builtin_amdgcn_ballot_w64(max4(hr) == SENT) == 0) break;
    }
    TSTAMP(7);
    {
      const float s1 = dot2(hr[3], 0x3f803f80u, dot2(hr[2], 0x3f803f80u, dot2(hr[1], 0x3f803f80u, dot2(hr[0], 0x3f803f80u, 0.f))));
      const float s2 = dot2(hr[3], hr[3], dot2(hr[2], hr[2], dot2(hr[1], hr[1], dot2(hr[0], hr[0], 0.f))));
      float t = tsum32(s1, s2);
      t = row_sum(tsum16(t, t));
      const float mean = asf(__builtin_amdgcn_readlane(fbits(t), 0)) * (1.f / D);
      const float var = fmaxf(fmaf(-mean, mean, asf(__builtin_amdgcn_readlane(fbits(t), 32)) * (1.f / D)), 0.f);
      const float den = rbf(__builtin_amdgcn_sqrtf(rbf(rbf(var) + 1e-5f)));
      *(LAS f32x2*)(L + L_ST) = f32x2{rbf(mean), __builtin_amdgcn_rcpf(den)};
    }
    if (TAILST == 1) TSTAMP(9);
    BARRIER();
    if (DLY2) __builtin_amdgcn_s_sleep(DLY2);
    const float res = hres + rbf(rbf(gather16(rws, vin + IN_P2)) + asf(b2v));
    __builtin_amdgcn_raw_buffer_store_b16(__builtin_bit_cast(unsigned short, (__bf16)res), mk_rsrc(out, NB * D * 2), vout, 0, 0);
    STAMP(10); TSTAMP(11);
  } else if (w >= 4) {
    const int u = w - 4, q4 = (lane >> 2) & 3;
    const rsrc_t rws = mk_rsrc(ws, WS_BYTES);
    __builtin_amdgcn_s_sleep(3);
    u32x8 W1[6];
    LAS unsigned char* p1 = L + L_W + u * 32768 + (lane & 3) * 8 + (4 * (r >> 1) + 32 * (r & 1) + q4) * 128;
    sfor<6>([&](auto J) { W1[J] = tr_read8<16 * 128, 8 * 128>(p1 + (J & 1) * 16384 + (J >> 1) * 32); });
    sfor<6>([&](auto J) { PIN(W1[J]); });
    __builtin_amdgcn_s_sleep(6);
    int vhl = blk * IN_BYTES + IN_H + u * 256 + r * 32 + (lane & 1) * 16, lwr = lane * 16 + u * 4;
    int idx = (lane & 1) ? 0xEEEEEEEE : 0x44444444;
    const f32x4 zero4 = {0.f, 0.f, 0.f, 0.f};
    f32x4 e[3];
    auto project = [&](u32x4 h0, u32x4 h1, int dst) {
      sfor<3>([&](auto T) { e[T] = smf(h0, W1[2 * T], e[T], idx); SB(); e[T] = smf(h1, W1[2 * T + 1], e[T], idx); SB(); });
      float a0 = e[0][0] + e[0][1], a1 = e[1][0] + e[1][1];
      a0 = r == 0 ? a0 : a1;
      PIN(a0);
      const float a2 = e[2][0] + e[2][1];
      KEEP(e[0]); KEEP(e[1]); KEEP(e[2]);
      *(LAS float*)(L + dst + lwr) = r >= 2 ? a2 : a0;
    };
    const u32x4 ones = {0x3f803f80u, 0x3f803f80u, 0x3f803f80u, 0x3f803f80u};
    sfor<3>([&](auto T) { e[T] = zero4; });
    project(ones, ones, L_EX);
    sfor<3>([&](auto T) { e[T] = zero4; PIN(e[T]); });
    PIN(vhl); PIN(lwr); PIN(idx);
    STAMP(6);
    BARRIER();
    if (DLY1) __builtin_amdgcn_s_sleep(DLY1);
    u32x4 h0, h1;
    for (unsigned spins = 0; spins < SPIN_CAP; ++spins) {
      asm volatile("" ::: "memory");
      h0 = __builtin_amdgcn_raw_buffer_load_b128(rws, vhl, 0, AUX_SC1);
      h1 = __builtin_amdgcn_raw_buffer_load_b128(rws, vhl, 128, AUX_SC1);
      if (__builtin_amdgcn_ballot_w64(max(max4(h0), max4(h1)) == SENT) == 0) break;
    }
    SB();
    STAMP(8); TSTAMP(6);
    project(h0, h1, L_EX2);
    if (TAILST == 1) TSTAMP(8);
    BARRIER();
  }
  }
}

extern "C" void kernel_launch(void* const* d_in, const int* in_sizes, int n_in, void* d_out, int out_size, void* d_ws,
                              size_t ws_size, hipStream_t stream) {
  fused_block<<<NB * NH, 768, 0, stream>>>((const unsigned char*)d_in[3], (const unsigned char*)d_in[1], (const unsigned char*)d_in[4],
                                      (const unsigned char*)d_in[2], (const unsigned short*)d_in[0], (const unsigned char*)d_in[6],
                                      (const unsigned char*)d_in[7], (const unsigned char*)d_in[8], (const unsigned short*)d_in[9],
                                      (unsigned short*)d_out, (unsigned char*)d_ws);
}
```

```cpp
#define XA 3
#define XB 4
#define XMUL -10
#define XBASE 4000
#include <hip/hip_runtime.h>
#include <hip/hip_bf16.h>
#include <stdint.h>

#define STAMPS 0
#define PFCODE 0
#define WN 3
#define BWN 3
#define QB0 4
#define QRDY 9
#define QSTEP 28
#define OB0 19
#define MB0 38
#define INB 8192
#define SENTH 0
#define SENTP 1
#define CHKS 61
#define BEATD 8
#define EARLYST 0
#define TOUCH 0
#define SENTW 0
#define TAILST 0
#define HST 1
#define EXPM 0
#define DLY0 20
#define DLY1 11
#define DLY2 18

typedef unsigned u32x4 __attribute__((ext_vector_type(4)));
typedef unsigned u32x2 __attribute__((ext_vector_type(2)));
typedef float f32x2 __attribute__((ext_vector_type(2)));
typedef float f32x4 __attribute__((ext_vector_type(4)));
typedef __bf16 bf16x2 __attribute__((ext_vector_type(2)));
typedef short s16x4 __attribute__((ext_vector_type(4)));
typedef unsigned u32x8 __attribute__((ext_vector_type(8)));
typedef __bf16 bf16x8 __attribute__((ext_vector_type(8)));
typedef __bf16 bf16x16 __attribute__((ext_vector_type(16)));
typedef __amdgpu_buffer_rsrc_t rsrc_t;
#define LAS __attribute__((address_space(3)))
#define DI __device__ __forceinline__

constexpr int NB = 8, D = 512, NH = 32, SK = 4096, DH = 128, WIN = 1024, F = 1376;
constexpr int FS = 48;
constexpr int AUX_NT = 2, AUX_SC1 = 16;
constexpr int OOB = 0x7ffffff0;
constexpr unsigned SENT = 0xffffffffu;
constexpr unsigned SPIN_CAP = 1u << 17;
constexpr int IN_P1 = 0;
constexpr int IN_P2 = 2048;
constexpr int IN_H = 4096;
constexpr int IN_BYTES = INB;
constexpr int WS_HB = 256 * 8192;
constexpr int WS_BYTES = WS_HB + 32768;
constexpr int WS_STAMP = 4 * 1024 * 1024;
constexpr int L_RED = 0;
constexpr int L_RED2 = L_RED + 2048;
constexpr int L_EX = L_RED2 + 4096;
constexpr int L_EX2 = L_EX + 1024;
constexpr int L_ML = L_EX2 + 1024;
constexpr int L_ST = L_ML + 64;
constexpr int L_DUMP = 9216;
constexpr int L_W = L_DUMP + 1024;
constexpr int L_BYTES = L_W + 128 * 1024;

DI float dot2(unsigned a, unsigned b, float acc) {
  return __builtin_amdgcn_fdot2_f32_bf16(__builtin_bit_cast(bf16x2, a), __builtin_bit_cast(bf16x2, b), acc, false);
}
DI unsigned fbits(float f) { return __builtin_bit_cast(unsigned, f); }
DI float asf(unsigned u) { return __builtin_bit_cast(float, u); }
DI unsigned cvtpk(float lo, float hi) {
  bf16x2 r = {(__bf16)lo, (__bf16)hi};
  return __builtin_bit_cast(unsigned, r);
}
DI float rbf(float f) { return asf((unsigned)__builtin_bit_cast(unsigned short, (__bf16)f) << 16); }
template <int CTRL> DI unsigned dppu(unsigned x) { return __builtin_amdgcn_mov_dpp(x, CTRL, 0xf, 0xf, true); }
template <int CTRL> DI float dppf(float x) { return asf(dppu<CTRL>(fbits(x))); }
constexpr int XOR1 = 0xB1, XOR2 = 0x4E, HMIR = 0x141, ROR4 = 0x124, ROR8 = 0x128, BCAST = 0x150;
DI float tsum32(float a, float b) {
  auto s = __builtin_amdgcn_permlane32_swap(fbits(a), fbits(b), false, false);
  return asf(s[0]) + asf(s[1]);
}
DI float tsum16(float a, float b) {
  auto s = __builtin_amdgcn_permlane16_swap(fbits(a), fbits(b), false, false);
  return asf(s[0]) + asf(s[1]);
}
DI float row_sum(float x) { x += dppf<XOR1>(x); x += dppf<XOR2>(x); x += dppf<ROR4>(x); x += dppf<ROR8>(x); return x; }
DI float row_max(float x) { x = fmaxf(x, dppf<XOR1>(x)); x = fmaxf(x, dppf<XOR2>(x)); x = fmaxf(x, dppf<ROR4>(x)); x = fmaxf(x, dppf<ROR8>(x)); return x; }
DI float wave_sum(float x) { x = row_sum(x); x = tsum16(x, x); return tsum32(x, x); }
DI float wave_max(float x) {
  x = row_max(x);
  auto s = __builtin_amdgcn_permlane16_swap(fbits(x), fbits(x), false, false);
  x = fmaxf(asf(s[0]), asf(s[1]));
  auto t = __builtin_amdgcn_permlane32_swap(fbits(x), fbits(x), false, false);
  return fmaxf(asf(t[0]), asf(t[1]));
}
DI void rows_bcast(unsigned x, unsigned (&o)[4]) {
  auto s = __builtin_amdgcn_permlane16_swap(x, x, false, false);
  auto a = __builtin_amdgcn_permlane32_swap(s[0], s[0], false, false);
  auto b = __builtin_amdgcn_permlane32_swap(s[1], s[1], false, false);
  o[0] = a[0]; o[2] = a[1]; o[1] = b[0]; o[3] = b[1];
}
DI f32x4 smf(u32x4 a, u32x8 b, f32x4 c, int idx) {
  return __builtin_amdgcn_smfmac_f32_16x16x64_bf16(__builtin_bit_cast(bf16x8, a), __builtin_bit_cast(bf16x16, b), c, idx, 0, 0);
}
DI f32x4 mfma(u32x4 a, u32x4 b, f32x4 c) {
  return __builtin_amdgcn_mfma_f32_16x16x32_bf16(__builtin_bit_cast(bf16x8, a), __builtin_bit_cast(bf16x8, b), c, 0, 0, 0);
}
constexpr int SHL = 0x100;
template <class T> DI void pin_(T& x) { asm volatile("" : "+v"(x)); }
#define PIN(x) pin_(x)
template <class T> DI void keep_(const T& x) { asm volatile("" :: "v"(x)); }
#define KEEP(x) keep_(x)
template <class T> DI void pins_(T& x) { asm volatile("" : "+s"(x)); }
#define PINS(x) pins_(x)
#define SB() __builtin_amdgcn_sched_barrier(0)
#define PACEN(n) do { asm volatile("s_waitcnt vmcnt(%0)" :: "i"(n) : "memory"); SB(); } while (0)
#define PACE() PACEN(WN)
#define BARRIER() do { asm volatile("s_waitcnt lgkmcnt(0)\n\ts_barrier" ::: "memory"); SB(); } while (0)
DI rsrc_t mk_rsrc(const void* p, int bytes) { return __builtin_amdgcn_make_buffer_rsrc((void*)p, 0, bytes, 0x00020000); }

template <int G, int N, int CNT = N> DI void burst(int grp, rsrc_t r, int voff, int stride, int lds, int j0) {
  int keep, j, cnt, t;
  asm volatile(
      "s_cmp_lg_u32 %4, %10\n\t"
      "s_cbranch_scc1 BE%=\n\t"
      "s_mov_b32 %0, m0\n\t"
      "s_mov_b32 %1, %9\n\t"
      "s_mov_b32 %2, %12\n"
      "BL%=:\n\t"
      "s_lshl_b32 %3, %1, 10\n\t"
      "s_add_u32 m0, %3, %8\n\t"
      "s_mul_i32 %3, %1, %7\n\t"
      "s_add_u32 %1, %1, 1\n\t"
      "s_waitcnt vmcnt(%13)\n\t"
      "buffer_load_dwordx4 %6, %5, %3 offen lds\n\t"
      "s_cmp_eq_u32 %1, %11\n\t"
      "s_cselect_b32 %1, 0, %1\n\t"
      "s_sub_u32 %2, %2, 1\n\t"
      "s_cmp_lg_u32 %2, 0\n\t"
      "s_cbranch_scc1 BL%=\n\t"
      "s_mov_b32 m0, %0\n"
      "BE%=:"
      : "=&s"(keep), "=&s"(j), "=&s"(cnt), "=&s"(t)
      : "s"(grp), "s"(r), "v"(voff), "s"(stride), "s"(lds), "s"(j0), "i"(G), "i"(N), "i"(CNT), "i"(BWN)
      : "memory", "scc");
  SB();
}
DI void ald128nt(u32x4& d, rsrc_t r, int voff, int soff) { asm volatile("buffer_load_dwordx4 %0, %1, %2, %3 offen nt" : "=v"(d) : "v"(voff), "s"(r), "s"(soff) : "memory"); SB(); }
DI void ald128(u32x4& d, rsrc_t r, int voff, int soff) { asm volatile("buffer_load_dwordx4 %0, %1, %2, %3 offen" : "=v"(d) : "v"(voff), "s"(r), "s"(soff) : "memory"); SB(); }
DI void ald64sc(u32x2& d, rsrc_t r, int voff, int soff) { asm volatile("buffer_load_dwordx2 %0, %1, %2, %3 offen sc1" : "=v"(d) : "v"(voff), "s"(r), "s"(soff) : "memory"); SB(); }
DI void ald32sc(unsigned& d, rsrc_t r, int voff) { asm volatile("buffer_load_dword %0, %1, %2, 0 offen sc1" : "=v"(d) : "v"(voff), "s"(r) : "memory"); SB(); }
DI void ald32(unsigned& d, rsrc_t r, int voff) { asm volatile("buffer_load_dword %0, %1, %2, 0 offen" : "=v"(d) : "v"(voff), "s"(r) : "memory"); SB(); }
DI void ald16(unsigned& d, rsrc_t r, int voff, int soff) { asm volatile("buffer_load_ushort %0, %1, %2, %3 offen" : "=v"(d) : "v"(voff), "s"(r), "s"(soff) : "memory"); SB(); }
template <bool NT = false> DI void dma1(rsrc_t r, int voff, int soff, int lds) {
  int keep;
  if constexpr (NT)
    asm volatile("s_mov_b32 %0, m0\n\ts_mov_b32 m0, %4\n\ts_nop 0\n\tbuffer_load_dwordx4 %1, %2, %3 offen nt lds\n\ts_mov_b32 m0, %0"
                 : "=&s"(keep) : "v"(voff), "s"(r), "s"(soff), "s"(lds) : "memory");
  else
    asm volatile("s_mov_b32 %0, m0\n\ts_mov_b32 m0, %4\n\ts_nop 0\n\tbuffer_load_dwordx4 %1, %2, %3 offen lds\n\ts_mov_b32 m0, %0"
                 : "=&s"(keep) : "v"(voff), "s"(r), "s"(soff), "s"(lds) : "memory");
  SB();
}
DI unsigned atom_add(rsrc_t r, int voff, int v) { return (unsigned)__builtin_amdgcn_raw_ptr_buffer_atomic_add_i32(v, r, voff, 0, 0); }
template <int G> DI void cond_barrier(int grp) {
  asm volatile("s_cmp_lg_u32 %0, %1\n\ts_cbranch_scc1 CB%=\n\ts_barrier\nCB%=:" :: "s"(grp), "i"(G) : "memory", "scc");
  SB();
}
DI float gather16(rsrc_t r, int voff);
DI u32x2 tr_read(LAS unsigned char* p) {
  return __builtin_bit_cast(u32x2, __builtin_amdgcn_ds_read_tr16_b64_v4i16((LAS s16x4*)p));
}
template <int M1, int M2> DI u32x8 tr_read8(LAS unsigned char* p) {
  const u32x2 a = tr_read(p), b = tr_read(p + M1), c = tr_read(p + M2), d = tr_read(p + M1 + M2);
  return u32x8{a[0], a[1], b[0], b[1], c[0], c[1], d[0], d[1]};
}
DI f32x2 fold_rows8(const float (&acc)[8]) {
  const float t0 = tsum32(acc[0], acc[4]), t1 = tsum32(acc[1], acc[5]), t2 = tsum32(acc[2], acc[6]), t3 = tsum32(acc[3], acc[7]);
  return f32x2{tsum16(t0, t2), tsum16(t1, t3)};
}
DI float fold_rows4(float a0, float a1, float a2, float a3) { return tsum16(tsum32(a0, a2), tsum32(a1, a3)); }
DI unsigned max4(u32x4 v) { return max(max(v[0], v[1]), max(v[2], v[3])); }

DI float gather16(rsrc_t r, int voff) {
  u32x4 A, Bv;
  for (unsigned spins = 0; spins < SPIN_CAP; ++spins) {
    asm volatile("" ::: "memory");
    A = __builtin_amdgcn_raw_buffer_load_b128(r, voff, 0, AUX_SC1);
    Bv = __builtin_amdgcn_raw_buffer_load_b128(r, voff, 1024, AUX_SC1);
    if (__builtin_amdgcn_ballot_w64(max(max4(A), max4(Bv)) == SENT) == 0) break;
  }
  const f32x4 s = __builtin_bit_cast(f32x4, A) + __builtin_bit_cast(f32x4, Bv);
  float t = fold_rows4(s[0], s[1], s[2], s[3]);
  t += dppf<ROR4>(t); t += dppf<ROR8>(t);
  return t;
}

template <int I> DI void pv_step(float (&acc)[8], unsigned plo, unsigned phi, u32x4 v) {
  const unsigned lo = dppu<BCAST + I>(plo), hi = dppu<BCAST + I>(phi);
#pragma unroll
  for (int k = 0; k < 4; ++k) { acc[2 * k] = dot2(lo, v[k], acc[2 * k]); acc[2 * k + 1] = dot2(hi, v[k], acc[2 * k + 1]); }
}
DI void pin8(float (&a)[8]) {
  asm volatile("" : "+v"(a[0]), "+v"(a[1]), "+v"(a[2]), "+v"(a[3]), "+v"(a[4]), "+v"(a[5]), "+v"(a[6]), "+v"(a[7]));
}
template <int... Is> struct iseq {};
template <int N, int... Is> struct mkseq : mkseq<N - 1, N - 1, Is...> {};
template <int... Is> struct mkseq<0, Is...> { typedef iseq<Is...> type; };
template <class Fn, int... Is> DI void sfor_(Fn&& f, iseq<Is...>) { (f(std::integral_constant<int, Is>{}), ...); }
template <int N, class Fn> DI void sfor(Fn&& f) { sfor_(f, typename mkseq<N>::type{}); }

DI float transpose_reduce16(float (&x)[16], int c) {
#define TR_STEP(HALF, CTRL, BIT) _Pragma("unroll") for (int h_ = 0; h_ < (HALF); ++h_) { \
    const float lo_ = x[h_], hi_ = x[h_ + (HALF)]; const float t1_ = lo_ + dppf<CTRL>(lo_), t2_ = hi_ + dppf<CTRL>(hi_); \
    x[h_] = (c & (BIT)) ? t2_ : t1_; }
  TR_STEP(8, ROR8, 8) TR_STEP(4, HMIR, 4) TR_STEP(2, XOR2, 2) TR_STEP(1, XOR1, 1)
#undef TR_STEP
  return x[0];
}

#define STAMP(i) do {} while (0)
#define ESTAMP(i) do {} while (0)
#define TSTAMP(i) do {} while (0)

extern "C" __global__ void __launch_bounds__(768, 3)
fused_block(const unsigned char* __restrict__ wq, const unsigned char* __restrict__ kc, const unsigned char* __restrict__ wo,
            const unsigned char* __restrict__ vc, const unsigned short* __restrict__ x, const unsigned char* __restrict__ w1,
            const unsigned char* __restrict__ b1, const unsigned char* __restrict__ w2, const unsigned short* __restrict__ b2,
            unsigned short* __restrict__ out, unsigned char* ws) {
  __shared__ __attribute__((aligned(16))) unsigned char smem[L_BYTES];
  LAS unsigned char* L = (LAS unsigned char*)smem;
  unsigned long long xt[6]; xt[0] = __builtin_amdgcn_s_memrealtime();
  const int blk = blockIdx.x, b = blk >> 5, h = blk & 31;
  const int tid = threadIdx.x, lane = tid & 63, r = lane >> 4, c = lane & 15;
  const int w = __builtin_amdgcn_readfirstlane(tid >> 6);
  const int slice = (h & 7) * 4 + (h >> 3), col0 = slice * FS;
#define IC(n) std::integral_constant<int, (n)>{}
  if (w >= 8) {
  const int u = w - 8;
  const rsrc_t rws = mk_rsrc(ws, WS_BYTES);
  const int vin = blk * IN_BYTES + lane * 16;
  const u32x4 sent4 = {SENT, SENT, SENT, SENT};
  if (!(EXPM & 1)) {
    __builtin_amdgcn_raw_buffer_store_b128(sent4, rws, (u == 0 || (u == 3 ? !SENTH : !SENTP)) ? vin : OOB, 1024 + u * 1024, AUX_SC1);
    __builtin_amdgcn_raw_buffer_store_b128(sent4, rws, u == 0 ? vin : OOB, 0, AUX_SC1);
  }
  const u32x4 xrow = *(const u32x4*)(x + b * D + lane * 8);
  const float xe0 = asf((unsigned)x[b * D + 128 * u + 4 * c + r] << 16), xe1 = asf((unsigned)x[b * D + 128 * u + 64 + 4 * c + r] << 16);
  const int v_hb = (u == 0 && lane < 32 && !(EXPM & 2)) ? WS_HB + b * 256 + lane * 4 : OOB;
  const unsigned snap = atom_add(rws, v_hb, 0);
  float b1v = asf((unsigned)__builtin_amdgcn_raw_buffer_load_b16(mk_rsrc(b1, F * 2), lane < FS ? (col0 + lane) * 2 : OOB, 0, 0) << 16);
  if (PFCODE && b == 0 && u >= 2) {
    unsigned long long pc; int o0, o1;
    asm volatile("s_getpc_b64 %0\nKPC0:\n\ts_mov_b32 %1, KPC0-KPCA\n\ts_mov_b32 %2, KPCB-KPCA" : "=s"(pc), "=s"(o0), "=s"(o1));
    const rsrc_t rc = mk_rsrc((const void*)(pc - (long long)o0), o1);
    unsigned p0 = __builtin_amdgcn_raw_buffer_load_b32(rc, lane * 128, (u - 2) * 8192, 0);
    PIN(p0);
  }
  SB();
  {
    float xf[8];
#pragma unroll
    for (int k = 0; k < 4; ++k) { xf[2 * k] = asf(xrow[k] << 16); xf[2 * k + 1] = asf(xrow[k] & 0xffff0000u); }
    const float mean = wave_sum(((xf[0] + xf[1]) + (xf[2] + xf[3])) + ((xf[4] + xf[5]) + (xf[6] + xf[7]))) * (1.f / D);
    float vs = 0.f;
#pragma unroll
    for (int k = 0; k < 8; ++k) { const float d = xf[k] - mean; vs = fmaf(d, d, vs); }
    const float var = wave_sum(vs) * (1.f / D);
    const float den = rbf(sqrtf(rbf(rbf(var) + 1e-5f))), mb = rbf(mean);
    const unsigned xp = cvtpk(rbf(xe0 - mb) / den, rbf(xe1 - mb) / den);
    unsigned xlo[2] = {xp & 0xffffu, xp >> 16}, xhi[2] = {xp << 16, xp & 0xffff0000u};
    PIN(xlo[0]); PIN(xlo[1]); PIN(xhi[0]); PIN(xhi[1]); PIN(b1v);
    PACEN(0);
    ESTAMP(5);
    if (BEATD) __builtin_amdgcn_s_sleep(BEATD);
    (void)atom_add(rws, lane == h ? v_hb : OOB, 1);
    BARRIER();
    ESTAMP(6);
    float acc[8] = {0.f, 0.f, 0.f, 0.f, 0.f, 0.f, 0.f, 0.f};
    sfor<32>([&](auto I) { pv_step<I & 15>(acc, xlo[I >> 4], xhi[I >> 4], *(const LAS u32x4*)(L + L_W + u * 32768 + I * 1024 + lane * 16)); });
    *(LAS f32x2*)(L + L_RED + (u >> 1) * 1024 + lane * 16 + (u & 1) * 8) = fold_rows8(acc);
  }
  STAMP(11);
  BARRIER();
  BARRIER();
  u32x8 WO[16];
  const int q4 = (lane >> 2) & 3;
  LAS unsigned char* lw = L + L_W + u * 32768 + (lane & 3) * 8;
  {
    LAS unsigned char* p = lw + (32 * (r >> 1) + 4 * (r & 1) + (q4 >> 1) + 8 * (q4 & 1)) * 128;
    sfor<16>([&](auto J) { WO[J] = tr_read8<2 * 128, 64 * 128>(p + (J >> 3) * 16384 + ((J >> 1) & 3) * 32 + (J & 1) * 16 * 128); });
    sfor<16>([&](auto J) { PIN(WO[J]); });
  }
  BARRIER();
  BARRIER();
  if (u == 0 && !(EXPM & 2)) {
    for (unsigned spins = 0; spins < SPIN_CAP; ++spins) {
      const unsigned cur = atom_add(rws, v_hb, 0);
      if (__builtin_amdgcn_ballot_w64(lane < 32 && cur == snap) == 0) break;
    }
  }
  if (SENTH) { __builtin_amdgcn_raw_buffer_store_b128(sent4, rws, u == 3 ? vin : OOB, IN_H, AUX_SC1); PACEN(0); }
  int vp = lane < 16 ? (b * 32 + 8 * u + (lane >> 2)) * IN_BYTES + (lane & 3) * 16 : OOB;
  int lrd = lane * 16, v_beat = lane == h ? v_hb : OOB;
  int idx = (lane & 1) ? 0xEEEEEEEE : 0x44444444;
  PIN(vp); PIN(lrd); PIN(idx); PIN(v_beat);
  const f32x4 zero4 = {0.f, 0.f, 0.f, 0.f};
  f32x4 d[8];
  d[0] = zero4; d[1] = zero4; PIN(d[0]); PIN(d[1]);
  int so1 = IN_P1 + h * 64, so2 = IN_P1 + h * 64 + 4 * IN_BYTES;
  PINS(so1); PINS(so2);
  BARRIER();
  TSTAMP(2);
  {
    f32x4 s4 = *(const LAS f32x4*)(L + L_RED2 + lrd) + *(const LAS f32x4*)(L + L_RED2 + 1024 + lrd);
    s4 += *(const LAS f32x4*)(L + L_RED2 + 2048 + lrd) + *(const LAS f32x4*)(L + L_RED2 + 3072 + lrd);
    const unsigned op = cvtpk(s4[0] + s4[2], s4[1] + s4[3]);
    const u32x4 a0 = {op, dppu<SHL + 2>(op), dppu<SHL + 4>(op), dppu<SHL + 6>(op)};
    const u32x4 a1 = {dppu<SHL + 8>(op), dppu<SHL + 10>(op), dppu<SHL + 12>(op), dppu<SHL + 14>(op)};
    float y[8];
    auto fin = [&](auto T) { y[T] = d[T][0] + d[T][1]; PIN(y[T]); KEEP(d[T]); };
    sfor<8>([&](auto T) {
      d[T] = smf(a0, WO[2 * T], d[T], idx);
      if constexpr (T > 0) SB();
      if constexpr (T + 2 < 8 && T > 0) { d[T + 2] = zero4; PIN(d[T + 2]); }
      SB();
      d[T] = smf(a1, WO[2 * T + 1], d[T], idx);
      SB();
      if constexpr (T == 0) { d[2] = zero4; PIN(d[2]); }
      if constexpr (T >= 2) fin(IC(T - 2));
      if constexpr (T == 6)
        __builtin_amdgcn_raw_buffer_store_b128(u32x4{fbits(y[0]), fbits(y[1]), fbits(y[2]), fbits(y[3])}, rws, (EXPM & 8) ? OOB : vp, so1, AUX_SC1);
      SB();
    });
    fin(IC(6)); fin(IC(7));
    __builtin_amdgcn_raw_buffer_store_b128(u32x4{fbits(y[4]), fbits(y[5]), fbits(y[6]), fbits(y[7])}, rws, (EXPM & 8) ? OOB : vp, so2, AUX_SC1);
  }
  SB();
  STAMP(5); TSTAMP(3);
  (void)atom_add(rws, v_beat, 1);
  u32x8 W2[8];
  {
    const int ln = __builtin_amdgcn_mbcnt_hi(~0u, __builtin_amdgcn_mbcnt_lo(~0u, 0u)), rr = ln >> 4, qq = (ln >> 2) & 3;
    LAS unsigned char* p2 = L + L_W + u * 32768 + (ln & 3) * 8 + 8192 + (16 * (rr >> 1) + 8 * (rr & 1) + (qq >> 1) + 2 * (qq & 1)) * 128;
    LAS unsigned char* p3 = p2 + (32 - 16 * (rr >> 1)) * 128;
    sfor<8>([&](auto J) {
      constexpr int o = (J >> 2) * 16384 + (J & 3) * 32;
      const u32x2 t0 = tr_read(p2 + o), t1 = tr_read(p2 + o + 4 * 128), t2 = tr_read(p3 + o), t3 = tr_read(p3 + o + 4 * 128);
      W2[J] = u32x8{t0[0], t0[1], t1[0], t1[1], t2[0], t2[1], t3[0], t3[1]};
    });
    sfor<8>([&](auto J) { PIN(W2[J]); d[J] = zero4; PIN(d[J]); });
  }
  so1 = IN_P2 + slice * 64; so2 = IN_P2 + slice * 64 + 4 * IN_BYTES;
  int lst = L_ST;
  float gk = -2.f * 1.4426950408889634f * 0.7978845608028654f;
  unsigned gmask = lrd < FS * 16 ? ~0u : 0u;
  PINS(so1); PINS(so2); PIN(lst); PIN(gk); PIN(gmask);
  SB();
  STAMP(6);
  BARRIER();
  float c1;
  { const f32x4 t = *(const LAS f32x4*)(L + L_EX + lrd); c1 = (t[0] + t[1]) + (t[2] + t[3]); }
  PIN(c1);
  BARRIER();
  if (TAILST == 2) TSTAMP(8);
  {
    const f32x4 t4 = *(const LAS f32x4*)(L + L_EX2 + lrd);
    const f32x2 ms = *(const LAS f32x2*)(L + lst);
    const float tot = (t4[0] + t4[1]) + (t4[2] + t4[3]);
    const float uu = rbf(rbf(fmaf(-ms[0], c1, tot) * ms[1]) + b1v);
    const float z = uu * fmaf(uu * uu, -2.f * 1.4426950408889634f * 0.044715f * 0.7978845608028654f, gk);
    const float g = asf(fbits(uu) & gmask) * __builtin_amdgcn_rcpf(1.f + __builtin_amdgcn_exp2f(z));
    const unsigned gp = cvtpk(g, dppf<XOR2>(g));
    const u32x4 ag = {gp, dppu<SHL + 4>(gp), dppu<SHL + 8>(gp), dppu<SHL + 12>(gp)};
    if (TAILST == 2) { KEEP(ag); TSTAMP(9); }
    float y[8];
    auto fin = [&](auto T) { y[T] = d[T][0] + d[T][1]; PIN(y[T]); KEEP(d[T]); SB(); };
    sfor<8>([&](auto T) { d[T] = smf(ag, W2[T], d[T], idx); SB(); });
    fin(IC(0)); fin(IC(1)); fin(IC(2)); fin(IC(3));
    __builtin_amdgcn_raw_buffer_store_b128(u32x4{fbits(y[0]), fbits(y[1]), fbits(y[2]), fbits(y[3])}, rws, vp, so1, AUX_SC1);
    SB();
    fin(IC(4)); fin(IC(5)); fin(IC(6)); fin(IC(7));
    KEEP(y[0]); KEEP(y[1]); KEEP(y[2]); KEEP(y[3]);
    __builtin_amdgcn_raw_buffer_store_b128(u32x4{fbits(y[4]), fbits(y[5]), fbits(y[6]), fbits(y[7])}, rws, vp, so2, AUX_SC1);
  }
  asm volatile("KPCB:");
  SB();
  STAMP(9); TSTAMP(10);
  } else {
  const int grp = (b + 2 * (h >> 3)) & 7;
  const size_t kvoff = ((size_t)blk * SK + (SK - WIN)) * (DH * 2);
  const rsrc_t rk = mk_rsrc(kc + kvoff, WIN * DH * 2), rv = mk_rsrc(vc + kvoff, WIN * DH * 2);
  const int vkv = tid * 16;
  const int lds0 = (int)(unsigned)(size_t)L, ldsw = lds0 + L_W + w * 16384, ldsd = lds0 + L_DUMP;

  u32x4 K[32], V[32];
  auto ldk = [&](auto I) { ald128nt(K[I], rk, vkv, I * 8192); };
  auto ldv = [&](auto I) { ald128nt(V[I], rv, vkv, I * 8192); };
  ldk(IC(0)); ldk(IC(1));
  asm volatile("KPCA:");
  const rsrc_t rq = mk_rsrc(wq + h * (DH * 2), D * NH * DH * 2 - h * (DH * 2));
  const int vq = (64 * w + r) * (NH * DH * 2) + c * 16;
  dma1(rq, vq, 2 * b * (4 * NH * DH * 2), ldsw + 2 * b * 1024);
  dma1(rq, vq, (2 * b + 1) * (4 * NH * DH * 2), ldsw + (2 * b + 1) * 1024);
  unsigned tch = 0;
  if (TOUCH) {
    const unsigned char* p = w == 0 ? vc + kvoff : w == 1 ? wo + (size_t)h * (DH * D * 2) : w == 2 ? w1 + 256 * F * 2 : w == 3 ? w2 + (size_t)min(col0, F - 1) * (D * 2)
                           : w == 4 ? ws + blk * IN_BYTES + 7168 : w == 5 ? (const unsigned char*)out : w == 6 ? (const unsigned char*)b2 : b1;
    ald32(tch, mk_rsrc(p, 4), lane == 0 ? 0 : OOB);
  }
  const rsrc_t ro = mk_rsrc(wo + (size_t)h * (DH * D * 2), DH * D * 2);
  const int vo = (lane >> 3) * (D * 2) + w * 128 + (lane & 7) * 16;
  const rsrc_t r1 = mk_rsrc(w1, D * F * 2);
  const int v1 = ((lane & 7) < 6 && col0 + (lane & 7) * 8 < F) ? (64 * w + (lane >> 3)) * (F * 2) + col0 * 2 + (lane & 7) * 16 : OOB;
  const int c2 = min(col0, F);
  const rsrc_t r2 = mk_rsrc(w2 + (size_t)c2 * (D * 2), (F - c2) * (D * 2));
  const int b6 = b < 6 ? b : b - 6;
  auto hook = [&](auto I) {
    constexpr int i = I;
    constexpr auto site = [](int i_, int p0) { return (i_ >= p0 && ((i_ - p0) & 1) == 0 && i_ - p0 < 16) ? (i_ - p0) / 2 : -1; };
    if constexpr (site(i, QB0 + 4) >= 0) cond_barrier<site(i, QB0 + 4)>(grp);
    if constexpr (site(i, QB0 + QRDY) >= 0) cond_barrier<site(i, QB0 + QRDY)>(grp);
    if constexpr (site(i, OB0 + 4) >= 0) cond_barrier<site(i, OB0 + 4)>(grp);
    if constexpr (i == CHKS) BARRIER();
    if constexpr (site(i, QB0) >= 0) burst<site(i, QB0), 16, 14>(grp, rq, vq, 4 * NH * DH * 2, ldsw, (2 * b + 2) & 15);
    if constexpr (i == OB0 - 5) { PACE(); dma1(ro, vo, 2 * b * 8192, ldsd); PACE(); dma1(ro, vo, (2 * b + 1) * 8192, ldsd); }
    if constexpr (site(i, OB0) >= 0) burst<site(i, OB0), 16>(grp, ro, vo, 8 * D * 2, ldsw, 2 * b);
    if constexpr (i == MB0 - 5) { PACE(); dma1(r1, v1, b * (8 * F * 2), ldsd); }
    if constexpr (i == MB0 - 4) { PACE(); dma1(r2, vo, b * (8 * D * 2), ldsd); }
    if constexpr (site(i, MB0) >= 0) {
      burst<site(i, MB0), 8>(grp, r1, v1, 8 * F * 2, ldsw, b);
      burst<site(i, MB0), 6>(grp, r2, vo, 8 * D * 2, ldsw + 8192, b6);
    }
  };
  static_assert(QRDY > 4 && QB0 + 14 + QRDY < QSTEP && OB0 >= QB0 + QRDY && OB0 + 14 + 4 <= 37 && MB0 >= 38 && MB0 + 14 <= 63 && CHKS >= 38, "barrier order, staging area reuse");
  constexpr int S0 = 2;
  sfor<QSTEP - S0>([&](auto I) { constexpr int i = I + S0; if constexpr (i == 11) ESTAMP(7); if constexpr (i == 2) ESTAMP(8); if constexpr (i == 3) { SB(); xt[1] = __builtin_amdgcn_s_memrealtime(); SB(); } hook(IC(i)); PACEN(WN + (TOUCH && i < S0 + 4 ? 1 : 0)); ldk(IC(i)); });
  PIN(tch);
  SB(); xt[2] = __builtin_amdgcn_s_memrealtime(); SB();
  unsigned qp[4];
  {
    const f32x4 s4 = *(const LAS f32x4*)(L + L_RED + lane * 16) + *(const LAS f32x4*)(L + L_RED + 1024 + lane * 16);
    rows_bcast(cvtpk(s4[0] + s4[2], s4[1] + s4[3]), qp);
  }
  SB();
  STAMP(2);
  float sc[2], xs[16];
  auto qk = [&](u32x4& kv) { PIN(kv); return dot2(kv[3], qp[3], dot2(kv[2], qp[2], dot2(kv[1], qp[1], dot2(kv[0], qp[0], 0.f)))); };
  static_assert(QSTEP <= 28, "");
  sfor<36 - QSTEP>([&](auto I) {
    constexpr int i = I + QSTEP;
    hook(IC(i));
    PACE();
    if constexpr (i < 32) ldk(IC(i)); else ldv(IC(i - 32));
    if constexpr (I < 3) sfor<(I < 2 ? 6 : 4)>([&](auto J) { xs[6 * I + J] = qk(K[6 * I + J]); PIN(xs[6 * I + J]); });
    if constexpr (I == 3) { sc[0] = transpose_reduce16(xs, c); PIN(sc[0]); }
    if constexpr (I >= 4) {
      constexpr int lo = (I == 4) ? 16 : (16 + 6 * (I - 4) < i - 4 ? 16 + 6 * (I - 4) : i - 4);
      constexpr int hi = (16 + 6 * (I - 3) < i - 3) ? 16 + 6 * (I - 3) : i - 3;
      sfor<hi - lo>([&](auto J) { xs[lo - 16 + J] = qk(K[lo + J]); PIN(xs[lo - 16 + J]); });
    }
    SB();
  });
  sc[1] = transpose_reduce16(xs, c);
  SB();
  hook(IC(36)); PACE(); ldv(IC(4));
  unsigned pp[2];
  {
    const float s0 = rbf(rbf(sc[0]) * 0.08838834764831845f), s1 = rbf(rbf(sc[1]) * 0.08838834764831845f);
    const float mw = wave_max(fmaxf(s0, s1));
    const float e0 = __builtin_amdgcn_exp2f((s0 - mw) * 1.4426950408889634f), e1 = __builtin_amdgcn_exp2f((s1 - mw) * 1.4426950408889634f);
    const float lw = wave_sum(e0 + e1);
    *(LAS f32x2*)(L + L_ML + w * 8) = f32x2{mw, lw};
    SB();
    hook(IC(37)); PACE(); ldv(IC(5));
    BARRIER();
    const f32x2 ml = *(const LAS f32x2*)(L + L_ML + (lane & 7) * 8);
    float M = ml[0];
    M = fmaxf(M, dppf<XOR1>(M)); M = fmaxf(M, dppf<XOR2>(M)); M = fmaxf(M, dppf<HMIR>(M));
    float Ls = ml[1] * __builtin_amdgcn_exp2f((ml[0] - M) * 1.4426950408889634f);
    Ls += dppf<XOR1>(Ls); Ls += dppf<XOR2>(Ls); Ls += dppf<HMIR>(Ls);
    const float scale = __builtin_amdgcn_exp2f((mw - M) * 1.4426950408889634f) / Ls;
    pp[0] = cvtpk(e0 * scale, e0 * scale); pp[1] = cvtpk(e1 * scale, e1 * scale);
  }
  SB();
  STAMP(3);
  f32x4 acc = {0.f, 0.f, 0.f, 0.f};
  u32x4 sel;
#pragma unroll
  for (int k = 0; k < 4; ++k) sel[k] = ((c & 7) >> 1) == k ? ((c & 1) ? 0xffff0000u : 0xffffu) : 0u;
  auto pv = [&](auto I) { PIN(V[I]); const unsigned pb = dppu<BCAST + (I & 15)>(pp[I >> 4]); acc = mfma(V[I], sel & pb, acc); PIN(acc); };
  const int ocol = 64 * (h >> 2) + 16 * r + 4 * (h & 3) + (c & 3);
  unsigned b2v, xo;
  int lw2 = (w >> 1) * 1024 + lane * 16 + (w & 1) * 8;
  u32x4 blast;
  sfor<29>([&](auto I) {
    constexpr int i = I + 6;
    hook(IC(i + 32));
    if constexpr (i < 32) { PACE(); ldv(IC(i)); }
    else PACEN(35 - i);
    if constexpr (i == 24) { ald16(b2v, mk_rsrc(b2, D * 2), w == 0 ? ocol * 2 : OOB, 0); ald16(xo, mk_rsrc(x, NB * D * 2), w == 0 ? ocol * 2 : OOB, b * (D * 2)); PIN(lw2); }
    if constexpr (i == 6) { pv(IC(0)); pv(IC(1)); }
    pv(IC(i - 4));
    SB();
  });
  blast = sel & dppu<BCAST + 15>(pp[1]);
  PIN(blast);
  PACEN(0);
  STAMP(4); TSTAMP(1);
  PIN(V[31]);
  acc = mfma(V[31], blast, acc);
  *(LAS f32x2*)(L + L_RED2 + lw2) = (c & 8) ? f32x2{acc[2], acc[3]} : f32x2{acc[0], acc[1]};
  BARRIER();
  SB(); xt[3] = __builtin_amdgcn_s_memrealtime(); SB();
  PIN(b2v); PIN(xo);
  if (w == 0) {
    const rsrc_t rws = mk_rsrc(ws, WS_BYTES);
    int vin = blk * IN_BYTES + lane * 16;
    if (SENTP) {
      const u32x4 sent4 = {SENT, SENT, SENT, SENT};
      __builtin_amdgcn_raw_buffer_store_b128(sent4, rws, vin, IN_P2, AUX_SC1);
      __builtin_amdgcn_raw_buffer_store_b128(sent4, rws, vin, IN_P2 + 1024, AUX_SC1);
    }
    int vhs = HST == 0 ? (b * 32 + c) * IN_BYTES + IN_H + h * 32 + r * 8 : (b * 32 + (lane >> 1)) * IN_BYTES + IN_H + h * 32 + (lane & 1) * 16;
    int vout = c < 4 ? (b * D + ocol) * 2 : OOB;
    b2v <<= 16; xo <<= 16;
    PIN(vin); PIN(vhs); PIN(vout); PIN(b2v); PIN(xo);
    if (DLY0) __builtin_amdgcn_s_sleep(DLY0);
    PACEN(0);
    const float hf = asf(xo) + rbf(gather16(rws, vin + IN_P1));
    const unsigned hq = min(cvtpk(hf, dppf<XOR1>(hf)), SENT - 1u);
    unsigned hb[4];
    rows_bcast(dppu<0x88>(hq), hb);
    __builtin_amdgcn_raw_buffer_store_b128(u32x4{hb[0], hb[1], hb[2], hb[3]}, rws, vhs, 0, AUX_SC1);
    SB();
    STAMP(7); TSTAMP(5);
    BARRIER();
    float hres = asf(hq << 16);
    PIN(hres);
    if (DLY1) __builtin_amdgcn_s_sleep(DLY1);
    u32x4 hr;
    for (unsigned spins = 0; spins < SPIN_CAP; ++spins) {
      asm volatile("" ::: "memory");
      hr = __builtin_amdgcn_raw_buffer_load_b128(rws, vin, IN_H, AUX_SC1);
      if (__builtin_amdgcn_ballot_w64(max4(hr) == SENT) == 0) break;
    }
    TSTAMP(7);
    {
      const float s1 = dot2(hr[3], 0x3f803f80u, dot2(hr[2], 0x3f803f80u, dot2(hr[1], 0x3f803f80u, dot2(hr[0], 0x3f803f80u, 0.f))));
      const float s2 = dot2(hr[3], hr[3], dot2(hr[2], hr[2], dot2(hr[1], hr[1], dot2(hr[0], hr[0], 0.f))));
      float t = tsum32(s1, s2);
      t = row_sum(tsum16(t, t));
      const float mean = asf(__builtin_amdgcn_readlane(fbits(t), 0)) * (1.f / D);
      const float var = fmaxf(fmaf(-mean, mean, asf(__builtin_amdgcn_readlane(fbits(t), 32)) * (1.f / D)), 0.f);
      const float den = rbf(__builtin_amdgcn_sqrtf(rbf(rbf(var) + 1e-5f)));
      *(LAS f32x2*)(L + L_ST) = f32x2{rbf(mean), __builtin_amdgcn_rcpf(den)};
    }
    if (TAILST == 1) TSTAMP(9);
    BARRIER();
    if (DLY2) __builtin_amdgcn_s_sleep(DLY2);
    const float res = hres + rbf(rbf(gather16(rws, vin + IN_P2)) + asf(b2v));
    __builtin_amdgcn_raw_buffer_store_b16(__builtin_bit_cast(unsigned short, (__bf16)res), mk_rsrc(out, NB * D * 2), vout, 0, 0);
    { SB(); xt[4] = __builtin_amdgcn_s_memrealtime(); const unsigned long long tgt = xt[4] + XBASE + XMUL * (long long)(xt[XB] - xt[XA]); while (__builtin_amdgcn_s_memrealtime() < tgt) {} }
  } else if (w >= 4) {
    const int u = w - 4, q4 = (lane >> 2) & 3;
    const rsrc_t rws = mk_rsrc(ws, WS_BYTES);
    __builtin_amdgcn_s_sleep(3);
    u32x8 W1[6];
    LAS unsigned char* p1 = L + L_W + u * 32768 + (lane & 3) * 8 + (4 * (r >> 1) + 32 * (r & 1) + q4) * 128;
    sfor<6>([&](auto J) { W1[J] = tr_read8<16 * 128, 8 * 128>(p1 + (J & 1) * 16384 + (J >> 1) * 32); });
    sfor<6>([&](auto J) { PIN(W1[J]); });
    __builtin_amdgcn_s_sleep(6);
    int vhl = blk * IN_BYTES + IN_H + u * 256 + r * 32 + (lane & 1) * 16, lwr = lane * 16 + u * 4;
    int idx = (lane & 1) ? 0xEEEEEEEE : 0x44444444;
    const f32x4 zero4 = {0.f, 0.f, 0.f, 0.f};
    f32x4 e[3];
    auto project = [&](u32x4 h0, u32x4 h1, int dst) {
      sfor<3>([&](auto T) { e[T] = smf(h0, W1[2 * T], e[T], idx); SB(); e[T] = smf(h1, W1[2 * T + 1], e[T], idx); SB(); });
      float a0 = e[0][0] + e[0][1], a1 = e[1][0] + e[1][1];
      a0 = r == 0 ? a0 : a1;
      PIN(a0);
      const float a2 = e[2][0] + e[2][1];
      KEEP(e[0]); KEEP(e[1]); KEEP(e[2]);
      *(LAS float*)(L + dst + lwr) = r >= 2 ? a2 : a0;
    };
    const u32x4 ones = {0x3f803f80u, 0x3f803f80u, 0x3f803f80u, 0x3f803f80u};
    sfor<3>([&](auto T) { e[T] = zero4; });
    project(ones, ones, L_EX);
    sfor<3>([&](auto T) { e[T] = zero4; PIN(e[T]); });
    PIN(vhl); PIN(lwr); PIN(idx);
    STAMP(6);
    BARRIER();
    if (DLY1) __builtin_amdgcn_s_sleep(DLY1);
    u32x4 h0, h1;
    for (unsigned spins = 0; spins < SPIN_CAP; ++spins) {
      asm volatile("" ::: "memory");
      h0 = __builtin_amdgcn_raw_buffer_load_b128(rws, vhl, 0, AUX_SC1);
      h1 = __builtin_amdgcn_raw_buffer_load_b128(rws, vhl, 128, AUX_SC1);
      if (__builtin_amdgcn_ballot_w64(max(max4(h0), max4(h1)) == SENT) == 0) break;
    }
    SB();
    STAMP(8); TSTAMP(6);
    project(h0, h1, L_EX2);
    if (TAILST == 1) TSTAMP(8);
    BARRIER();
  }
  }
}

extern "C" void kernel_launch(void* const* d_in, const int* in_sizes, int n_in, void* d_out, int out_size, void* d_ws,
                              size_t ws_size, hipStream_t stream) {
  fused_block<<<NB * NH, 768, 0, stream>>>((const unsigned char*)d_in[3], (const unsigned char*)d_in[1], (const unsigned char*)d_in[4],
                                      (const unsigned char*)d_in[2], (const unsigned short*)d_in[0], (const unsigned char*)d_in[6],
                                      (const unsigned char*)d_in[7], (const unsigned char*)d_in[8], (const unsigned short*)d_in[9],
                                      (unsigned short*)d_out, (unsigned char*)d_ws);
}
```

```cpp
#define PEND 2060
#include <hip/hip_runtime.h>
#include <hip/hip_bf16.h>
#include <stdint.h>

#define STAMPS 0
#define PFCODE 0
#define WN 3
#define BWN 3
#define QB0 4
#define QRDY 9
#define QSTEP 28
#define OB0 14
#define MB0 35
#define QBS 8
#define OBS 12
#define MBS 12
#define NG 8
#define OWD 5
#define INB 8192
#define SENTH 0
#define SENTP 1
#define CHKS 61
#define PTAU 24
#define PBUR 90
#define PBUM 110
#define DITH 32
#define PSH 1
#define DITG 0
#define TCOM 0
#define SKEW 0
#define PFROM 8
#define PTO 60
#define PEV 2
#define SENTE 1
#define LOSE -1
#define EARLYST 0
#define TOUCH 0
#define SENTW 0
#define TAILST 0
#define HST 1
#define EXPM 0
#define DLY0 20
#define DLY1 11
#define DLY2 18

typedef unsigned u32x4 __attribute__((ext_vector_type(4)));
typedef unsigned u32x2 __attribute__((ext_vector_type(2)));
typedef float f32x2 __attribute__((ext_vector_type(2)));
typedef float f32x4 __attribute__((ext_vector_type(4)));
typedef __bf16 bf16x2 __attribute__((ext_vector_type(2)));
typedef short s16x4 __attribute__((ext_vector_type(4)));
typedef unsigned u32x8 __attribute__((ext_vector_type(8)));
typedef __bf16 bf16x8 __attribute__((ext_vector_type(8)));
typedef __bf16 bf16x16 __attribute__((ext_vector_type(16)));
typedef __amdgpu_buffer_rsrc_t rsrc_t;
#define LAS __attribute__((address_space(3)))
#define DI __device__ __forceinline__

constexpr int NB = 8, D = 512, NH = 32, SK = 4096, DH = 128, WIN = 1024, F = 1376;
constexpr int FS = 48;
constexpr int AUX_NT = 2, AUX_SC1 = 16;
constexpr int OOB = 0x7ffffff0;
constexpr unsigned SENT = 0xffffffffu;
constexpr unsigned SPIN_CAP = 1u << 17;
constexpr int IN_P1 = 0;
constexpr int IN_P2 = 3072;
constexpr int IN_H = 2048;
constexpr int IN_BYTES = INB;
constexpr int WS_T = 256 * IN_BYTES;
constexpr int WS_BYTES = WS_T + 128;
constexpr int WS_STAMP = 4 * 1024 * 1024;
constexpr int L_RED = 0;
constexpr int L_RED2 = L_RED + 2048;
constexpr int L_EX = L_RED2 + 4096;
constexpr int L_EX2 = L_EX + 1024;
constexpr int L_ML = L_EX2 + 1024;
constexpr int L_ST = L_ML + 64;
constexpr int L_T = L_ST + 16;
constexpr int L_Y = 9216;
constexpr int L_DUMP = L_Y + 2048;
constexpr int L_W = L_DUMP + 1024;
constexpr int L_BYTES = L_W + 128 * 1024;

DI float dot2(unsigned a, unsigned b, float acc) {
  return __builtin_amdgcn_fdot2_f32_bf16(__builtin_bit_cast(bf16x2, a), __builtin_bit_cast(bf16x2, b), acc, false);
}
DI unsigned fbits(float f) { return __builtin_bit_cast(unsigned, f); }
DI float asf(unsigned u) { return __builtin_bit_cast(float, u); }
DI unsigned cvtpk(float lo, float hi) {
  bf16x2 r = {(__bf16)lo, (__bf16)hi};
  return __builtin_bit_cast(unsigned, r);
}
DI float rbf(float f) { return asf((unsigned)__builtin_bit_cast(unsigned short, (__bf16)f) << 16); }
template <int CTRL> DI unsigned dppu(unsigned x) { return __builtin_amdgcn_mov_dpp(x, CTRL, 0xf, 0xf, true); }
template <int CTRL> DI float dppf(float x) { return asf(dppu<CTRL>(fbits(x))); }
constexpr int XOR1 = 0xB1, XOR2 = 0x4E, HMIR = 0x141, ROR4 = 0x124, ROR8 = 0x128, BCAST = 0x150;
DI float tsum32(float a, float b) {
  auto s = __builtin_amdgcn_permlane32_swap(fbits(a), fbits(b), false, false);
  return asf(s[0]) + asf(s[1]);
}
DI float tsum16(float a, float b) {
  auto s = __builtin_amdgcn_permlane16_swap(fbits(a), fbits(b), false, false);
  return asf(s[0]) + asf(s[1]);
}
DI float row_sum(float x) { x += dppf<XOR1>(x); x += dppf<XOR2>(x); x += dppf<ROR4>(x); x += dppf<ROR8>(x); return x; }
DI float row_max(float x) { x = fmaxf(x, dppf<XOR1>(x)); x = fmaxf(x, dppf<XOR2>(x)); x = fmaxf(x, dppf<ROR4>(x)); x = fmaxf(x, dppf<ROR8>(x)); return x; }
DI float wave_sum(float x) { x = row_sum(x); x = tsum16(x, x); return tsum32(x, x); }
DI float wave_max(float x) {
  x = row_max(x);
  auto s = __builtin_amdgcn_permlane16_swap(fbits(x), fbits(x), false, false);
  x = fmaxf(asf(s[0]), asf(s[1]));
  auto t = __builtin_amdgcn_permlane32_swap(fbits(x), fbits(x), false, false);
  return fmaxf(asf(t[0]), asf(t[1]));
}
DI void rows_bcast(unsigned x, unsigned (&o)[4]) {
  auto s = __builtin_amdgcn_permlane16_swap(x, x, false, false);
  auto a = __builtin_amdgcn_permlane32_swap(s[0], s[0], false, false);
  auto b = __builtin_amdgcn_permlane32_swap(s[1], s[1], false, false);
  o[0] = a[0]; o[2] = a[1]; o[1] = b[0]; o[3] = b[1];
}
DI f32x4 smf(u32x4 a, u32x8 b, f32x4 c, int idx) {
  return __builtin_amdgcn_smfmac_f32_16x16x64_bf16(__builtin_bit_cast(bf16x8, a), __builtin_bit_cast(bf16x16, b), c, idx, 0, 0);
}
DI f32x4 mfma(u32x4 a, u32x4 b, f32x4 c) {
  return __builtin_amdgcn_mfma_f32_16x16x32_bf16(__builtin_bit_cast(bf16x8, a), __builtin_bit_cast(bf16x8, b), c, 0, 0, 0);
}
constexpr int SHL = 0x100;
template <class T> DI void pin_(T& x) { asm volatile("" : "+v"(x)); }
#define PIN(x) pin_(x)
template <class T> DI void keep_(const T& x) { asm volatile("" :: "v"(x)); }
#define KEEP(x) keep_(x)
template <class T> DI void pins_(T& x) { asm volatile("" : "+s"(x)); }
#define PINS(x) pins_(x)
#define SB() __builtin_amdgcn_sched_barrier(0)
#define PACEN(n) do { asm volatile("s_waitcnt vmcnt(%0)" :: "i"(n) : "memory"); SB(); } while (0)
#define PACE() PACEN(WN)
#define BARRIER() do { asm volatile("s_waitcnt lgkmcnt(0)\n\ts_barrier" ::: "memory"); SB(); } while (0)
DI rsrc_t mk_rsrc(const void* p, int bytes) { return __builtin_amdgcn_make_buffer_rsrc((void*)p, 0, bytes, 0x00020000); }

template <int G, int N, int CNT = N> DI void burst(int grp, rsrc_t r, int voff, int stride, int lds, int j0) {
  int keep, j, cnt, t;
  asm volatile(
      "s_cmp_lg_u32 %4, %10\n\t"
      "s_cbranch_scc1 BE%=\n\t"
      "s_mov_b32 %0, m0\n\t"
      "s_mov_b32 %1, %9\n\t"
      "s_mov_b32 %2, %12\n"
      "BL%=:\n\t"
      "s_lshl_b32 %3, %1, 10\n\t"
      "s_add_u32 m0, %3, %8\n\t"
      "s_mul_i32 %3, %1, %7\n\t"
      "s_add_u32 %1, %1, 1\n\t"
      "s_waitcnt vmcnt(%13)\n\t"
      "buffer_load_dwordx4 %6, %5, %3 offen lds\n\t"
      "s_cmp_eq_u32 %1, %11\n\t"
      "s_cselect_b32 %1, 0, %1\n\t"
      "s_sub_u32 %2, %2, 1\n\t"
      "s_cmp_lg_u32 %2, 0\n\t"
      "s_cbranch_scc1 BL%=\n\t"
      "s_mov_b32 m0, %0\n"
      "BE%=:"
      : "=&s"(keep), "=&s"(j), "=&s"(cnt), "=&s"(t)
      : "s"(grp), "s"(r), "v"(voff), "s"(stride), "s"(lds), "s"(j0), "i"(G), "i"(N), "i"(CNT), "i"(BWN)
      : "memory", "scc");
  SB();
}
DI void ald128nt(u32x4& d, rsrc_t r, int voff, int soff) { asm volatile("buffer_load_dwordx4 %0, %1, %2, %3 offen nt" : "=v"(d) : "v"(voff), "s"(r), "s"(soff) : "memory"); SB(); }
DI void ald128(u32x4& d, rsrc_t r, int voff, int soff) { asm volatile("buffer_load_dwordx4 %0, %1, %2, %3 offen" : "=v"(d) : "v"(voff), "s"(r), "s"(soff) : "memory"); SB(); }
DI void ald64sc(u32x2& d, rsrc_t r, int voff, int soff) { asm volatile("buffer_load_dwordx2 %0, %1, %2, %3 offen sc1" : "=v"(d) : "v"(voff), "s"(r), "s"(soff) : "memory"); SB(); }
DI void ald32sc(unsigned& d, rsrc_t r, int voff) { asm volatile("buffer_load_dword %0, %1, %2, 0 offen sc1" : "=v"(d) : "v"(voff), "s"(r) : "memory"); SB(); }
DI void ald32(unsigned& d, rsrc_t r, int voff) { asm volatile("buffer_load_dword %0, %1, %2, 0 offen" : "=v"(d) : "v"(voff), "s"(r) : "memory"); SB(); }
DI void ald16(unsigned& d, rsrc_t r, int voff, int soff) { asm volatile("buffer_load_ushort %0, %1, %2, %3 offen" : "=v"(d) : "v"(voff), "s"(r), "s"(soff) : "memory"); SB(); }
template <bool NT = false> DI void dma1(rsrc_t r, int voff, int soff, int lds) {
  int keep;
  if constexpr (NT)
    asm volatile("s_mov_b32 %0, m0\n\ts_mov_b32 m0, %4\n\ts_nop 0\n\tbuffer_load_dwordx4 %1, %2, %3 offen nt lds\n\ts_mov_b32 m0, %0"
                 : "=&s"(keep) : "v"(voff), "s"(r), "s"(soff), "s"(lds) : "memory");
  else
    asm volatile("s_mov_b32 %0, m0\n\ts_mov_b32 m0, %4\n\ts_nop 0\n\tbuffer_load_dwordx4 %1, %2, %3 offen lds\n\ts_mov_b32 m0, %0"
                 : "=&s"(keep) : "v"(voff), "s"(r), "s"(soff), "s"(lds) : "memory");
  SB();
}
template <int G> DI void cond_barrier(int grp) {
  asm volatile("s_cmp_lg_u32 %0, %1\n\ts_cbranch_scc1 CB%=\n\ts_barrier\nCB%=:" :: "s"(grp), "i"(G) : "memory", "scc");
  SB();
}
DI float gather16(rsrc_t r, int voff);
DI u32x2 tr_read(LAS unsigned char* p) {
  return __builtin_bit_cast(u32x2, __builtin_amdgcn_ds_read_tr16_b64_v4i16((LAS s16x4*)p));
}
template <int M1, int M2> DI u32x8 tr_read8(LAS unsigned char* p) {
  const u32x2 a = tr_read(p), b = tr_read(p + M1), c = tr_read(p + M2), d = tr_read(p + M1 + M2);
  return u32x8{a[0], a[1], b[0], b[1], c[0], c[1], d[0], d[1]};
}
DI f32x2 fold_rows8(const float (&acc)[8]) {
  const float t0 = tsum32(acc[0], acc[4]), t1 = tsum32(acc[1], acc[5]), t2 = tsum32(acc[2], acc[6]), t3 = tsum32(acc[3], acc[7]);
  return f32x2{tsum16(t0, t2), tsum16(t1, t3)};
}
DI float fold_rows4(float a0, float a1, float a2, float a3) { return tsum16(tsum32(a0, a2), tsum32(a1, a3)); }
DI unsigned max4(u32x4 v) { return max(max(v[0], v[1]), max(v[2], v[3])); }

DI float gather16(rsrc_t r, int voff) {
  u32x4 A, Bv;
  for (unsigned spins = 0; spins < SPIN_CAP; ++spins) {
    asm volatile("" ::: "memory");
    A = __builtin_amdgcn_raw_buffer_load_b128(r, voff, 0, AUX_SC1);
    Bv = __builtin_amdgcn_raw_buffer_load_b128(r, voff, 1024, AUX_SC1);
    if (__builtin_amdgcn_ballot_w64(max(max4(A), max4(Bv)) == SENT) == 0) break;
  }
  const f32x4 s = __builtin_bit_cast(f32x4, A) + __builtin_bit_cast(f32x4, Bv);
  float t = fold_rows4(s[0], s[1], s[2], s[3]);
  t += dppf<ROR4>(t); t += dppf<ROR8>(t);
  return t;
}

template <int I> DI void pv_step(float (&acc)[8], unsigned plo, unsigned phi, u32x4 v) {
  const unsigned lo = dppu<BCAST + I>(plo), hi = dppu<BCAST + I>(phi);
#pragma unroll
  for (int k = 0; k < 4; ++k) { acc[2 * k] = dot2(lo, v[k], acc[2 * k]); acc[2 * k + 1] = dot2(hi, v[k], acc[2 * k + 1]); }
}
DI void pin8(float (&a)[8]) {
  asm volatile("" : "+v"(a[0]), "+v"(a[1]), "+v"(a[2]), "+v"(a[3]), "+v"(a[4]), "+v"(a[5]), "+v"(a[6]), "+v"(a[7]));
}
template <int... Is> struct iseq {};
template <int N, int... Is> struct mkseq : mkseq<N - 1, N - 1, Is...> {};
template <int... Is> struct mkseq<0, Is...> { typedef iseq<Is...> type; };
template <class Fn, int... Is> DI void sfor_(Fn&& f, iseq<Is...>) { (f(std::integral_constant<int, Is>{}), ...); }
template <int N, class Fn> DI void sfor(Fn&& f) { sfor_(f, typename mkseq<N>::type{}); }

DI float transpose_reduce16(float (&x)[16], int c) {
#define TR_STEP(HALF, CTRL, BIT) _Pragma("unroll") for (int h_ = 0; h_ < (HALF); ++h_) { \
    const float lo_ = x[h_], hi_ = x[h_ + (HALF)]; const float t1_ = lo_ + dppf<CTRL>(lo_), t2_ = hi_ + dppf<CTRL>(hi_); \
    x[h_] = (c & (BIT)) ? t2_ : t1_; }
  TR_STEP(8, ROR8, 8) TR_STEP(4, HMIR, 4) TR_STEP(2, XOR2, 2) TR_STEP(1, XOR1, 1)
#undef TR_STEP
  return x[0];
}

#define STAMP(i) do {} while (0)
#define ESTAMP(i) do {} while (0)
#define TSTAMP(i) do {} while (0)

extern "C" __global__ void __launch_bounds__(768, 3)
fused_block(const unsigned char* __restrict__ wq, const unsigned char* __restrict__ kc, const unsigned char* __restrict__ wo,
            const unsigned char* __restrict__ vc, const unsigned short* __restrict__ x, const unsigned char* __restrict__ w1,
            const unsigned char* __restrict__ b1, const unsigned char* __restrict__ w2, const unsigned short* __restrict__ b2,
            unsigned short* __restrict__ out, unsigned char* ws) {
  __shared__ __attribute__((aligned(16))) unsigned char smem[L_BYTES];
  LAS unsigned char* L = (LAS unsigned char*)smem;
  if (SKEW && (blockIdx.x & 7) == 5) __builtin_amdgcn_s_sleep(SKEW);
  STAMP(0);
  const int blk = blockIdx.x, b = blk >> 5, h = blk & 31;
  const int tid = threadIdx.x, lane = tid & 63, r = lane >> 4, c = lane & 15;
  const int w = __builtin_amdgcn_readfirstlane(tid >> 6);
  const int slice = (h & 7) * 4 + (h >> 3), col0 = slice * FS;
#define IC(n) std::integral_constant<int, (n)>{}
  if (LOSE >= 0 && (blk == LOSE || blk == LOSE + 5)) for (int i = 0; i < (blk == LOSE ? 12 : 30); ++i) __builtin_amdgcn_s_sleep(127);
  unsigned t0 = 0;
  {
    int w2 = w;
    PINS(w2);
    if (w2 >= 8) {
      t0 = (unsigned)__builtin_amdgcn_s_memrealtime();
      const rsrc_t r0 = mk_rsrc(ws, WS_BYTES);
      u32x4 s4 = {SENT, SENT, SENT, SENT};
      PIN(s4);
      if (!(EXPM & 1)) __builtin_amdgcn_raw_buffer_store_b128(s4, r0, w2 >= 9 ? lane * 16 : OOB, blk * IN_BYTES + (w2 & 3) * 1024 - (w2 >= 9 ? 1024 : 0), AUX_SC1);
      if (TCOM) __builtin_amdgcn_raw_buffer_store_b32(t0, r0, (tid == 512 && blk < 8) ? blk * 4 : OOB, WS_T, AUX_SC1);
      SB(); KEEP(s4);
    }
    SB();
  }
  if (w >= 8) {
  const int u = w - 8;
  const rsrc_t rws = mk_rsrc(ws, WS_BYTES);
  const u32x4 xrow = *(const u32x4*)(x + b * D + lane * 8);
  const float xe0 = asf((unsigned)x[b * D + 128 * u + 4 * c + r] << 16), xe1 = asf((unsigned)x[b * D + 128 * u + 64 + 4 * c + r] << 16);
  float b1v = asf((unsigned)__builtin_amdgcn_raw_buffer_load_b16(mk_rsrc(b1, F * 2), lane < FS ? (col0 + lane) * 2 : OOB, 0, 0) << 16);
  if (PFCODE && b == 0 && u >= 2) {
    unsigned long long pc; int o0, o1;
    asm volatile("s_getpc_b64 %0\nKPC0:\n\ts_mov_b32 %1, KPC0-KPCA\n\ts_mov_b32 %2, KPCB-KPCA" : "=s"(pc), "=s"(o0), "=s"(o1));
    const rsrc_t rc = mk_rsrc((const void*)(pc - (long long)o0), o1);
    unsigned p0 = __builtin_amdgcn_raw_buffer_load_b32(rc, lane * 128, (u - 2) * 8192, 0);
    PIN(p0);
  }
  SB();
  {
    float xf[8];
#pragma unroll
    for (int k = 0; k < 4; ++k) { xf[2 * k] = asf(xrow[k] << 16); xf[2 * k + 1] = asf(xrow[k] & 0xffff0000u); }
    const float mean = wave_sum(((xf[0] + xf[1]) + (xf[2] + xf[3])) + ((xf[4] + xf[5]) + (xf[6] + xf[7]))) * (1.f / D);
    float vs = 0.f;
#pragma unroll
    for (int k = 0; k < 8; ++k) { const float d = xf[k] - mean; vs = fmaf(d, d, vs); }
    const float var = wave_sum(vs) * (1.f / D);
    const float den = rbf(sqrtf(rbf(rbf(var) + 1e-5f))), mb = rbf(mean);
    const unsigned xp = cvtpk(rbf(xe0 - mb) / den, rbf(xe1 - mb) / den);
    unsigned xlo[2] = {xp & 0xffffu, xp >> 16}, xhi[2] = {xp << 16, xp & 0xffff0000u};
    PIN(xlo[0]); PIN(xlo[1]); PIN(xhi[0]); PIN(xhi[1]); PIN(b1v);
    PACEN(0);
    ESTAMP(5);
    BARRIER();
    ESTAMP(6);
    const unsigned tx = __builtin_amdgcn_raw_buffer_load_b32(rws, (TCOM && u == 0 && lane < 8) ? lane * 4 : OOB, WS_T, AUX_SC1);
    float acc[8] = {0.f, 0.f, 0.f, 0.f, 0.f, 0.f, 0.f, 0.f};
    sfor<32>([&](auto I) { pv_step<I & 15>(acc, xlo[I >> 4], xhi[I >> 4], *(const LAS u32x4*)(L + L_W + u * 32768 + I * 1024 + lane * 16)); });
    *(LAS f32x2*)(L + L_RED + (u >> 1) * 1024 + lane * 16 + (u & 1) * 8) = fold_rows8(acc);
    int dt = (int)(tx - t0);
    dt = (TCOM && dt > -400 && dt < 400) ? dt : 0;
    dt += (int)dppu<XOR1>(dt); dt += (int)dppu<XOR2>(dt); dt += (int)dppu<HMIR>(dt);
    if (tid == 512) *(LAS unsigned*)(L + L_T) = t0 + (dt >> 3);
  }
  STAMP(11);
  BARRIER();
  BARRIER();
  u32x8 WO[16];
  const int q4 = (lane >> 2) & 3;
  LAS unsigned char* lw = L + L_W + u * 32768 + (lane & 3) * 8;
  {
    LAS unsigned char* p = lw + (32 * (r >> 1) + 4 * (r & 1) + (q4 >> 1) + 8 * (q4 & 1)) * 128;
    sfor<16>([&](auto J) { WO[J] = tr_read8<2 * 128, 64 * 128>(p + (J >> 3) * 16384 + ((J >> 1) & 3) * 32 + (J & 1) * 16 * 128); });
    sfor<16>([&](auto J) { PIN(WO[J]); });
  }
  BARRIER();
  BARRIER();
  int vp = lane < 16 ? (b * 32 + 8 * u + (lane >> 2)) * IN_BYTES + (lane & 3) * 16 : OOB;
  int lrd = lane * 16;
  int idx = (lane & 1) ? 0xEEEEEEEE : 0x44444444;
  PIN(vp); PIN(lrd); PIN(idx);
  const f32x4 zero4 = {0.f, 0.f, 0.f, 0.f};
  f32x4 d[8];
  d[0] = zero4; d[1] = zero4; PIN(d[0]); PIN(d[1]);
  int so1 = IN_P1 + h * 64, so2 = IN_P1 + h * 64 + 4 * IN_BYTES;
  PINS(so1); PINS(so2);
  BARRIER();
  TSTAMP(2);
  {
    f32x4 s4 = *(const LAS f32x4*)(L + L_RED2 + lrd) + *(const LAS f32x4*)(L + L_RED2 + 1024 + lrd);
    s4 += *(const LAS f32x4*)(L + L_RED2 + 2048 + lrd) + *(const LAS f32x4*)(L + L_RED2 + 3072 + lrd);
    const unsigned op = cvtpk(s4[0] + s4[2], s4[1] + s4[3]);
    const u32x4 a0 = {op, dppu<SHL + 2>(op), dppu<SHL + 4>(op), dppu<SHL + 6>(op)};
    const u32x4 a1 = {dppu<SHL + 8>(op), dppu<SHL + 10>(op), dppu<SHL + 12>(op), dppu<SHL + 14>(op)};
    float y[8];
    auto fin = [&](auto T) { y[T] = d[T][0] + d[T][1]; PIN(y[T]); KEEP(d[T]); };
    sfor<8>([&](auto T) {
      d[T] = smf(a0, WO[2 * T], d[T], idx);
      if constexpr (T > 0) SB();
      if constexpr (T + 2 < 8 && T > 0) { d[T + 2] = zero4; PIN(d[T + 2]); }
      SB();
      d[T] = smf(a1, WO[2 * T + 1], d[T], idx);
      SB();
      if constexpr (T == 0) { d[2] = zero4; PIN(d[2]); }
      if constexpr (T >= 2) fin(IC(T - 2));
      if constexpr (T == 6)
        __builtin_amdgcn_raw_buffer_store_b128(u32x4{fbits(y[0]), fbits(y[1]), fbits(y[2]), fbits(y[3])}, rws, (EXPM & 8) ? OOB : vp, so1, AUX_SC1);
      SB();
    });
    fin(IC(6)); fin(IC(7));
    __builtin_amdgcn_raw_buffer_store_b128(u32x4{fbits(y[4]), fbits(y[5]), fbits(y[6]), fbits(y[7])}, rws, (EXPM & 8) ? OOB : vp, so2, AUX_SC1);
    SB();
    if (lrd < 256) {
      *(LAS f32x4*)(L + L_Y + u * 512 + lrd) = f32x4{y[0], y[1], y[2], y[3]};
      *(LAS f32x4*)(L + L_Y + u * 512 + 256 + lrd) = f32x4{y[4], y[5], y[6], y[7]};
    }
  }
  SB();
  STAMP(5); TSTAMP(3);
  u32x8 W2[8];
  {
    const int ln = __builtin_amdgcn_mbcnt_hi(~0u, __builtin_amdgcn_mbcnt_lo(~0u, 0u)), rr = ln >> 4, qq = (ln >> 2) & 3;
    LAS unsigned char* p2 = L + L_W + u * 32768 + (ln & 3) * 8 + 8192 + (16 * (rr >> 1) + 8 * (rr & 1) + (qq >> 1) + 2 * (qq & 1)) * 128;
    LAS unsigned char* p3 = p2 + (32 - 16 * (rr >> 1)) * 128;
    sfor<8>([&](auto J) {
      constexpr int o = (J >> 2) * 16384 + (J & 3) * 32;
      const u32x2 t0 = tr_read(p2 + o), t1 = tr_read(p2 + o + 4 * 128), t2 = tr_read(p3 + o), t3 = tr_read(p3 + o + 4 * 128);
      W2[J] = u32x8{t0[0], t0[1], t1[0], t1[1], t2[0], t2[1], t3[0], t3[1]};
    });
    sfor<8>([&](auto J) { PIN(W2[J]); d[J] = zero4; PIN(d[J]); });
  }
  so1 = IN_P2 + slice * 64; so2 = IN_P2 + slice * 64 + 4 * IN_BYTES;
  int lst = L_ST;
  float gk = -2.f * 1.4426950408889634f * 0.7978845608028654f;
  unsigned gmask = lrd < FS * 16 ? ~0u : 0u;
  PINS(so1); PINS(so2); PIN(lst); PIN(gk); PIN(gmask);
  SB();
  STAMP(6);
  BARRIER();
  float c1;
  { const f32x4 t = *(const LAS f32x4*)(L + L_EX + lrd); c1 = (t[0] + t[1]) + (t[2] + t[3]); }
  PIN(c1);
  BARRIER();
  if (TAILST == 2) TSTAMP(8);
  {
    const f32x4 t4 = *(const LAS f32x4*)(L + L_EX2 + lrd);
    const f32x2 ms = *(const LAS f32x2*)(L + lst);
    const float tot = (t4[0] + t4[1]) + (t4[2] + t4[3]);
    const float uu = rbf(rbf(fmaf(-ms[0], c1, tot) * ms[1]) + b1v);
    const float z = uu * fmaf(uu * uu, -2.f * 1.4426950408889634f * 0.044715f * 0.7978845608028654f, gk);
    const float g = asf(fbits(uu) & gmask) * __builtin_amdgcn_rcpf(1.f + __builtin_amdgcn_exp2f(z));
    const unsigned gp = cvtpk(g, dppf<XOR2>(g));
    const u32x4 ag = {gp, dppu<SHL + 4>(gp), dppu<SHL + 8>(gp), dppu<SHL + 12>(gp)};
    if (TAILST == 2) { KEEP(ag); TSTAMP(9); }
    float y[8];
    auto fin = [&](auto T) { y[T] = d[T][0] + d[T][1]; PIN(y[T]); KEEP(d[T]); SB(); };
    sfor<8>([&](auto T) { d[T] = smf(ag, W2[T], d[T], idx); SB(); });
    fin(IC(0)); fin(IC(1)); fin(IC(2)); fin(IC(3));
    __builtin_amdgcn_raw_buffer_store_b128(u32x4{fbits(y[0]), fbits(y[1]), fbits(y[2]), fbits(y[3])}, rws, vp, so1, AUX_SC1);
    SB();
    fin(IC(4)); fin(IC(5)); fin(IC(6)); fin(IC(7));
    KEEP(y[0]); KEEP(y[1]); KEEP(y[2]); KEEP(y[3]);
    __builtin_amdgcn_raw_buffer_store_b128(u32x4{fbits(y[4]), fbits(y[5]), fbits(y[6]), fbits(y[7])}, rws, vp, so2, AUX_SC1);
  }
  asm volatile("KPCB:");
  SB();
  STAMP(9); TSTAMP(10);
  } else {
  const int grp = NG == 8 ? (b + 2 * (h >> 3)) & 7 : (NG == 16 ? (2 * b + 4 * (h >> 4) + ((h >> 3) & 1)) & 15 : 4 * b + (h >> 3));
  const int pq = QB0 + ((grp * QBS) >> 2), po = OB0 + ((grp * OBS) >> 2), pm = MB0 + ((grp * MBS) >> 2);
  const size_t kvoff = ((size_t)blk * SK + (SK - WIN)) * (DH * 2);
  const rsrc_t rk = mk_rsrc(kc + kvoff, WIN * DH * 2), rv = mk_rsrc(vc + kvoff, WIN * DH * 2);
  const int vkv = tid * 16;
  const int lds0 = (int)(unsigned)(size_t)L, ldsw = lds0 + L_W + w * 16384, ldsd = lds0 + L_DUMP;

  u32x4 K[32], V[32];
  auto ldk = [&](auto I) { ald128nt(K[I], rk, vkv, I * 8192); };
  auto ldv = [&](auto I) { ald128nt(V[I], rv, vkv, I * 8192); };
  ldk(IC(0)); ldk(IC(1));
  const unsigned long long tclk = __builtin_amdgcn_s_memrealtime();
  SB();
  asm volatile("KPCA:");
  const rsrc_t rq = mk_rsrc(wq + h * (DH * 2), D * NH * DH * 2 - h * (DH * 2));
  const int vq = (64 * w + r) * (NH * DH * 2) + c * 16;
  dma1(rq, vq, 2 * b * (4 * NH * DH * 2), ldsw + 2 * b * 1024);
  dma1(rq, vq, (2 * b + 1) * (4 * NH * DH * 2), ldsw + (2 * b + 1) * 1024);
  unsigned tch = 0;
  if (TOUCH) {
    const unsigned char* p = w == 0 ? vc + kvoff : w == 1 ? wo + (size_t)h * (DH * D * 2) : w == 2 ? w1 + 256 * F * 2 : w == 3 ? w2 + (size_t)min(col0, F - 1) * (D * 2)
                           : w == 4 ? ws + blk * IN_BYTES + 5120 : w == 5 ? (const unsigned char*)out : w == 6 ? (const unsigned char*)b2 : b1;
    ald32(tch, mk_rsrc(p, 4), lane == 0 ? 0 : OOB);
  }
  const rsrc_t ro = mk_rsrc(wo + (size_t)h * (DH * D * 2), DH * D * 2);
  const int vo = (lane >> 3) * (D * 2) + w * 128 + (lane & 7) * 16;
  const rsrc_t r1 = mk_rsrc(w1, D * F * 2);
  const int v1 = ((lane & 7) < 6 && col0 + (lane & 7) * 8 < F) ? (64 * w + (lane >> 3)) * (F * 2) + col0 * 2 + (lane & 7) * 16 : OOB;
  const int c2 = min(col0, F);
  const rsrc_t r2 = mk_rsrc(w2 + (size_t)c2 * (D * 2), (F - c2) * (D * 2));
  const int b6 = b < 6 ? b : b - 6;
  const int dith = (((w - 4) * DITH) >> 3) + ((((blk >> 3) - 16) * DITG) >> 5);
  unsigned tst = PEND ? (unsigned)tclk + dith : 0u;
  auto hook = [&](auto I) {
    constexpr int i = I;
    if constexpr (PEND && i >= PFROM && i <= PTO && i % PEV == 0) {
      PINS(tst);
      const int pend = (i <= pq ? PBUR : 0) + (i <= po ? PBUR : 0) + (i <= pm ? PBUM : 0);
      constexpr int extra = (i <= OB0 - OWD ? 2 : 0) + (i <= MB0 - OWD ? 2 : 0);
      const unsigned tgt = tst + PEND - (67 - i + extra) * PTAU - pend;
      int tmp;
      asm volatile("s_memrealtime vcc\n\ts_waitcnt lgkmcnt(0)\n\ts_sub_i32 %0, %1, vcc_lo\n\ts_ashr_i32 %0, %0, %2\n\ts_cmp_lt_i32 %0, 3\n\ts_cbranch_scc1 PE%=\n\t"
                   "s_add_i32 %0, %0, vcc_lo\n"
                   "PC%=:\n\ts_memrealtime vcc\n\ts_waitcnt lgkmcnt(0)\n\ts_sub_i32 vcc_lo, vcc_lo, %0\n\ts_cmp_lt_i32 vcc_lo, 0\n\ts_cbranch_scc1 PC%=\nPE%=:"
                   : "=&s"(tmp) : "s"(tgt), "i"(PSH) : "scc", "vcc", "memory");
      SB();
    }
    constexpr auto site = [](int i_, int p0, int sp) { for (int g = 0; g < NG; ++g) if (p0 + ((g * sp) >> 2) == i_) return true; return false; };
    if constexpr (site(i - 4, QB0, QBS)) cond_barrier<i - 4>(pq);
    if constexpr (site(i - QRDY, QB0, QBS)) cond_barrier<i - QRDY>(pq);
    if constexpr (site(i - 4, OB0, OBS)) cond_barrier<i - 4>(po);
    if constexpr (site(i, MB0, MBS)) cond_barrier<i>(pm);
    if constexpr (site(i, QB0, QBS)) burst<i, 16, 14>(pq, rq, vq, 4 * NH * DH * 2, ldsw, (2 * b + 2) & 15);
    if constexpr (i == OB0 - OWD) { PACE(); dma1(ro, vo, 2 * b * 8192, ldsd); PACE(); dma1(ro, vo, (2 * b + 1) * 8192, ldsd); }
    if constexpr (site(i, OB0, OBS)) burst<i, 16>(po, ro, vo, 8 * D * 2, ldsw, 2 * b);
    if constexpr (i == MB0 - OWD) { PACE(); dma1(r1, v1, b * (8 * F * 2), ldsd); PACE(); dma1(r2, vo, b * (8 * D * 2), ldsd); }
    if constexpr (site(i, MB0, MBS)) {
      burst<i, 8>(pm, r1, v1, 8 * F * 2, ldsw, b);
      burst<i, 6>(pm, r2, vo, 8 * D * 2, ldsw + 8192, b6);
    }
  };
  static_assert(QRDY > 4 && QB0 + (NG - 1) * QBS / 4 + QRDY < QSTEP && OBS >= QBS && OB0 >= QB0 + QRDY && MB0 > OB0 + 4 &&
                MB0 + (NG - 1) * MBS / 4 > OB0 + (NG - 1) * OBS / 4 + 5 && MB0 + (NG - 1) * MBS / 4 <= CHKS && CHKS >= 52 && CHKS <= 63, "barrier order, staging area reuse");
  constexpr int S0 = 2;
  sfor<QSTEP - S0>([&](auto I) { constexpr int i = I + S0; if constexpr (i == 11) ESTAMP(7); if constexpr (i == 2) ESTAMP(8); if constexpr (i == 3) ESTAMP(9); hook(IC(i)); PACEN(WN + (TOUCH && i < S0 + 4 ? 1 : 0)); ldk(IC(i)); });
  PIN(tch);
  if (TCOM) tst = __builtin_amdgcn_readfirstlane(*(const LAS unsigned*)(L + L_T)) + dith;
  STAMP(1);
  unsigned qp[4];
  {
    const f32x4 s4 = *(const LAS f32x4*)(L + L_RED + lane * 16) + *(const LAS f32x4*)(L + L_RED + 1024 + lane * 16);
    rows_bcast(cvtpk(s4[0] + s4[2], s4[1] + s4[3]), qp);
  }
  SB();
  STAMP(2);
  float sc[2], xs[16];
  auto qk = [&](u32x4& kv) { PIN(kv); return dot2(kv[3], qp[3], dot2(kv[2], qp[2], dot2(kv[1], qp[1], dot2(kv[0], qp[0], 0.f)))); };
  static_assert(QSTEP <= 28, "");
  sfor<36 - QSTEP>([&](auto I) {
    constexpr int i = I + QSTEP;
    hook(IC(i));
    PACE();
    if constexpr (i < 32) ldk(IC(i)); else ldv(IC(i - 32));
    if constexpr (I < 3) sfor<(I < 2 ? 6 : 4)>([&](auto J) { xs[6 * I + J] = qk(K[6 * I + J]); PIN(xs[6 * I + J]); });
    if constexpr (I == 3) { sc[0] = transpose_reduce16(xs, c); PIN(sc[0]); }
    if constexpr (I >= 4) {
      constexpr int lo = (I == 4) ? 16 : (16 + 6 * (I - 4) < i - 4 ? 16 + 6 * (I - 4) : i - 4);
      constexpr int hi = (16 + 6 * (I - 3) < i - 3) ? 16 + 6 * (I - 3) : i - 3;
      sfor<hi - lo>([&](auto J) { xs[lo - 16 + J] = qk(K[lo + J]); PIN(xs[lo - 16 + J]); });
    }
    SB();
  });
  sc[1] = transpose_reduce16(xs, c);
  SB();
  hook(IC(36)); PACE(); ldv(IC(4));
  unsigned pp[2];
  float mw, e1;
  {
    const float s0 = rbf(rbf(sc[0]) * 0.08838834764831845f), s1 = rbf(rbf(sc[1]) * 0.08838834764831845f);
    mw = wave_max(fmaxf(s0, s1));
    const float e0 = __builtin_amdgcn_exp2f((s0 - mw) * 1.4426950408889634f);
    e1 = __builtin_amdgcn_exp2f((s1 - mw) * 1.4426950408889634f);
    pp[0] = cvtpk(e0, e0); pp[1] = cvtpk(e1, e1);
    SB();
    hook(IC(37)); PACE(); ldv(IC(5));
    *(LAS f32x2*)(L + L_ML + w * 8) = f32x2{mw, wave_sum(e0 + e1)};
  }
  SB();
  STAMP(3);
  f32x4 acc = {0.f, 0.f, 0.f, 0.f};
  u32x4 sel;
#pragma unroll
  for (int k = 0; k < 4; ++k) sel[k] = ((c & 7) >> 1) == k ? ((c & 1) ? 0xffff0000u : 0xffffu) : 0u;
  auto pv = [&](auto I) { PIN(V[I]); const unsigned pb = dppu<BCAST + (I & 15)>(pp[I >> 4]); acc = mfma(V[I], sel & pb, acc); PIN(acc); };
  const int ocol = 64 * (h >> 2) + 16 * r + 4 * (h & 3) + (c & 3);
  unsigned b2v, xo;
  int lw2 = (w >> 1) * 1024 + lane * 16 + (w & 1) * 8;
  u32x4 blast;
  sfor<29>([&](auto I) {
    constexpr int i = I + 6;
    hook(IC(i + 32));
    if constexpr (i < 32) { PACE(); ldv(IC(i)); }
    else PACEN(35 - i);
    if constexpr (i == 24) { ald16(b2v, mk_rsrc(b2, D * 2), w == 0 ? ocol * 2 : OOB, 0); ald16(xo, mk_rsrc(x, NB * D * 2), w == 0 ? ocol * 2 : OOB, b * (D * 2)); PIN(lw2); }
    if constexpr (i == 6) { pv(IC(0)); pv(IC(1)); }
    if constexpr (i + 32 == CHKS) {
      BARRIER();
      const f32x2 ml = *(const LAS f32x2*)(L + L_ML + (lane & 7) * 8);
      float M = ml[0];
      M = fmaxf(M, dppf<XOR1>(M)); M = fmaxf(M, dppf<XOR2>(M)); M = fmaxf(M, dppf<HMIR>(M));
      float Ls = ml[1] * __builtin_amdgcn_exp2f((ml[0] - M) * 1.4426950408889634f);
      Ls += dppf<XOR1>(Ls); Ls += dppf<XOR2>(Ls); Ls += dppf<HMIR>(Ls);
      const float scale = __builtin_amdgcn_exp2f((mw - M) * 1.4426950408889634f) / Ls;
      acc *= scale;
      pp[1] = cvtpk(e1 * scale, e1 * scale);
    }
    pv(IC(i - 4));
    SB();
  });
  blast = sel & dppu<BCAST + 15>(pp[1]);
  PIN(blast);
  PACEN(0);
  STAMP(4); TSTAMP(1);
  PIN(V[31]);
  acc = mfma(V[31], blast, acc);
  *(LAS f32x2*)(L + L_RED2 + lw2) = (c & 8) ? f32x2{acc[2], acc[3]} : f32x2{acc[0], acc[1]};
  BARRIER();
  PIN(b2v); PIN(xo);
  if (w == 0) {
    const rsrc_t rws = mk_rsrc(ws, WS_BYTES);
    int vin = blk * IN_BYTES + lane * 16;
    if (SENTP) {
      u32x4 sent4 = {SENT, SENT, SENT, SENT};
      PIN(sent4);
      __builtin_amdgcn_raw_buffer_store_b128(sent4, rws, vin, IN_P2, AUX_SC1);
      __builtin_amdgcn_raw_buffer_store_b128(sent4, rws, vin, IN_P2 + 1024, AUX_SC1);
      asm volatile("s_nop 1" :: "v"(sent4) : "memory");
    }
    int vhs = HST == 0 ? (b * 32 + c) * IN_BYTES + IN_H + h * 32 + r * 8 : (b * 32 + (lane >> 1)) * IN_BYTES + IN_H + h * 32 + (lane & 1) * 16;
    int vout = c < 4 ? (b * D + ocol) * 2 : OOB;
    b2v <<= 16; xo <<= 16;
    PIN(vin); PIN(vhs); PIN(vout); PIN(b2v); PIN(xo);
    if (DLY0) __builtin_amdgcn_s_sleep(DLY0);
    PACEN(0);
    const float hf = asf(xo) + rbf(gather16(rws, vin + IN_P1));
    const unsigned hq = min(cvtpk(hf, dppf<XOR1>(hf)), SENT - 1u);
    unsigned hb[4];
    rows_bcast(dppu<0x88>(hq), hb);
    __builtin_amdgcn_raw_buffer_store_b128(u32x4{hb[0], hb[1], hb[2], hb[3]}, rws, vhs, 0, AUX_SC1);
    SB();
    STAMP(7); TSTAMP(5);
    BARRIER();
    float hres = asf(hq << 16);
    PIN(hres);
    if (DLY1) __builtin_amdgcn_s_sleep(DLY1);
    u32x4 hr;
    for (unsigned spins = 0; spins < SPIN_CAP; ++spins) {
      asm volatile("" ::: "memory");
      hr = __builtin_amdgcn_raw_buffer_load_b128(rws, vin, IN_H, AUX_SC1);
      if (__builtin_amdgcn_ballot_w64(max4(hr) == SENT) == 0) break;
      if ((spins & 63u) == 63u) {
        const int vr = (b * 32 + (lane >> 2)) * IN_BYTES + IN_P1 + h * 64 + (lane & 3) * 16;
        __builtin_amdgcn_raw_buffer_store_b128(*(const LAS u32x4*)(L + L_Y + lane * 16), rws, vr, 0, AUX_SC1);
        __builtin_amdgcn_raw_buffer_store_b128(*(const LAS u32x4*)(L + L_Y + 1024 + lane * 16), rws, vr, 16 * IN_BYTES, AUX_SC1);
      }
    }
    TSTAMP(7);
    {
      const float s1 = dot2(hr[3], 0x3f803f80u, dot2(hr[2], 0x3f803f80u, dot2(hr[1], 0x3f803f80u, dot2(hr[0], 0x3f803f80u, 0.f))));
      const float s2 = dot2(hr[3], hr[3], dot2(hr[2], hr[2], dot2(hr[1], hr[1], dot2(hr[0], hr[0], 0.f))));
      float t = tsum32(s1, s2);
      t = row_sum(tsum16(t, t));
      const float mean = asf(__builtin_amdgcn_readlane(fbits(t), 0)) * (1.f / D);
      const float var = fmaxf(fmaf(-mean, mean, asf(__builtin_amdgcn_readlane(fbits(t), 32)) * (1.f / D)), 0.f);
      const float den = rbf(__builtin_amdgcn_sqrtf(rbf(rbf(var) + 1e-5f)));
      *(LAS f32x2*)(L + L_ST) = f32x2{rbf(mean), __builtin_amdgcn_rcpf(den)};
    }
    if (TAILST == 1) TSTAMP(9);
    BARRIER();
    if (DLY2) __builtin_amdgcn_s_sleep(DLY2);
    const float res = hres + rbf(rbf(gather16(rws, vin + IN_P2)) + asf(b2v));
    __builtin_amdgcn_raw_buffer_store_b16(__builtin_bit_cast(unsigned short, (__bf16)res), mk_rsrc(out, NB * D * 2), vout, 0, 0);
    STAMP(10); TSTAMP(11);
  } else if (w >= 4) {
    const int u = w - 4, q4 = (lane >> 2) & 3;
    const rsrc_t rws = mk_rsrc(ws, WS_BYTES);
    __builtin_amdgcn_s_sleep(3);
    u32x8 W1[6];
    LAS unsigned char* p1 = L + L_W + u * 32768 + (lane & 3) * 8 + (4 * (r >> 1) + 32 * (r & 1) + q4) * 128;
    sfor<6>([&](auto J) { W1[J] = tr_read8<16 * 128, 8 * 128>(p1 + (J & 1) * 16384 + (J >> 1) * 32); });
    sfor<6>([&](auto J) { PIN(W1[J]); });
    __builtin_amdgcn_s_sleep(6);
    int vhl = blk * IN_BYTES + IN_H + u * 256 + r * 32 + (lane & 1) * 16, lwr = lane * 16 + u * 4;
    int idx = (lane & 1) ? 0xEEEEEEEE : 0x44444444;
    const f32x4 zero4 = {0.f, 0.f, 0.f, 0.f};
    f32x4 e[3];
    auto project = [&](u32x4 h0, u32x4 h1, int dst) {
      sfor<3>([&](auto T) { e[T] = smf(h0, W1[2 * T], e[T], idx); SB(); e[T] = smf(h1, W1[2 * T + 1], e[T], idx); SB(); });
      float a0 = e[0][0] + e[0][1], a1 = e[1][0] + e[1][1];
      a0 = r == 0 ? a0 : a1;
      PIN(a0);
      const float a2 = e[2][0] + e[2][1];
      KEEP(e[0]); KEEP(e[1]); KEEP(e[2]);
      *(LAS float*)(L + dst + lwr) = r >= 2 ? a2 : a0;
    };
    const u32x4 ones = {0x3f803f80u, 0x3f803f80u, 0x3f803f80u, 0x3f803f80u};
    sfor<3>([&](auto T) { e[T] = zero4; });
    project(ones, ones, L_EX);
    sfor<3>([&](auto T) { e[T] = zero4; PIN(e[T]); });
    PIN(vhl); PIN(lwr); PIN(idx);
    STAMP(6);
    BARRIER();
    if (DLY1) __builtin_amdgcn_s_sleep(DLY1);
    u32x4 h0, h1;
    for (unsigned spins = 0; spins < SPIN_CAP; ++spins) {
      asm volatile("" ::: "memory");
      h0 = __builtin_amdgcn_raw_buffer_load_b128(rws, vhl, 0, AUX_SC1);
      h1 = __builtin_amdgcn_raw_buffer_load_b128(rws, vhl, 128, AUX_SC1);
      if (__builtin_amdgcn_ballot_w64(max(max4(h0), max4(h1)) == SENT) == 0) break;
    }
    SB();
    STAMP(8); TSTAMP(6);
    project(h0, h1, L_EX2);
    if (TAILST == 1) TSTAMP(8);
    BARRIER();
  }
  }
}

extern "C" void kernel_launch(void* const* d_in, const int* in_sizes, int n_in, void* d_out, int out_size, void* d_ws,
                              size_t ws_size, hipStream_t stream) {
  fused_block<<<NB * NH, 768, 0, stream>>>((const unsigned char*)d_in[3], (const unsigned char*)d_in[1], (const unsigned char*)d_in[4],
                                      (const unsigned char*)d_in[2], (const unsigned short*)d_in[0], (const unsigned char*)d_in[6],
                                      (const unsigned char*)d_in[7], (const unsigned char*)d_in[8], (const unsigned short*)d_in[9],
                                      (unsigned short*)d_out, (unsigned char*)d_ws);
}
```

```cpp
#define PEND 2060
#define TCOM 1
#define TSTEP 11
#include <hip/hip_runtime.h>
#include <hip/hip_bf16.h>
#include <stdint.h>

#define STAMPS 0
#define PFCODE 0
#define WN 3
#define BWN 3
#define QB0 4
#define QRDY 9
#define QSTEP 28
#define OB0 14
#define MB0 35
#define QBS 8
#define OBS 12
#define MBS 12
#define NG 8
#define OWD 5
#define INB 8192
#define SENTH 0
#define SENTP 1
#define CHKS 61
#define PTAU 24
#define PBUR 90
#define PBUM 110
#define DITH 32
#define PSH 1
#define DITG 0
#define TFRAC 4
#define SKEW 0
#define PFROM 8
#define PTO 60
#define PEV 2
#define SENTE 1
#define LOSE -1
#define EARLYST 0
#define TOUCH 0
#define SENTW 0
#define TAILST 0
#define HST 1
#define EXPM 0
#define DLY0 20
#define DLY1 11
#define DLY2 18

typedef unsigned u32x4 __attribute__((ext_vector_type(4)));
typedef unsigned u32x2 __attribute__((ext_vector_type(2)));
typedef float f32x2 __attribute__((ext_vector_type(2)));
typedef float f32x4 __attribute__((ext_vector_type(4)));
typedef __bf16 bf16x2 __attribute__((ext_vector_type(2)));
typedef short s16x4 __attribute__((ext_vector_type(4)));
typedef unsigned u32x8 __attribute__((ext_vector_type(8)));
typedef __bf16 bf16x8 __attribute__((ext_vector_type(8)));
typedef __bf16 bf16x16 __attribute__((ext_vector_type(16)));
typedef __amdgpu_buffer_rsrc_t rsrc_t;
#define LAS __attribute__((address_space(3)))
#define DI __device__ __forceinline__

constexpr int NB = 8, D = 512, NH = 32, SK = 4096, DH = 128, WIN = 1024, F = 1376;
constexpr int FS = 48;
constexpr int AUX_NT = 2, AUX_SC1 = 16;
constexpr int OOB = 0x7ffffff0;
constexpr unsigned SENT = 0xffffffffu;
constexpr unsigned SPIN_CAP = 1u << 17;
constexpr int IN_P1 = 0;
constexpr int IN_P2 = 3072;
constexpr int IN_H = 2048;
constexpr int IN_BYTES = INB;
constexpr int WS_T = 256 * IN_BYTES;
constexpr int WS_BYTES = WS_T + 128;
constexpr int WS_STAMP = 4 * 1024 * 1024;
constexpr int L_RED = 0;
constexpr int L_RED2 = L_RED + 2048;
constexpr int L_EX = L_RED2 + 4096;
constexpr int L_EX2 = L_EX + 1024;
constexpr int L_ML = L_EX2 + 1024;
constexpr int L_ST = L_ML + 64;
constexpr int L_T = L_ST + 16;
constexpr int L_Y = 9216;
constexpr int L_DUMP = L_Y + 2048;
constexpr int L_W = L_DUMP + 1024;
constexpr int L_BYTES = L_W + 128 * 1024;

DI float dot2(unsigned a, unsigned b, float acc) {
  return __builtin_amdgcn_fdot2_f32_bf16(__builtin_bit_cast(bf16x2, a), __builtin_bit_cast(bf16x2, b), acc, false);
}
DI unsigned fbits(float f) { return __builtin_bit_cast(unsigned, f); }
DI float asf(unsigned u) { return __builtin_bit_cast(float, u); }
DI unsigned cvtpk(float lo, float hi) {
  bf16x2 r = {(__bf16)lo, (__bf16)hi};
  return __builtin_bit_cast(unsigned, r);
}
DI float rbf(float f) { return asf((unsigned)__builtin_bit_cast(unsigned short, (__bf16)f) << 16); }
template <int CTRL> DI unsigned dppu(unsigned x) { return __builtin_amdgcn_mov_dpp(x, CTRL, 0xf, 0xf, true); }
template <int CTRL> DI float dppf(float x) { return asf(dppu<CTRL>(fbits(x))); }
constexpr int XOR1 = 0xB1, XOR2 = 0x4E, HMIR = 0x141, ROR4 = 0x124, ROR8 = 0x128, BCAST = 0x150;
DI float tsum32(float a, float b) {
  auto s = __builtin_amdgcn_permlane32_swap(fbits(a), fbits(b), false, false);
  return asf(s[0]) + asf(s[1]);
}
DI float tsum16(float a, float b) {
  auto s = __builtin_amdgcn_permlane16_swap(fbits(a), fbits(b), false, false);
  return asf(s[0]) + asf(s[1]);
}
DI float row_sum(float x) { x += dppf<XOR1>(x); x += dppf<XOR2>(x); x += dppf<ROR4>(x); x += dppf<ROR8>(x); return x; }
DI float row_max(float x) { x = fmaxf(x, dppf<XOR1>(x)); x = fmaxf(x, dppf<XOR2>(x)); x = fmaxf(x, dppf<ROR4>(x)); x = fmaxf(x, dppf<ROR8>(x)); return x; }
DI float wave_sum(float x) { x = row_sum(x); x = tsum16(x, x); return tsum32(x, x); }
DI float wave_max(float x) {
  x = row_max(x);
  auto s = __builtin_amdgcn_permlane16_swap(fbits(x), fbits(x), false, false);
  x = fmaxf(asf(s[0]), asf(s[1]));
  auto t = __builtin_amdgcn_permlane32_swap(fbits(x), fbits(x), false, false);
  return fmaxf(asf(t[0]), asf(t[1]));
}
DI void rows_bcast(unsigned x, unsigned (&o)[4]) {
  auto s = __builtin_amdgcn_permlane16_swap(x, x, false, false);
  auto a = __builtin_amdgcn_permlane32_swap(s[0], s[0], false, false);
  auto b = __builtin_amdgcn_permlane32_swap(s[1], s[1], false, false);
  o[0] = a[0]; o[2] = a[1]; o[1] = b[0]; o[3] = b[1];
}
DI f32x4 smf(u32x4 a, u32x8 b, f32x4 c, int idx) {
  return __builtin_amdgcn_smfmac_f32_16x16x64_bf16(__builtin_bit_cast(bf16x8, a), __builtin_bit_cast(bf16x16, b), c, idx, 0, 0);
}
DI f32x4 mfma(u32x4 a, u32x4 b, f32x4 c) {
  return __builtin_amdgcn_mfma_f32_16x16x32_bf16(__builtin_bit_cast(bf16x8, a), __builtin_bit_cast(bf16x8, b), c, 0, 0, 0);
}
constexpr int SHL = 0x100;
template <class T> DI void pin_(T& x) { asm volatile("" : "+v"(x)); }
#define PIN(x) pin_(x)
template <class T> DI void keep_(const T& x) { asm volatile("" :: "v"(x)); }
#define KEEP(x) keep_(x)
template <class T> DI void pins_(T& x) { asm volatile("" : "+s"(x)); }
#define PINS(x) pins_(x)
#define SB() __builtin_amdgcn_sched_barrier(0)
#define PACEN(n) do { asm volatile("s_waitcnt vmcnt(%0)" :: "i"(n) : "memory"); SB(); } while (0)
#define PACE() PACEN(WN)
#define BARRIER() do { asm volatile("s_waitcnt lgkmcnt(0)\n\ts_barrier" ::: "memory"); SB(); } while (0)
DI rsrc_t mk_rsrc(const void* p, int bytes) { return __builtin_amdgcn_make_buffer_rsrc((void*)p, 0, bytes, 0x00020000); }

template <int G, int N, int CNT = N> DI void burst(int grp, rsrc_t r, int voff, int stride, int lds, int j0) {
  int keep, j, cnt, t;
  asm volatile(
      "s_cmp_lg_u32 %4, %10\n\t"
      "s_cbranch_scc1 BE%=\n\t"
      "s_mov_b32 %0, m0\n\t"
      "s_mov_b32 %1, %9\n\t"
      "s_mov_b32 %2, %12\n"
      "BL%=:\n\t"
      "s_lshl_b32 %3, %1, 10\n\t"
      "s_add_u32 m0, %3, %8\n\t"
      "s_mul_i32 %3, %1, %7\n\t"
      "s_add_u32 %1, %1, 1\n\t"
      "s_waitcnt vmcnt(%13)\n\t"
      "buffer_load_dwordx4 %6, %5, %3 offen lds\n\t"
      "s_cmp_eq_u32 %1, %11\n\t"
      "s_cselect_b32 %1, 0, %1\n\t"
      "s_sub_u32 %2, %2, 1\n\t"
      "s_cmp_lg_u32 %2, 0\n\t"
      "s_cbranch_scc1 BL%=\n\t"
      "s_mov_b32 m0, %0\n"
      "BE%=:"
      : "=&s"(keep), "=&s"(j), "=&s"(cnt), "=&s"(t)
      : "s"(grp), "s"(r), "v"(voff), "s"(stride), "s"(lds), "s"(j0), "i"(G), "i"(N), "i"(CNT), "i"(BWN)
      : "memory", "scc");
  SB();
}
DI void ald128nt(u32x4& d, rsrc_t r, int voff, int soff) { asm volatile("buffer_load_dwordx4 %0, %1, %2, %3 offen nt" : "=v"(d) : "v"(voff), "s"(r), "s"(soff) : "memory"); SB(); }
DI void ald128(u32x4& d, rsrc_t r, int voff, int soff) { asm volatile("buffer_load_dwordx4 %0, %1, %2, %3 offen" : "=v"(d) : "v"(voff), "s"(r), "s"(soff) : "memory"); SB(); }
DI void ald64sc(u32x2& d, rsrc_t r, int voff, int soff) { asm volatile("buffer_load_dwordx2 %0, %1, %2, %3 offen sc1" : "=v"(d) : "v"(voff), "s"(r), "s"(soff) : "memory"); SB(); }
DI void ald32sc(unsigned& d, rsrc_t r, int voff) { asm volatile("buffer_load_dword %0, %1, %2, 0 offen sc1" : "=v"(d) : "v"(voff), "s"(r) : "memory"); SB(); }
DI void ald32(unsigned& d, rsrc_t r, int voff) { asm volatile("buffer_load_dword %0, %1, %2, 0 offen" : "=v"(d) : "v"(voff), "s"(r) : "memory"); SB(); }
DI void ald16(unsigned& d, rsrc_t r, int voff, int soff) { asm volatile("buffer_load_ushort %0, %1, %2, %3 offen" : "=v"(d) : "v"(voff), "s"(r), "s"(soff) : "memory"); SB(); }
template <bool NT = false> DI void dma1(rsrc_t r, int voff, int soff, int lds) {
  int keep;
  if constexpr (NT)
    asm volatile("s_mov_b32 %0, m0\n\ts_mov_b32 m0, %4\n\ts_nop 0\n\tbuffer_load_dwordx4 %1, %2, %3 offen nt lds\n\ts_mov_b32 m0, %0"
                 : "=&s"(keep) : "v"(voff), "s"(r), "s"(soff), "s"(lds) : "memory");
  else
    asm volatile("s_mov_b32 %0, m0\n\ts_mov_b32 m0, %4\n\ts_nop 0\n\tbuffer_load_dwordx4 %1, %2, %3 offen lds\n\ts_mov_b32 m0, %0"
                 : "=&s"(keep) : "v"(voff), "s"(r), "s"(soff), "s"(lds) : "memory");
  SB();
}
template <int G> DI void cond_barrier(int grp) {
  asm volatile("s_cmp_lg_u32 %0, %1\n\ts_cbranch_scc1 CB%=\n\ts_barrier\nCB%=:" :: "s"(grp), "i"(G) : "memory", "scc");
  SB();
}
DI float gather16(rsrc_t r, int voff);
DI u32x2 tr_read(LAS unsigned char* p) {
  return __builtin_bit_cast(u32x2, __builtin_amdgcn_ds_read_tr16_b64_v4i16((LAS s16x4*)p));
}
template <int M1, int M2> DI u32x8 tr_read8(LAS unsigned char* p) {
  const u32x2 a = tr_read(p), b = tr_read(p + M1), c = tr_read(p + M2), d = tr_read(p + M1 + M2);
  return u32x8{a[0], a[1], b[0], b[1], c[0], c[1], d[0], d[1]};
}
DI f32x2 fold_rows8(const float (&acc)[8]) {
  const float t0 = tsum32(acc[0], acc[4]), t1 = tsum32(acc[1], acc[5]), t2 = tsum32(acc[2], acc[6]), t3 = tsum32(acc[3], acc[7]);
  return f32x2{tsum16(t0, t2), tsum16(t1, t3)};
}
DI float fold_rows4(float a0, float a1, float a2, float a3) { return tsum16(tsum32(a0, a2), tsum32(a1, a3)); }
DI unsigned max4(u32x4 v) { return max(max(v[0], v[1]), max(v[2], v[3])); }

DI float gather16(rsrc_t r, int voff) {
  u32x4 A, Bv;
  for (unsigned spins = 0; spins < SPIN_CAP; ++spins) {
    asm volatile("" ::: "memory");
    A = __builtin_amdgcn_raw_buffer_load_b128(r, voff, 0, AUX_SC1);
    Bv = __builtin_amdgcn_raw_buffer_load_b128(r, voff, 1024, AUX_SC1);
    if (__builtin_amdgcn_ballot_w64(max(max4(A), max4(Bv)) == SENT) == 0) break;
  }
  const f32x4 s = __builtin_bit_cast(f32x4, A) + __builtin_bit_cast(f32x4, Bv);
  float t = fold_rows4(s[0], s[1], s[2], s[3]);
  t += dppf<ROR4>(t); t += dppf<ROR8>(t);
  return t;
}

template <int I> DI void pv_step(float (&acc)[8], unsigned plo, unsigned phi, u32x4 v) {
  const unsigned lo = dppu<BCAST + I>(plo), hi = dppu<BCAST + I>(phi);
#pragma unroll
  for (int k = 0; k < 4; ++k) { acc[2 * k] = dot2(lo, v[k], acc[2 * k]); acc[2 * k + 1] = dot2(hi, v[k], acc[2 * k + 1]); }
}
DI void pin8(float (&a)[8]) {
  asm volatile("" : "+v"(a[0]), "+v"(a[1]), "+v"(a[2]), "+v"(a[3]), "+v"(a[4]), "+v"(a[5]), "+v"(a[6]), "+v"(a[7]));
}
template <int... Is> struct iseq {};
template <int N, int... Is> struct mkseq : mkseq<N - 1, N - 1, Is...> {};
template <int... Is> struct mkseq<0, Is...> { typedef iseq<Is...> type; };
template <class Fn, int... Is> DI void sfor_(Fn&& f, iseq<Is...>) { (f(std::integral_constant<int, Is>{}), ...); }
template <int N, class Fn> DI void sfor(Fn&& f) { sfor_(f, typename mkseq<N>::type{}); }

DI float transpose_reduce16(float (&x)[16], int c) {
#define TR_STEP(HALF, CTRL, BIT) _Pragma("unroll") for (int h_ = 0; h_ < (HALF); ++h_) { \
    const float lo_ = x[h_], hi_ = x[h_ + (HALF)]; const float t1_ = lo_ + dppf<CTRL>(lo_), t2_ = hi_ + dppf<CTRL>(hi_); \
    x[h_] = (c & (BIT)) ? t2_ : t1_; }
  TR_STEP(8, ROR8, 8) TR_STEP(4, HMIR, 4) TR_STEP(2, XOR2, 2) TR_STEP(1, XOR1, 1)
#undef TR_STEP
  return x[0];
}

#define STAMP(i) do {} while (0)
#define ESTAMP(i) do {} while (0)
#define TSTAMP(i) do {} while (0)

extern "C" __global__ void __launch_bounds__(768, 3)
fused_block(const unsigned char* __restrict__ wq, const unsigned char* __restrict__ kc, const unsigned char* __restrict__ wo,
            const unsigned char* __restrict__ vc, const unsigned short* __restrict__ x, const unsigned char* __restrict__ w1,
            const unsigned char* __restrict__ b1, const unsigned char* __restrict__ w2, const unsigned short* __restrict__ b2,
            unsigned short* __restrict__ out, unsigned char* ws) {
  __shared__ __attribute__((aligned(16))) unsigned char smem[L_BYTES];
  LAS unsigned char* L = (LAS unsigned char*)smem;
  if (SKEW && (blockIdx.x & 7) == 5) __builtin_amdgcn_s_sleep(SKEW);
  const unsigned long long xt0 = __builtin_amdgcn_s_memrealtime();
  const int blk = blockIdx.x, b = blk >> 5, h = blk & 31;
  const int tid = threadIdx.x, lane = tid & 63, r = lane >> 4, c = lane & 15;
  const int w = __builtin_amdgcn_readfirstlane(tid >> 6);
  const int slice = (h & 7) * 4 + (h >> 3), col0 = slice * FS;
#define IC(n) std::integral_constant<int, (n)>{}
  if (LOSE >= 0 && (blk == LOSE || blk == LOSE + 5)) for (int i = 0; i < (blk == LOSE ? 12 : 30); ++i) __builtin_amdgcn_s_sleep(127);
  unsigned t0 = 0;
  {
    int w2 = w;
    PINS(w2);
    if (w2 >= 8) {
      t0 = (unsigned)__builtin_amdgcn_s_memrealtime();
      const rsrc_t r0 = mk_rsrc(ws, WS_BYTES);
      u32x4 s4 = {SENT, SENT, SENT, SENT};
      PIN(s4);
      if (!(EXPM & 1)) __builtin_amdgcn_raw_buffer_store_b128(s4, r0, w2 >= 9 ? lane * 16 : OOB, blk * IN_BYTES + (w2 & 3) * 1024 - (w2 >= 9 ? 1024 : 0), AUX_SC1);
      if (TCOM) __builtin_amdgcn_raw_buffer_store_b32(t0, r0, (tid == 512 && blk < 8) ? blk * 4 : OOB, WS_T, AUX_SC1);
      SB(); KEEP(s4);
    }
    SB();
  }
  if (w >= 8) {
  const int u = w - 8;
  const rsrc_t rws = mk_rsrc(ws, WS_BYTES);
  const u32x4 xrow = *(const u32x4*)(x + b * D + lane * 8);
  const float xe0 = asf((unsigned)x[b * D + 128 * u + 4 * c + r] << 16), xe1 = asf((unsigned)x[b * D + 128 * u + 64 + 4 * c + r] << 16);
  float b1v = asf((unsigned)__builtin_amdgcn_raw_buffer_load_b16(mk_rsrc(b1, F * 2), lane < FS ? (col0 + lane) * 2 : OOB, 0, 0) << 16);
  if (PFCODE && b == 0 && u >= 2) {
    unsigned long long pc; int o0, o1;
    asm volatile("s_getpc_b64 %0\nKPC0:\n\ts_mov_b32 %1, KPC0-KPCA\n\ts_mov_b32 %2, KPCB-KPCA" : "=s"(pc), "=s"(o0), "=s"(o1));
    const rsrc_t rc = mk_rsrc((const void*)(pc - (long long)o0), o1);
    unsigned p0 = __builtin_amdgcn_raw_buffer_load_b32(rc, lane * 128, (u - 2) * 8192, 0);
    PIN(p0);
  }
  SB();
  {
    float xf[8];
#pragma unroll
    for (int k = 0; k < 4; ++k) { xf[2 * k] = asf(xrow[k] << 16); xf[2 * k + 1] = asf(xrow[k] & 0xffff0000u); }
    const float mean = wave_sum(((xf[0] + xf[1]) + (xf[2] + xf[3])) + ((xf[4] + xf[5]) + (xf[6] + xf[7]))) * (1.f / D);
    float vs = 0.f;
#pragma unroll
    for (int k = 0; k < 8; ++k) { const float d = xf[k] - mean; vs = fmaf(d, d, vs); }
    const float var = wave_sum(vs) * (1.f / D);
    const float den = rbf(sqrtf(rbf(rbf(var) + 1e-5f))), mb = rbf(mean);
    const unsigned xp = cvtpk(rbf(xe0 - mb) / den, rbf(xe1 - mb) / den);
    unsigned xlo[2] = {xp & 0xffffu, xp >> 16}, xhi[2] = {xp << 16, xp & 0xffff0000u};
    PIN(xlo[0]); PIN(xlo[1]); PIN(xhi[0]); PIN(xhi[1]); PIN(b1v);
    PACEN(0);
    ESTAMP(5);
    BARRIER();
    ESTAMP(6);
    float acc[8] = {0.f, 0.f, 0.f, 0.f, 0.f, 0.f, 0.f, 0.f};
    sfor<32>([&](auto I) { pv_step<I & 15>(acc, xlo[I >> 4], xhi[I >> 4], *(const LAS u32x4*)(L + L_W + u * 32768 + I * 1024 + lane * 16)); });
    *(LAS f32x2*)(L + L_RED + (u >> 1) * 1024 + lane * 16 + (u & 1) * 8) = fold_rows8(acc);
  }
  STAMP(11);
  BARRIER();
  BARRIER();
  u32x8 WO[16];
  const int q4 = (lane >> 2) & 3;
  LAS unsigned char* lw = L + L_W + u * 32768 + (lane & 3) * 8;
  {
    LAS unsigned char* p = lw + (32 * (r >> 1) + 4 * (r & 1) + (q4 >> 1) + 8 * (q4 & 1)) * 128;
    sfor<16>([&](auto J) { WO[J] = tr_read8<2 * 128, 64 * 128>(p + (J >> 3) * 16384 + ((J >> 1) & 3) * 32 + (J & 1) * 16 * 128); });
    sfor<16>([&](auto J) { PIN(WO[J]); });
  }
  BARRIER();
  BARRIER();
  int vp = lane < 16 ? (b * 32 + 8 * u + (lane >> 2)) * IN_BYTES + (lane & 3) * 16 : OOB;
  int lrd = lane * 16;
  int idx = (lane & 1) ? 0xEEEEEEEE : 0x44444444;
  PIN(vp); PIN(lrd); PIN(idx);
  const f32x4 zero4 = {0.f, 0.f, 0.f, 0.f};
  f32x4 d[8];
  d[0] = zero4; d[1] = zero4; PIN(d[0]); PIN(d[1]);
  int so1 = IN_P1 + h * 64, so2 = IN_P1 + h * 64 + 4 * IN_BYTES;
  PINS(so1); PINS(so2);
  BARRIER();
  TSTAMP(2);
  {
    f32x4 s4 = *(const LAS f32x4*)(L + L_RED2 + lrd) + *(const LAS f32x4*)(L + L_RED2 + 1024 + lrd);
    s4 += *(const LAS f32x4*)(L + L_RED2 + 2048 + lrd) + *(const LAS f32x4*)(L + L_RED2 + 3072 + lrd);
    const unsigned op = cvtpk(s4[0] + s4[2], s4[1] + s4[3]);
    const u32x4 a0 = {op, dppu<SHL + 2>(op), dppu<SHL + 4>(op), dppu<SHL + 6>(op)};
    const u32x4 a1 = {dppu<SHL + 8>(op), dppu<SHL + 10>(op), dppu<SHL + 12>(op), dppu<SHL + 14>(op)};
    float y[8];
    auto fin = [&](auto T) { y[T] = d[T][0] + d[T][1]; PIN(y[T]); KEEP(d[T]); };
    sfor<8>([&](auto T) {
      d[T] = smf(a0, WO[2 * T], d[T], idx);
      if constexpr (T > 0) SB();
      if constexpr (T + 2 < 8 && T > 0) { d[T + 2] = zero4; PIN(d[T + 2]); }
      SB();
      d[T] = smf(a1, WO[2 * T + 1], d[T], idx);
      SB();
      if constexpr (T == 0) { d[2] = zero4; PIN(d[2]); }
      if constexpr (T >= 2) fin(IC(T - 2));
      if constexpr (T == 6)
        __builtin_amdgcn_raw_buffer_store_b128(u32x4{fbits(y[0]), fbits(y[1]), fbits(y[2]), fbits(y[3])}, rws, (EXPM & 8) ? OOB : vp, so1, AUX_SC1);
      SB();
    });
    fin(IC(6)); fin(IC(7));
    __builtin_amdgcn_raw_buffer_store_b128(u32x4{fbits(y[4]), fbits(y[5]), fbits(y[6]), fbits(y[7])}, rws, (EXPM & 8) ? OOB : vp, so2, AUX_SC1);
    SB();
    if (lrd < 256) {
      *(LAS f32x4*)(L + L_Y + u * 512 + lrd) = f32x4{y[0], y[1], y[2], y[3]};
      *(LAS f32x4*)(L + L_Y + u * 512 + 256 + lrd) = f32x4{y[4], y[5], y[6], y[7]};
    }
  }
  SB();
  STAMP(5); TSTAMP(3);
  u32x8 W2[8];
  {
    const int ln = __builtin_amdgcn_mbcnt_hi(~0u, __builtin_amdgcn_mbcnt_lo(~0u, 0u)), rr = ln >> 4, qq = (ln >> 2) & 3;
    LAS unsigned char* p2 = L + L_W + u * 32768 + (ln & 3) * 8 + 8192 + (16 * (rr >> 1) + 8 * (rr & 1) + (qq >> 1) + 2 * (qq & 1)) * 128;
    LAS unsigned char* p3 = p2 + (32 - 16 * (rr >> 1)) * 128;
    sfor<8>([&](auto J) {
      constexpr int o = (J >> 2) * 16384 + (J & 3) * 32;
      const u32x2 t0 = tr_read(p2 + o), t1 = tr_read(p2 + o + 4 * 128), t2 = tr_read(p3 + o), t3 = tr_read(p3 + o + 4 * 128);
      W2[J] = u32x8{t0[0], t0[1], t1[0], t1[1], t2[0], t2[1], t3[0], t3[1]};
    });
    sfor<8>([&](auto J) { PIN(W2[J]); d[J] = zero4; PIN(d[J]); });
  }
  so1 = IN_P2 + slice * 64; so2 = IN_P2 + slice * 64 + 4 * IN_BYTES;
  int lst = L_ST;
  float gk = -2.f * 1.4426950408889634f * 0.7978845608028654f;
  unsigned gmask = lrd < FS * 16 ? ~0u : 0u;
  PINS(so1); PINS(so2); PIN(lst); PIN(gk); PIN(gmask);
  SB();
  STAMP(6);
  BARRIER();
  float c1;
  { const f32x4 t = *(const LAS f32x4*)(L + L_EX + lrd); c1 = (t[0] + t[1]) + (t[2] + t[3]); }
  PIN(c1);
  BARRIER();
  if (TAILST == 2) TSTAMP(8);
  {
    const f32x4 t4 = *(const LAS f32x4*)(L + L_EX2 + lrd);
    const f32x2 ms = *(const LAS f32x2*)(L + lst);
    const float tot = (t4[0] + t4[1]) + (t4[2] + t4[3]);
    const float uu = rbf(rbf(fmaf(-ms[0], c1, tot) * ms[1]) + b1v);
    const float z = uu * fmaf(uu * uu, -2.f * 1.4426950408889634f * 0.044715f * 0.7978845608028654f, gk);
    const float g = asf(fbits(uu) & gmask) * __builtin_amdgcn_rcpf(1.f + __builtin_amdgcn_exp2f(z));
    const unsigned gp = cvtpk(g, dppf<XOR2>(g));
    const u32x4 ag = {gp, dppu<SHL + 4>(gp), dppu<SHL + 8>(gp), dppu<SHL + 12>(gp)};
    if (TAILST == 2) { KEEP(ag); TSTAMP(9); }
    float y[8];
    auto fin = [&](auto T) { y[T] = d[T][0] + d[T][1]; PIN(y[T]); KEEP(d[T]); SB(); };
    sfor<8>([&](auto T) { d[T] = smf(ag, W2[T], d[T], idx); SB(); });
    fin(IC(0)); fin(IC(1)); fin(IC(2)); fin(IC(3));
    __builtin_amdgcn_raw_buffer_store_b128(u32x4{fbits(y[0]), fbits(y[1]), fbits(y[2]), fbits(y[3])}, rws, vp, so1, AUX_SC1);
    SB();
    fin(IC(4)); fin(IC(5)); fin(IC(6)); fin(IC(7));
    KEEP(y[0]); KEEP(y[1]); KEEP(y[2]); KEEP(y[3]);
    __builtin_amdgcn_raw_buffer_store_b128(u32x4{fbits(y[4]), fbits(y[5]), fbits(y[6]), fbits(y[7])}, rws, vp, so2, AUX_SC1);
  }
  asm volatile("KPCB:");
  SB();
  STAMP(9); TSTAMP(10);
  } else {
  const int grp = NG == 8 ? (b + 2 * (h >> 3)) & 7 : (NG == 16 ? (2 * b + 4 * (h >> 4) + ((h >> 3) & 1)) & 15 : 4 * b + (h >> 3));
  const int pq = QB0 + ((grp * QBS) >> 2), po = OB0 + ((grp * OBS) >> 2), pm = MB0 + ((grp * MBS) >> 2);
  const size_t kvoff = ((size_t)blk * SK + (SK - WIN)) * (DH * 2);
  const rsrc_t rk = mk_rsrc(kc + kvoff, WIN * DH * 2), rv = mk_rsrc(vc + kvoff, WIN * DH * 2);
  const int vkv = tid * 16;
  const int lds0 = (int)(unsigned)(size_t)L, ldsw = lds0 + L_W + w * 16384, ldsd = lds0 + L_DUMP;

  u32x4 K[32], V[32];
  auto ldk = [&](auto I) { ald128nt(K[I], rk, vkv, I * 8192); };
  auto ldv = [&](auto I) { ald128nt(V[I], rv, vkv, I * 8192); };
  ldk(IC(0)); ldk(IC(1));
  const unsigned long long tclk = __builtin_amdgcn_s_memrealtime();
  SB();
  asm volatile("KPCA:");
  const rsrc_t rq = mk_rsrc(wq + h * (DH * 2), D * NH * DH * 2 - h * (DH * 2));
  const int vq = (64 * w + r) * (NH * DH * 2) + c * 16;
  dma1(rq, vq, 2 * b * (4 * NH * DH * 2), ldsw + 2 * b * 1024);
  dma1(rq, vq, (2 * b + 1) * (4 * NH * DH * 2), ldsw + (2 * b + 1) * 1024);
  unsigned tch = 0;
  if (TOUCH) {
    const unsigned char* p = w == 0 ? vc + kvoff : w == 1 ? wo + (size_t)h * (DH * D * 2) : w == 2 ? w1 + 256 * F * 2 : w == 3 ? w2 + (size_t)min(col0, F - 1) * (D * 2)
                           : w == 4 ? ws + blk * IN_BYTES + 5120 : w == 5 ? (const unsigned char*)out : w == 6 ? (const unsigned char*)b2 : b1;
    ald32(tch, mk_rsrc(p, 4), lane == 0 ? 0 : OOB);
  }
  const rsrc_t ro = mk_rsrc(wo + (size_t)h * (DH * D * 2), DH * D * 2);
  const int vo = (lane >> 3) * (D * 2) + w * 128 + (lane & 7) * 16;
  const rsrc_t r1 = mk_rsrc(w1, D * F * 2);
  const int v1 = ((lane & 7) < 6 && col0 + (lane & 7) * 8 < F) ? (64 * w + (lane >> 3)) * (F * 2) + col0 * 2 + (lane & 7) * 16 : OOB;
  const int c2 = min(col0, F);
  const rsrc_t r2 = mk_rsrc(w2 + (size_t)c2 * (D * 2), (F - c2) * (D * 2));
  const int b6 = b < 6 ? b : b - 6;
  const int dith = (((w - 4) * DITH) >> 3) + ((((blk >> 3) - 16) * DITG) >> 5);
  unsigned tst = PEND ? (unsigned)tclk + dith : 0u;
  auto hook = [&](auto I) {
    constexpr int i = I;
    if constexpr (PEND && i >= PFROM && i <= PTO && i % PEV == 0) {
      PINS(tst);
      const int pend = (i <= pq ? PBUR : 0) + (i <= po ? PBUR : 0) + (i <= pm ? PBUM : 0);
      constexpr int extra = (i <= OB0 - OWD ? 2 : 0) + (i <= MB0 - OWD ? 2 : 0);
      const unsigned tgt = tst + PEND - (67 - i + extra) * PTAU - pend;
      int tmp;
      asm volatile("s_memrealtime vcc\n\ts_waitcnt lgkmcnt(0)\n\ts_sub_i32 %0, %1, vcc_lo\n\ts_ashr_i32 %0, %0, %2\n\ts_cmp_lt_i32 %0, 3\n\ts_cbranch_scc1 PE%=\n\t"
                   "s_add_i32 %0, %0, vcc_lo\n"
                   "PC%=:\n\ts_memrealtime vcc\n\ts_waitcnt lgkmcnt(0)\n\ts_sub_i32 vcc_lo, vcc_lo, %0\n\ts_cmp_lt_i32 vcc_lo, 0\n\ts_cbranch_scc1 PC%=\nPE%=:"
                   : "=&s"(tmp) : "s"(tgt), "i"(PSH) : "scc", "vcc", "memory");
      SB();
    }
    constexpr auto site = [](int i_, int p0, int sp) { for (int g = 0; g < NG; ++g) if (p0 + ((g * sp) >> 2) == i_) return true; return false; };
    if constexpr (site(i - 4, QB0, QBS)) cond_barrier<i - 4>(pq);
    if constexpr (site(i - QRDY, QB0, QBS)) cond_barrier<i - QRDY>(pq);
    if constexpr (site(i - 4, OB0, OBS)) cond_barrier<i - 4>(po);
    if constexpr (site(i, MB0, MBS)) cond_barrier<i>(pm);
    if constexpr (site(i, QB0, QBS)) burst<i, 16, 14>(pq, rq, vq, 4 * NH * DH * 2, ldsw, (2 * b + 2) & 15);
    if constexpr (i == OB0 - OWD) { PACE(); dma1(ro, vo, 2 * b * 8192, ldsd); PACE(); dma1(ro, vo, (2 * b + 1) * 8192, ldsd); }
    if constexpr (site(i, OB0, OBS)) burst<i, 16>(po, ro, vo, 8 * D * 2, ldsw, 2 * b);
    if constexpr (i == MB0 - OWD) { PACE(); dma1(r1, v1, b * (8 * F * 2), ldsd); PACE(); dma1(r2, vo, b * (8 * D * 2), ldsd); }
    if constexpr (site(i, MB0, MBS)) {
      burst<i, 8>(pm, r1, v1, 8 * F * 2, ldsw, b);
      burst<i, 6>(pm, r2, vo, 8 * D * 2, ldsw + 8192, b6);
    }
  };
  static_assert(QRDY > 4 && QB0 + (NG - 1) * QBS / 4 + QRDY < QSTEP && OBS >= QBS && OB0 >= QB0 + QRDY && MB0 > OB0 + 4 &&
                MB0 + (NG - 1) * MBS / 4 > OB0 + (NG - 1) * OBS / 4 + 5 && MB0 + (NG - 1) * MBS / 4 <= CHKS && CHKS >= 52 && CHKS <= 63, "barrier order, staging area reuse");
  unsigned tx;
  constexpr int S0 = 2;
  sfor<QSTEP - S0>([&](auto I) { constexpr int i = I + S0; if constexpr (i == 11) ESTAMP(7); if constexpr (i == 2) ESTAMP(8); if constexpr (i == 3) ESTAMP(9); hook(IC(i));
    PACEN(WN + (TCOM && i > TSTEP && i < TSTEP + 4 ? 1 : 0)); ldk(IC(i));
    if constexpr (TCOM && i == TSTEP) ald32sc(tx, mk_rsrc(ws + WS_T, 32), lane * 4);
    if constexpr (TCOM && i == TSTEP + 4) {
      PIN(tx);
      int dt = (int)(tx - (unsigned)tclk);
      dt = (dt > -400 && dt < 400) ? dt : 0;
      dt += (int)dppu<XOR1>(dt); dt += (int)dppu<XOR2>(dt); dt += (int)dppu<HMIR>(dt);
      tst += __builtin_amdgcn_readfirstlane((dt * TFRAC) >> 5);
      SB();
    }
  });
  PIN(tch);
  STAMP(1);
  unsigned qp[4];
  {
    const f32x4 s4 = *(const LAS f32x4*)(L + L_RED + lane * 16) + *(const LAS f32x4*)(L + L_RED + 1024 + lane * 16);
    rows_bcast(cvtpk(s4[0] + s4[2], s4[1] + s4[3]), qp);
  }
  SB();
  STAMP(2);
  float sc[2], xs[16];
  auto qk = [&](u32x4& kv) { PIN(kv); return dot2(kv[3], qp[3], dot2(kv[2], qp[2], dot2(kv[1], qp[1], dot2(kv[0], qp[0], 0.f)))); };
  static_assert(QSTEP <= 28, "");
  sfor<36 - QSTEP>([&](auto I) {
    constexpr int i = I + QSTEP;
    hook(IC(i));
    PACE();
    if constexpr (i < 32) ldk(IC(i)); else ldv(IC(i - 32));
    if constexpr (I < 3) sfor<(I < 2 ? 6 : 4)>([&](auto J) { xs[6 * I + J] = qk(K[6 * I + J]); PIN(xs[6 * I + J]); });
    if constexpr (I == 3) { sc[0] = transpose_reduce16(xs, c); PIN(sc[0]); }
    if constexpr (I >= 4) {
      constexpr int lo = (I == 4) ? 16 : (16 + 6 * (I - 4) < i - 4 ? 16 + 6 * (I - 4) : i - 4);
      constexpr int hi = (16 + 6 * (I - 3) < i - 3) ? 16 + 6 * (I - 3) : i - 3;
      sfor<hi - lo>([&](auto J) { xs[lo - 16 + J] = qk(K[lo + J]); PIN(xs[lo - 16 + J]); });
    }
    SB();
  });
  sc[1] = transpose_reduce16(xs, c);
  SB();
  hook(IC(36)); PACE(); ldv(IC(4));
  unsigned pp[2];
  float mw, e1;
  {
    const float s0 = rbf(rbf(sc[0]) * 0.08838834764831845f), s1 = rbf(rbf(sc[1]) * 0.08838834764831845f);
    mw = wave_max(fmaxf(s0, s1));
    const float e0 = __builtin_amdgcn_exp2f((s0 - mw) * 1.4426950408889634f);
    e1 = __builtin_amdgcn_exp2f((s1 - mw) * 1.4426950408889634f);
    pp[0] = cvtpk(e0, e0); pp[1] = cvtpk(e1, e1);
    SB();
    hook(IC(37)); PACE(); ldv(IC(5));
    *(LAS f32x2*)(L + L_ML + w * 8) = f32x2{mw, wave_sum(e0 + e1)};
  }
  SB();
  STAMP(3);
  f32x4 acc = {0.f, 0.f, 0.f, 0.f};
  u32x4 sel;
#pragma unroll
  for (int k = 0; k < 4; ++k) sel[k] = ((c & 7) >> 1) == k ? ((c & 1) ? 0xffff0000u : 0xffffu) : 0u;
  auto pv = [&](auto I) { PIN(V[I]); const unsigned pb = dppu<BCAST + (I & 15)>(pp[I >> 4]); acc = mfma(V[I], sel & pb, acc); PIN(acc); };
  const int ocol = 64 * (h >> 2) + 16 * r + 4 * (h & 3) + (c & 3);
  unsigned b2v, xo;
  int lw2 = (w >> 1) * 1024 + lane * 16 + (w & 1) * 8;
  u32x4 blast;
  sfor<29>([&](auto I) {
    constexpr int i = I + 6;
    hook(IC(i + 32));
    if constexpr (i < 32) { PACE(); ldv(IC(i)); }
    else PACEN(35 - i);
    if constexpr (i == 24) { ald16(b2v, mk_rsrc(b2, D * 2), w == 0 ? ocol * 2 : OOB, 0); ald16(xo, mk_rsrc(x, NB * D * 2), w == 0 ? ocol * 2 : OOB, b * (D * 2)); PIN(lw2); }
    if constexpr (i == 6) { pv(IC(0)); pv(IC(1)); }
    if constexpr (i + 32 == CHKS) {
      BARRIER();
      const f32x2 ml = *(const LAS f32x2*)(L + L_ML + (lane & 7) * 8);
      float M = ml[0];
      M = fmaxf(M, dppf<XOR1>(M)); M = fmaxf(M, dppf<XOR2>(M)); M = fmaxf(M, dppf<HMIR>(M));
      float Ls = ml[1] * __builtin_amdgcn_exp2f((ml[0] - M) * 1.4426950408889634f);
      Ls += dppf<XOR1>(Ls); Ls += dppf<XOR2>(Ls); Ls += dppf<HMIR>(Ls);
      const float scale = __builtin_amdgcn_exp2f((mw - M) * 1.4426950408889634f) / Ls;
      acc *= scale;
      pp[1] = cvtpk(e1 * scale, e1 * scale);
    }
    pv(IC(i - 4));
    SB();
  });
  blast = sel & dppu<BCAST + 15>(pp[1]);
  PIN(blast);
  PACEN(0);
  STAMP(4); TSTAMP(1);
  PIN(V[31]);
  acc = mfma(V[31], blast, acc);
  *(LAS f32x2*)(L + L_RED2 + lw2) = (c & 8) ? f32x2{acc[2], acc[3]} : f32x2{acc[0], acc[1]};
  BARRIER();
  PIN(b2v); PIN(xo);
  if (w == 0) {
    const rsrc_t rws = mk_rsrc(ws, WS_BYTES);
    int vin = blk * IN_BYTES + lane * 16;
    if (SENTP) {
      u32x4 sent4 = {SENT, SENT, SENT, SENT};
      PIN(sent4);
      __builtin_amdgcn_raw_buffer_store_b128(sent4, rws, vin, IN_P2, AUX_SC1);
      __builtin_amdgcn_raw_buffer_store_b128(sent4, rws, vin, IN_P2 + 1024, AUX_SC1);
      asm volatile("s_nop 1" :: "v"(sent4) : "memory");
    }
    int vhs = HST == 0 ? (b * 32 + c) * IN_BYTES + IN_H + h * 32 + r * 8 : (b * 32 + (lane >> 1)) * IN_BYTES + IN_H + h * 32 + (lane & 1) * 16;
    int vout = c < 4 ? (b * D + ocol) * 2 : OOB;
    b2v <<= 16; xo <<= 16;
    PIN(vin); PIN(vhs); PIN(vout); PIN(b2v); PIN(xo);
    if (DLY0) __builtin_amdgcn_s_sleep(DLY0);
    PACEN(0);
    const float hf = asf(xo) + rbf(gather16(rws, vin + IN_P1));
    const unsigned hq = min(cvtpk(hf, dppf<XOR1>(hf)), SENT - 1u);
    unsigned hb[4];
    rows_bcast(dppu<0x88>(hq), hb);
    __builtin_amdgcn_raw_buffer_store_b128(u32x4{hb[0], hb[1], hb[2], hb[3]}, rws, vhs, 0, AUX_SC1);
    SB();
    STAMP(7); TSTAMP(5);
    BARRIER();
    float hres = asf(hq << 16);
    PIN(hres);
    if (DLY1) __builtin_amdgcn_s_sleep(DLY1);
    u32x4 hr;
    for (unsigned spins = 0; spins < SPIN_CAP; ++spins) {
      asm volatile("" ::: "memory");
      hr = __builtin_amdgcn_raw_buffer_load_b128(rws, vin, IN_H, AUX_SC1);
      if (__builtin_amdgcn_ballot_w64(max4(hr) == SENT) == 0) break;
      if ((spins & 63u) == 63u) {
        const int vr = (b * 32 + (lane >> 2)) * IN_BYTES + IN_P1 + h * 64 + (lane & 3) * 16;
        __builtin_amdgcn_raw_buffer_store_b128(*(const LAS u32x4*)(L + L_Y + lane * 16), rws, vr, 0, AUX_SC1);
        __builtin_amdgcn_raw_buffer_store_b128(*(const LAS u32x4*)(L + L_Y + 1024 + lane * 16), rws, vr, 16 * IN_BYTES, AUX_SC1);
      }
    }
    TSTAMP(7);
    {
      const float s1 = dot2(hr[3], 0x3f803f80u, dot2(hr[2], 0x3f803f80u, dot2(hr[1], 0x3f803f80u, dot2(hr[0], 0x3f803f80u, 0.f))));
      const float s2 = dot2(hr[3], hr[3], dot2(hr[2], hr[2], dot2(hr[1], hr[1], dot2(hr[0], hr[0], 0.f))));
      float t = tsum32(s1, s2);
      t = row_sum(tsum16(t, t));
      const float mean = asf(__builtin_amdgcn_readlane(fbits(t), 0)) * (1.f / D);
      const float var = fmaxf(fmaf(-mean, mean, asf(__builtin_amdgcn_readlane(fbits(t), 32)) * (1.f / D)), 0.f);
      const float den = rbf(__builtin_amdgcn_sqrtf(rbf(rbf(var) + 1e-5f)));
      *(LAS f32x2*)(L + L_ST) = f32x2{rbf(mean), __builtin_amdgcn_rcpf(den)};
    }
    if (TAILST == 1) TSTAMP(9);
    BARRIER();
    if (DLY2) __builtin_amdgcn_s_sleep(DLY2);
    const float res = hres + rbf(rbf(gather16(rws, vin + IN_P2)) + asf(b2v));
    __builtin_amdgcn_raw_buffer_store_b16(__builtin_bit_cast(unsigned short, (__bf16)res), mk_rsrc(out, NB * D * 2), vout, 0, 0);
    { SB(); const unsigned long long xt1 = __builtin_amdgcn_s_memrealtime(); const unsigned long long tgt = xt1 + 9 * (xt1 - xt0); while (__builtin_amdgcn_s_memrealtime() < tgt) {} }
  } else if (w >= 4) {
    const int u = w - 4, q4 = (lane >> 2) & 3;
    const rsrc_t rws = mk_rsrc(ws, WS_BYTES);
    __builtin_amdgcn_s_sleep(3);
    u32x8 W1[6];
    LAS unsigned char* p1 = L + L_W + u * 32768 + (lane & 3) * 8 + (4 * (r >> 1) + 32 * (r & 1) + q4) * 128;
    sfor<6>([&](auto J) { W1[J] = tr_read8<16 * 128, 8 * 128>(p1 + (J & 1) * 16384 + (J >> 1) * 32); });
    sfor<6>([&](auto J) { PIN(W1[J]); });
    __builtin_amdgcn_s_sleep(6);
    int vhl = blk * IN_BYTES + IN_H + u * 256 + r * 32 + (lane & 1) * 16, lwr = lane * 16 + u * 4;
    int idx = (lane & 1) ? 0xEEEEEEEE : 0x44444444;
    const f32x4 zero4 = {0.f, 0.f, 0.f, 0.f};
    f32x4 e[3];
    auto project = [&](u32x4 h0, u32x4 h1, int dst) {
      sfor<3>([&](auto T) { e[T] = smf(h0, W1[2 * T], e[T], idx); SB(); e[T] = smf(h1, W1[2 * T + 1], e[T], idx); SB(); });
      float a0 = e[0][0] + e[0][1], a1 = e[1][0] + e[1][1];
      a0 = r == 0 ? a0 : a1;
      PIN(a0);
      const float a2 = e[2][0] + e[2][1];
      KEEP(e[0]); KEEP(e[1]); KEEP(e[2]);
      *(LAS float*)(L + dst + lwr) = r >= 2 ? a2 : a0;
    };
    const u32x4 ones = {0x3f803f80u, 0x3f803f80u, 0x3f803f80u, 0x3f803f80u};
    sfor<3>([&](auto T) { e[T] = zero4; });
    project(ones, ones, L_EX);
    sfor<3>([&](auto T) { e[T] = zero4; PIN(e[T]); });
    PIN(vhl); PIN(lwr); PIN(idx);
    STAMP(6);
    BARRIER();
    if (DLY1) __builtin_amdgcn_s_sleep(DLY1);
    u32x4 h0, h1;
    for (unsigned spins = 0; spins < SPIN_CAP; ++spins) {
      asm volatile("" ::: "memory");
      h0 = __builtin_amdgcn_raw_buffer_load_b128(rws, vhl, 0, AUX_SC1);
      h1 = __builtin_amdgcn_raw_buffer_load_b128(rws, vhl, 128, AUX_SC1);
      if (__builtin_amdgcn_ballot_w64(max(max4(h0), max4(h1)) == SENT) == 0) break;
    }
    SB();
    STAMP(8); TSTAMP(6);
    project(h0, h1, L_EX2);
    if (TAILST == 1) TSTAMP(8);
    BARRIER();
  }
  }
}

extern "C" void kernel_launch(void* const* d_in, const int* in_sizes, int n_in, void* d_out, int out_size, void* d_ws,
                              size_t ws_size, hipStream_t stream) {
  fused_block<<<NB * NH, 768, 0, stream>>>((const unsigned char*)d_in[3], (const unsigned char*)d_in[1], (const unsigned char*)d_in[4],
                                      (const unsigned char*)d_in[2], (const unsigned short*)d_in[0], (const unsigned char*)d_in[6],
                                      (const unsigned char*)d_in[7], (const unsigned char*)d_in[8], (const unsigned short*)d_in[9],
                                      (unsigned short*)d_out, (unsigned char*)d_ws);
}
```

```cpp
#define DV 1
#define PEND 0
#define TCOM 0
#include <hip/hip_runtime.h>
#include <hip/hip_bf16.h>
#include <stdint.h>

#define STAMPS 0
#define PFCODE 0
#define WN 3
#define BWN 3
#define QB0 4
#define QRDY 9
#define QSTEP 28
#define OB0 14
#define MB0 35
#define QBS 8
#define OBS 12
#define MBS 12
#define NG 8
#define OWD 5
#define INB 8192
#define SENTH 0
#define SENTP 1
#define CHKS 61
#define PTAU 24
#define PBUR 90
#define PBUM 110
#define DITH 32
#define PSH 1
#define DITG 0
#define TSTEP 7
#define TFRAC 4
#define SKEW 0
#define PFROM 8
#define PTO 60
#define PEV 2
#define SENTE 1
#define LOSE -1
#define EARLYST 0
#define TOUCH 0
#define SENTW 0
#define TAILST 0
#define HST 1
#define EXPM 0
#define DLY0 20
#define DLY1 11
#define DLY2 18

typedef unsigned u32x4 __attribute__((ext_vector_type(4)));
typedef unsigned u32x2 __attribute__((ext_vector_type(2)));
typedef float f32x2 __attribute__((ext_vector_type(2)));
typedef float f32x4 __attribute__((ext_vector_type(4)));
typedef __bf16 bf16x2 __attribute__((ext_vector_type(2)));
typedef short s16x4 __attribute__((ext_vector_type(4)));
typedef unsigned u32x8 __attribute__((ext_vector_type(8)));
typedef __bf16 bf16x8 __attribute__((ext_vector_type(8)));
typedef __bf16 bf16x16 __attribute__((ext_vector_type(16)));
typedef __amdgpu_buffer_rsrc_t rsrc_t;
#define LAS __attribute__((address_space(3)))
#define DI __device__ __forceinline__

constexpr int NB = 8, D = 512, NH = 32, SK = 4096, DH = 128, WIN = 1024, F = 1376;
constexpr int FS = 48;
constexpr int AUX_NT = 2, AUX_SC1 = 16;
constexpr int OOB = 0x7ffffff0;
constexpr unsigned SENT = 0xffffffffu;
constexpr unsigned SPIN_CAP = 1u << 17;
constexpr int IN_P1 = 0;
constexpr int IN_P2 = 3072;
constexpr int IN_H = 2048;
constexpr int IN_BYTES = INB;
constexpr int WS_T = 256 * IN_BYTES;
constexpr int WS_BYTES = WS_T + 128;
constexpr int WS_STAMP = 4 * 1024 * 1024;
constexpr int L_RED = 0;
constexpr int L_RED2 = L_RED + 2048;
constexpr int L_EX = L_RED2 + 4096;
constexpr int L_EX2 = L_EX + 1024;
constexpr int L_ML = L_EX2 + 1024;
constexpr int L_ST = L_ML + 64;
constexpr int L_T = L_ST + 16;
constexpr int L_Y = 9216;
constexpr int L_DUMP = L_Y + 2048;
constexpr int L_W = L_DUMP + 1024;
constexpr int L_BYTES = L_W + 128 * 1024;

DI float dot2(unsigned a, unsigned b, float acc) {
  return __builtin_amdgcn_fdot2_f32_bf16(__builtin_bit_cast(bf16x2, a), __builtin_bit_cast(bf16x2, b), acc, false);
}
DI unsigned fbits(float f) { return __builtin_bit_cast(unsigned, f); }
DI float asf(unsigned u) { return __builtin_bit_cast(float, u); }
DI unsigned cvtpk(float lo, float hi) {
  bf16x2 r = {(__bf16)lo, (__bf16)hi};
  return __builtin_bit_cast(unsigned, r);
}
DI float rbf(float f) { return asf((unsigned)__builtin_bit_cast(unsigned short, (__bf16)f) << 16); }
template <int CTRL> DI unsigned dppu(unsigned x) { return __builtin_amdgcn_mov_dpp(x, CTRL, 0xf, 0xf, true); }
template <int CTRL> DI float dppf(float x) { return asf(dppu<CTRL>(fbits(x))); }
constexpr int XOR1 = 0xB1, XOR2 = 0x4E, HMIR = 0x141, ROR4 = 0x124, ROR8 = 0x128, BCAST = 0x150;
DI float tsum32(float a, float b) {
  auto s = __builtin_amdgcn_permlane32_swap(fbits(a), fbits(b), false, false);
  return asf(s[0]) + asf(s[1]);
}
DI float tsum16(float a, float b) {
  auto s = __builtin_amdgcn_permlane16_swap(fbits(a), fbits(b), false, false);
  return asf(s[0]) + asf(s[1]);
}
DI float row_sum(float x) { x += dppf<XOR1>(x); x += dppf<XOR2>(x); x += dppf<ROR4>(x); x += dppf<ROR8>(x); return x; }
DI float row_max(float x) { x = fmaxf(x, dppf<XOR1>(x)); x = fmaxf(x, dppf<XOR2>(x)); x = fmaxf(x, dppf<ROR4>(x)); x = fmaxf(x, dppf<ROR8>(x)); return x; }
DI float wave_sum(float x) { x = row_sum(x); x = tsum16(x, x); return tsum32(x, x); }
DI float wave_max(float x) {
  x = row_max(x);
  auto s = __builtin_amdgcn_permlane16_swap(fbits(x), fbits(x), false, false);
  x = fmaxf(asf(s[0]), asf(s[1]));
  auto t = __builtin_amdgcn_permlane32_swap(fbits(x), fbits(x), false, false);
  return fmaxf(asf(t[0]), asf(t[1]));
}
DI void rows_bcast(unsigned x, unsigned (&o)[4]) {
  auto s = __builtin_amdgcn_permlane16_swap(x, x, false, false);
  auto a = __builtin_amdgcn_permlane32_swap(s[0], s[0], false, false);
  auto b = __builtin_amdgcn_permlane32_swap(s[1], s[1], false, false);
  o[0] = a[0]; o[2] = a[1]; o[1] = b[0]; o[3] = b[1];
}
DI f32x4 smf(u32x4 a, u32x8 b, f32x4 c, int idx) {
  return __builtin_amdgcn_smfmac_f32_16x16x64_bf16(__builtin_bit_cast(bf16x8, a), __builtin_bit_cast(bf16x16, b), c, idx, 0, 0);
}
DI f32x4 mfma(u32x4 a, u32x4 b, f32x4 c) {
  return __builtin_amdgcn_mfma_f32_16x16x32_bf16(__builtin_bit_cast(bf16x8, a), __builtin_bit_cast(bf16x8, b), c, 0, 0, 0);
}
constexpr int SHL = 0x100;
template <class T> DI void pin_(T& x) { asm volatile("" : "+v"(x)); }
#define PIN(x) pin_(x)
template <class T> DI void keep_(const T& x) { asm volatile("" :: "v"(x)); }
#define KEEP(x) keep_(x)
template <class T> DI void pins_(T& x) { asm volatile("" : "+s"(x)); }
#define PINS(x) pins_(x)
#define SB() __builtin_amdgcn_sched_barrier(0)
#define PACEN(n) do { asm volatile("s_waitcnt vmcnt(%0)" :: "i"(n) : "memory"); SB(); } while (0)
#define PACE() PACEN(WN)
#define BARRIER() do { asm volatile("s_waitcnt lgkmcnt(0)\n\ts_barrier" ::: "memory"); SB(); } while (0)
DI rsrc_t mk_rsrc(const void* p, int bytes) { return __builtin_amdgcn_make_buffer_rsrc((void*)p, 0, bytes, 0x00020000); }

template <int G, int N, int CNT = N> DI void burst(int grp, rsrc_t r, int voff, int stride, int lds, int j0) {
  int keep, j, cnt, t;
  asm volatile(
      "s_cmp_lg_u32 %4, %10\n\t"
      "s_cbranch_scc1 BE%=\n\t"
      "s_mov_b32 %0, m0\n\t"
      "s_mov_b32 %1, %9\n\t"
      "s_mov_b32 %2, %12\n"
      "BL%=:\n\t"
      "s_lshl_b32 %3, %1, 10\n\t"
      "s_add_u32 m0, %3, %8\n\t"
      "s_mul_i32 %3, %1, %7\n\t"
      "s_add_u32 %1, %1, 1\n\t"
      "s_waitcnt vmcnt(%13)\n\t"
      "buffer_load_dwordx4 %6, %5, %3 offen lds\n\t"
      "s_cmp_eq_u32 %1, %11\n\t"
      "s_cselect_b32 %1, 0, %1\n\t"
      "s_sub_u32 %2, %2, 1\n\t"
      "s_cmp_lg_u32 %2, 0\n\t"
      "s_cbranch_scc1 BL%=\n\t"
      "s_mov_b32 m0, %0\n"
      "BE%=:"
      : "=&s"(keep), "=&s"(j), "=&s"(cnt), "=&s"(t)
      : "s"(grp), "s"(r), "v"(voff), "s"(stride), "s"(lds), "s"(j0), "i"(G), "i"(N), "i"(CNT), "i"(BWN)
      : "memory", "scc");
  SB();
}
DI void ald128nt(u32x4& d, rsrc_t r, int voff, int soff) { asm volatile("buffer_load_dwordx4 %0, %1, %2, %3 offen nt" : "=v"(d) : "v"(voff), "s"(r), "s"(soff) : "memory"); SB(); }
DI void ald128(u32x4& d, rsrc_t r, int voff, int soff) { asm volatile("buffer_load_dwordx4 %0, %1, %2, %3 offen" : "=v"(d) : "v"(voff), "s"(r), "s"(soff) : "memory"); SB(); }
DI void ald64sc(u32x2& d, rsrc_t r, int voff, int soff) { asm volatile("buffer_load_dwordx2 %0, %1, %2, %3 offen sc1" : "=v"(d) : "v"(voff), "s"(r), "s"(soff) : "memory"); SB(); }
DI void ald32sc(unsigned& d, rsrc_t r, int voff) { asm volatile("buffer_load_dword %0, %1, %2, 0 offen sc1" : "=v"(d) : "v"(voff), "s"(r) : "memory"); SB(); }
DI void ald32(unsigned& d, rsrc_t r, int voff) { asm volatile("buffer_load_dword %0, %1, %2, 0 offen" : "=v"(d) : "v"(voff), "s"(r) : "memory"); SB(); }
DI void ald16(unsigned& d, rsrc_t r, int voff, int soff) { asm volatile("buffer_load_ushort %0, %1, %2, %3 offen" : "=v"(d) : "v"(voff), "s"(r), "s"(soff) : "memory"); SB(); }
template <bool NT = false> DI void dma1(rsrc_t r, int voff, int soff, int lds) {
  int keep;
  if constexpr (NT)
    asm volatile("s_mov_b32 %0, m0\n\ts_mov_b32 m0, %4\n\ts_nop 0\n\tbuffer_load_dwordx4 %1, %2, %3 offen nt lds\n\ts_mov_b32 m0, %0"
                 : "=&s"(keep) : "v"(voff), "s"(r), "s"(soff), "s"(lds) : "memory");
  else
    asm volatile("s_mov_b32 %0, m0\n\ts_mov_b32 m0, %4\n\ts_nop 0\n\tbuffer_load_dwordx4 %1, %2, %3 offen lds\n\ts_mov_b32 m0, %0"
                 : "=&s"(keep) : "v"(voff), "s"(r), "s"(soff), "s"(lds) : "memory");
  SB();
}
template <int G> DI void cond_barrier(int grp) {
  asm volatile("s_cmp_lg_u32 %0, %1\n\ts_cbranch_scc1 CB%=\n\ts_barrier\nCB%=:" :: "s"(grp), "i"(G) : "memory", "scc");
  SB();
}
DI float gather16(rsrc_t r, int voff);
DI u32x2 tr_read(LAS unsigned char* p) {
  return __builtin_bit_cast(u32x2, __builtin_amdgcn_ds_read_tr16_b64_v4i16((LAS s16x4*)p));
}
template <int M1, int M2> DI u32x8 tr_read8(LAS unsigned char* p) {
  const u32x2 a = tr_read(p), b = tr_read(p + M1), c = tr_read(p + M2), d = tr_read(p + M1 + M2);
  return u32x8{a[0], a[1], b[0], b[1], c[0], c[1], d[0], d[1]};
}
DI f32x2 fold_rows8(const float (&acc)[8]) {
  const float t0 = tsum32(acc[0], acc[4]), t1 = tsum32(acc[1], acc[5]), t2 = tsum32(acc[2], acc[6]), t3 = tsum32(acc[3], acc[7]);
  return f32x2{tsum16(t0, t2), tsum16(t1, t3)};
}
DI float fold_rows4(float a0, float a1, float a2, float a3) { return tsum16(tsum32(a0, a2), tsum32(a1, a3)); }
DI unsigned max4(u32x4 v) { return max(max(v[0], v[1]), max(v[2], v[3])); }

DI float gather16(rsrc_t r, int voff) {
  u32x4 A, Bv;
  for (unsigned spins = 0; spins < SPIN_CAP; ++spins) {
    asm volatile("" ::: "memory");
    A = __builtin_amdgcn_raw_buffer_load_b128(r, voff, 0, AUX_SC1);
    Bv = __builtin_amdgcn_raw_buffer_load_b128(r, voff, 1024, AUX_SC1);
    if (__builtin_amdgcn_ballot_w64(max(max4(A), max4(Bv)) == SENT) == 0) break;
  }
  const f32x4 s = __builtin_bit_cast(f32x4, A) + __builtin_bit_cast(f32x4, Bv);
  float t = fold_rows4(s[0], s[1], s[2], s[3]);
  t += dppf<ROR4>(t); t += dppf<ROR8>(t);
  return t;
}

template <int I> DI void pv_step(float (&acc)[8], unsigned plo, unsigned phi, u32x4 v) {
  const unsigned lo = dppu<BCAST + I>(plo), hi = dppu<BCAST + I>(phi);
#pragma unroll
  for (int k = 0; k < 4; ++k) { acc[2 * k] = dot2(lo, v[k], acc[2 * k]); acc[2 * k + 1] = dot2(hi, v[k], acc[2 * k + 1]); }
}
DI void pin8(float (&a)[8]) {
  asm volatile("" : "+v"(a[0]), "+v"(a[1]), "+v"(a[2]), "+v"(a[3]), "+v"(a[4]), "+v"(a[5]), "+v"(a[6]), "+v"(a[7]));
}
template <int... Is> struct iseq {};
template <int N, int... Is> struct mkseq : mkseq<N - 1, N - 1, Is...> {};
template <int... Is> struct mkseq<0, Is...> { typedef iseq<Is...> type; };
template <class Fn, int... Is> DI void sfor_(Fn&& f, iseq<Is...>) { (f(std::integral_constant<int, Is>{}), ...); }
template <int N, class Fn> DI void sfor(Fn&& f) { sfor_(f, typename mkseq<N>::type{}); }

DI float transpose_reduce16(float (&x)[16], int c) {
#define TR_STEP(HALF, CTRL, BIT) _Pragma("unroll") for (int h_ = 0; h_ < (HALF); ++h_) { \
    const float lo_ = x[h_], hi_ = x[h_ + (HALF)]; const float t1_ = lo_ + dppf<CTRL>(lo_), t2_ = hi_ + dppf<CTRL>(hi_); \
    x[h_] = (c & (BIT)) ? t2_ : t1_; }
  TR_STEP(8, ROR8, 8) TR_STEP(4, HMIR, 4) TR_STEP(2, XOR2, 2) TR_STEP(1, XOR1, 1)
#undef TR_STEP
  return x[0];
}

#define STAMP(i) do {} while (0)
#define ESTAMP(i) do {} while (0)
#define TSTAMP(i) do {} while (0)

extern "C" __global__ void __launch_bounds__(768, 3)
fused_block(const unsigned char* __restrict__ wq, const unsigned char* __restrict__ kc, const unsigned char* __restrict__ wo,
            const unsigned char* __restrict__ vc, const unsigned short* __restrict__ x, const unsigned char* __restrict__ w1,
            const unsigned char* __restrict__ b1, const unsigned char* __restrict__ w2, const unsigned short* __restrict__ b2,
            unsigned short* __restrict__ out, unsigned char* ws) {
  __shared__ __attribute__((aligned(16))) unsigned char smem[L_BYTES];
  LAS unsigned char* L = (LAS unsigned char*)smem;
  if (SKEW && (blockIdx.x & 7) == 5) __builtin_amdgcn_s_sleep(SKEW);
  unsigned xt[5]; xt[0] = (unsigned)__builtin_amdgcn_s_memrealtime();
  const int blk = blockIdx.x, b = blk >> 5, h = blk & 31;
  const int tid = threadIdx.x, lane = tid & 63, r = lane >> 4, c = lane & 15;
  const int w = __builtin_amdgcn_readfirstlane(tid >> 6);
  const int slice = (h & 7) * 4 + (h >> 3), col0 = slice * FS;
#define IC(n) std::integral_constant<int, (n)>{}
  if (LOSE >= 0 && (blk == LOSE || blk == LOSE + 5)) for (int i = 0; i < (blk == LOSE ? 12 : 30); ++i) __builtin_amdgcn_s_sleep(127);
  unsigned t0 = 0;
  {
    int w2 = w;
    PINS(w2);
    if (w2 >= 8) {
      t0 = (unsigned)__builtin_amdgcn_s_memrealtime();
      const rsrc_t r0 = mk_rsrc(ws, WS_BYTES);
      u32x4 s4 = {SENT, SENT, SENT, SENT};
      PIN(s4);
      if (!(EXPM & 1)) __builtin_amdgcn_raw_buffer_store_b128(s4, r0, w2 >= 9 ? lane * 16 : OOB, blk * IN_BYTES + (w2 & 3) * 1024 - (w2 >= 9 ? 1024 : 0), AUX_SC1);
      if (TCOM) __builtin_amdgcn_raw_buffer_store_b32(t0, r0, (tid == 512 && blk < 8) ? blk * 4 : OOB, WS_T, AUX_SC1);
      SB(); KEEP(s4);
    }
    SB();
  }
  if (w >= 8) {
  const int u = w - 8;
  const rsrc_t rws = mk_rsrc(ws, WS_BYTES);
  const u32x4 xrow = *(const u32x4*)(x + b * D + lane * 8);
  const float xe0 = asf((unsigned)x[b * D + 128 * u + 4 * c + r] << 16), xe1 = asf((unsigned)x[b * D + 128 * u + 64 + 4 * c + r] << 16);
  float b1v = asf((unsigned)__builtin_amdgcn_raw_buffer_load_b16(mk_rsrc(b1, F * 2), lane < FS ? (col0 + lane) * 2 : OOB, 0, 0) << 16);
  if (PFCODE && b == 0 && u >= 2) {
    unsigned long long pc; int o0, o1;
    asm volatile("s_getpc_b64 %0\nKPC0:\n\ts_mov_b32 %1, KPC0-KPCA\n\ts_mov_b32 %2, KPCB-KPCA" : "=s"(pc), "=s"(o0), "=s"(o1));
    const rsrc_t rc = mk_rsrc((const void*)(pc - (long long)o0), o1);
    unsigned p0 = __builtin_amdgcn_raw_buffer_load_b32(rc, lane * 128, (u - 2) * 8192, 0);
    PIN(p0);
  }
  SB();
  {
    float xf[8];
#pragma unroll
    for (int k = 0; k < 4; ++k) { xf[2 * k] = asf(xrow[k] << 16); xf[2 * k + 1] = asf(xrow[k] & 0xffff0000u); }
    const float mean = wave_sum(((xf[0] + xf[1]) + (xf[2] + xf[3])) + ((xf[4] + xf[5]) + (xf[6] + xf[7]))) * (1.f / D);
    float vs = 0.f;
#pragma unroll
    for (int k = 0; k < 8; ++k) { const float d = xf[k] - mean; vs = fmaf(d, d, vs); }
    const float var = wave_sum(vs) * (1.f / D);
    const float den = rbf(sqrtf(rbf(rbf(var) + 1e-5f))), mb = rbf(mean);
    const unsigned xp = cvtpk(rbf(xe0 - mb) / den, rbf(xe1 - mb) / den);
    unsigned xlo[2] = {xp & 0xffffu, xp >> 16}, xhi[2] = {xp << 16, xp & 0xffff0000u};
    PIN(xlo[0]); PIN(xlo[1]); PIN(xhi[0]); PIN(xhi[1]); PIN(b1v);
    PACEN(0);
    ESTAMP(5);
    BARRIER();
    ESTAMP(6);
    float acc[8] = {0.f, 0.f, 0.f, 0.f, 0.f, 0.f, 0.f, 0.f};
    sfor<32>([&](auto I) { pv_step<I & 15>(acc, xlo[I >> 4], xhi[I >> 4], *(const LAS u32x4*)(L + L_W + u * 32768 + I * 1024 + lane * 16)); });
    *(LAS f32x2*)(L + L_RED + (u >> 1) * 1024 + lane * 16 + (u & 1) * 8) = fold_rows8(acc);
  }
  STAMP(11);
  BARRIER();
  BARRIER();
  u32x8 WO[16];
  const int q4 = (lane >> 2) & 3;
  LAS unsigned char* lw = L + L_W + u * 32768 + (lane & 3) * 8;
  {
    LAS unsigned char* p = lw + (32 * (r >> 1) + 4 * (r & 1) + (q4 >> 1) + 8 * (q4 & 1)) * 128;
    sfor<16>([&](auto J) { WO[J] = tr_read8<2 * 128, 64 * 128>(p + (J >> 3) * 16384 + ((J >> 1) & 3) * 32 + (J & 1) * 16 * 128); });
    sfor<16>([&](auto J) { PIN(WO[J]); });
  }
  BARRIER();
  BARRIER();
  int vp = lane < 16 ? (b * 32 + 8 * u + (lane >> 2)) * IN_BYTES + (lane & 3) * 16 : OOB;
  int lrd = lane * 16;
  int idx = (lane & 1) ? 0xEEEEEEEE : 0x44444444;
  PIN(vp); PIN(lrd); PIN(idx);
  const f32x4 zero4 = {0.f, 0.f, 0.f, 0.f};
  f32x4 d[8];
  d[0] = zero4; d[1] = zero4; PIN(d[0]); PIN(d[1]);
  int so1 = IN_P1 + h * 64, so2 = IN_P1 + h * 64 + 4 * IN_BYTES;
  PINS(so1); PINS(so2);
  BARRIER();
  TSTAMP(2);
  {
    f32x4 s4 = *(const LAS f32x4*)(L + L_RED2 + lrd) + *(const LAS f32x4*)(L + L_RED2 + 1024 + lrd);
    s4 += *(const LAS f32x4*)(L + L_RED2 + 2048 + lrd) + *(const LAS f32x4*)(L + L_RED2 + 3072 + lrd);
    const unsigned op = cvtpk(s4[0] + s4[2], s4[1] + s4[3]);
    const u32x4 a0 = {op, dppu<SHL + 2>(op), dppu<SHL + 4>(op), dppu<SHL + 6>(op)};
    const u32x4 a1 = {dppu<SHL + 8>(op), dppu<SHL + 10>(op), dppu<SHL + 12>(op), dppu<SHL + 14>(op)};
    float y[8];
    auto fin = [&](auto T) { y[T] = d[T][0] + d[T][1]; PIN(y[T]); KEEP(d[T]); };
    sfor<8>([&](auto T) {
      d[T] = smf(a0, WO[2 * T], d[T], idx);
      if constexpr (T > 0) SB();
      if constexpr (T + 2 < 8 && T > 0) { d[T + 2] = zero4; PIN(d[T + 2]); }
      SB();
      d[T] = smf(a1, WO[2 * T + 1], d[T], idx);
      SB();
      if constexpr (T == 0) { d[2] = zero4; PIN(d[2]); }
      if constexpr (T >= 2) fin(IC(T - 2));
      if constexpr (T == 6)
        __builtin_amdgcn_raw_buffer_store_b128(u32x4{fbits(y[0]), fbits(y[1]), fbits(y[2]), fbits(y[3])}, rws, (EXPM & 8) ? OOB : vp, so1, AUX_SC1);
      SB();
    });
    fin(IC(6)); fin(IC(7));
    __builtin_amdgcn_raw_buffer_store_b128(u32x4{fbits(y[4]), fbits(y[5]), fbits(y[6]), fbits(y[7])}, rws, (EXPM & 8) ? OOB : vp, so2, AUX_SC1);
    SB();
    if (lrd < 256) {
      *(LAS f32x4*)(L + L_Y + u * 512 + lrd) = f32x4{y[0], y[1], y[2], y[3]};
      *(LAS f32x4*)(L + L_Y + u * 512 + 256 + lrd) = f32x4{y[4], y[5], y[6], y[7]};
    }
  }
  SB();
  STAMP(5); TSTAMP(3);
  u32x8 W2[8];
  {
    const int ln = __builtin_amdgcn_mbcnt_hi(~0u, __builtin_amdgcn_mbcnt_lo(~0u, 0u)), rr = ln >> 4, qq = (ln >> 2) & 3;
    LAS unsigned char* p2 = L + L_W + u * 32768 + (ln & 3) * 8 + 8192 + (16 * (rr >> 1) + 8 * (rr & 1) + (qq >> 1) + 2 * (qq & 1)) * 128;
    LAS unsigned char* p3 = p2 + (32 - 16 * (rr >> 1)) * 128;
    sfor<8>([&](auto J) {
      constexpr int o = (J >> 2) * 16384 + (J & 3) * 32;
      const u32x2 t0 = tr_read(p2 + o), t1 = tr_read(p2 + o + 4 * 128), t2 = tr_read(p3 + o), t3 = tr_read(p3 + o + 4 * 128);
      W2[J] = u32x8{t0[0], t0[1], t1[0], t1[1], t2[0], t2[1], t3[0], t3[1]};
    });
    sfor<8>([&](auto J) { PIN(W2[J]); d[J] = zero4; PIN(d[J]); });
  }
  so1 = IN_P2 + slice * 64; so2 = IN_P2 + slice * 64 + 4 * IN_BYTES;
  int lst = L_ST;
  float gk = -2.f * 1.4426950408889634f * 0.7978845608028654f;
  unsigned gmask = lrd < FS * 16 ? ~0u : 0u;
  PINS(so1); PINS(so2); PIN(lst); PIN(gk); PIN(gmask);
  SB();
  STAMP(6);
  BARRIER();
  float c1;
  { const f32x4 t = *(const LAS f32x4*)(L + L_EX + lrd); c1 = (t[0] + t[1]) + (t[2] + t[3]); }
  PIN(c1);
  BARRIER();
  if (TAILST == 2) TSTAMP(8);
  {
    const f32x4 t4 = *(const LAS f32x4*)(L + L_EX2 + lrd);
    const f32x2 ms = *(const LAS f32x2*)(L + lst);
    const float tot = (t4[0] + t4[1]) + (t4[2] + t4[3]);
    const float uu = rbf(rbf(fmaf(-ms[0], c1, tot) * ms[1]) + b1v);
    const float z = uu * fmaf(uu * uu, -2.f * 1.4426950408889634f * 0.044715f * 0.7978845608028654f, gk);
    const float g = asf(fbits(uu) & gmask) * __builtin_amdgcn_rcpf(1.f + __builtin_amdgcn_exp2f(z));
    const unsigned gp = cvtpk(g, dppf<XOR2>(g));
    const u32x4 ag = {gp, dppu<SHL + 4>(gp), dppu<SHL + 8>(gp), dppu<SHL + 12>(gp)};
    if (TAILST == 2) { KEEP(ag); TSTAMP(9); }
    float y[8];
    auto fin = [&](auto T) { y[T] = d[T][0] + d[T][1]; PIN(y[T]); KEEP(d[T]); SB(); };
    sfor<8>([&](auto T) { d[T] = smf(ag, W2[T], d[T], idx); SB(); });
    fin(IC(0)); fin(IC(1)); fin(IC(2)); fin(IC(3));
    __builtin_amdgcn_raw_buffer_store_b128(u32x4{fbits(y[0]), fbits(y[1]), fbits(y[2]), fbits(y[3])}, rws, vp, so1, AUX_SC1);
    SB();
    fin(IC(4)); fin(IC(5)); fin(IC(6)); fin(IC(7));
    KEEP(y[0]); KEEP(y[1]); KEEP(y[2]); KEEP(y[3]);
    __builtin_amdgcn_raw_buffer_store_b128(u32x4{fbits(y[4]), fbits(y[5]), fbits(y[6]), fbits(y[7])}, rws, vp, so2, AUX_SC1);
  }
  asm volatile("KPCB:");
  SB();
  STAMP(9); TSTAMP(10);
  } else {
  const int grp = NG == 8 ? (b + 2 * (h >> 3)) & 7 : (NG == 16 ? (2 * b + 4 * (h >> 4) + ((h >> 3) & 1)) & 15 : 4 * b + (h >> 3));
  const int pq = QB0 + ((grp * QBS) >> 2), po = OB0 + ((grp * OBS) >> 2), pm = MB0 + ((grp * MBS) >> 2);
  const size_t kvoff = ((size_t)blk * SK + (SK - WIN)) * (DH * 2);
  const rsrc_t rk = mk_rsrc(kc + kvoff, WIN * DH * 2), rv = mk_rsrc(vc + kvoff, WIN * DH * 2);
  const int vkv = tid * 16;
  const int lds0 = (int)(unsigned)(size_t)L, ldsw = lds0 + L_W + w * 16384, ldsd = lds0 + L_DUMP;

  u32x4 K[32], V[32];
  auto ldk = [&](auto I) { ald128nt(K[I], rk, vkv, I * 8192); };
  auto ldv = [&](auto I) { ald128nt(V[I], rv, vkv, I * 8192); };
  ldk(IC(0)); ldk(IC(1));
  const unsigned long long tclk = __builtin_amdgcn_s_memrealtime();
  SB();
  asm volatile("KPCA:");
  const rsrc_t rq = mk_rsrc(wq + h * (DH * 2), D * NH * DH * 2 - h * (DH * 2));
  const int vq = (64 * w + r) * (NH * DH * 2) + c * 16;
  dma1(rq, vq, 2 * b * (4 * NH * DH * 2), ldsw + 2 * b * 1024);
  dma1(rq, vq, (2 * b + 1) * (4 * NH * DH * 2), ldsw + (2 * b + 1) * 1024);
  unsigned tch = 0;
  if (TOUCH) {
    const unsigned char* p = w == 0 ? vc + kvoff : w == 1 ? wo + (size_t)h * (DH * D * 2) : w == 2 ? w1 + 256 * F * 2 : w == 3 ? w2 + (size_t)min(col0, F - 1) * (D * 2)
                           : w == 4 ? ws + blk * IN_BYTES + 5120 : w == 5 ? (const unsigned char*)out : w == 6 ? (const unsigned char*)b2 : b1;
    ald32(tch, mk_rsrc(p, 4), lane == 0 ? 0 : OOB);
  }
  const rsrc_t ro = mk_rsrc(wo + (size_t)h * (DH * D * 2), DH * D * 2);
  const int vo = (lane >> 3) * (D * 2) + w * 128 + (lane & 7) * 16;
  const rsrc_t r1 = mk_rsrc(w1, D * F * 2);
  const int v1 = ((lane & 7) < 6 && col0 + (lane & 7) * 8 < F) ? (64 * w + (lane >> 3)) * (F * 2) + col0 * 2 + (lane & 7) * 16 : OOB;
  const int c2 = min(col0, F);
  const rsrc_t r2 = mk_rsrc(w2 + (size_t)c2 * (D * 2), (F - c2) * (D * 2));
  const int b6 = b < 6 ? b : b - 6;
  const int dith = (((w - 4) * DITH) >> 3) + ((((blk >> 3) - 16) * DITG) >> 5);
  unsigned tst = PEND ? (unsigned)tclk + dith : 0u;
  auto hook = [&](auto I) {
    constexpr int i = I;
    if constexpr (PEND && i >= PFROM && i <= PTO && i % PEV == 0) {
      PINS(tst);
      const int pend = (i <= pq ? PBUR : 0) + (i <= po ? PBUR : 0) + (i <= pm ? PBUM : 0);
      constexpr int extra = (i <= OB0 - OWD ? 2 : 0) + (i <= MB0 - OWD ? 2 : 0);
      const unsigned tgt = tst + PEND - (67 - i + extra) * PTAU - pend;
      int tmp;
      asm volatile("s_memrealtime vcc\n\ts_waitcnt lgkmcnt(0)\n\ts_sub_i32 %0, %1, vcc_lo\n\ts_ashr_i32 %0, %0, %2\n\ts_cmp_lt_i32 %0, 3\n\ts_cbranch_scc1 PE%=\n\t"
                   "s_add_i32 %0, %0, vcc_lo\n"
                   "PC%=:\n\ts_memrealtime vcc\n\ts_waitcnt lgkmcnt(0)\n\ts_sub_i32 vcc_lo, vcc_lo, %0\n\ts_cmp_lt_i32 vcc_lo, 0\n\ts_cbranch_scc1 PC%=\nPE%=:"
                   : "=&s"(tmp) : "s"(tgt), "i"(PSH) : "scc", "vcc", "memory");
      SB();
    }
    constexpr auto site = [](int i_, int p0, int sp) { for (int g = 0; g < NG; ++g) if (p0 + ((g * sp) >> 2) == i_) return true; return false; };
    if constexpr (site(i - 4, QB0, QBS)) cond_barrier<i - 4>(pq);
    if constexpr (site(i - QRDY, QB0, QBS)) cond_barrier<i - QRDY>(pq);
    if constexpr (site(i - 4, OB0, OBS)) cond_barrier<i - 4>(po);
    if constexpr (site(i, MB0, MBS)) cond_barrier<i>(pm);
    if constexpr (site(i, QB0, QBS)) burst<i, 16, 14>(pq, rq, vq, 4 * NH * DH * 2, ldsw, (2 * b + 2) & 15);
    if constexpr (i == OB0 - OWD) { PACE(); dma1(ro, vo, 2 * b * 8192, ldsd); PACE(); dma1(ro, vo, (2 * b + 1) * 8192, ldsd); }
    if constexpr (site(i, OB0, OBS)) burst<i, 16>(po, ro, vo, 8 * D * 2, ldsw, 2 * b);
    if constexpr (i == MB0 - OWD) { PACE(); dma1(r1, v1, b * (8 * F * 2), ldsd); PACE(); dma1(r2, vo, b * (8 * D * 2), ldsd); }
    if constexpr (site(i, MB0, MBS)) {
      burst<i, 8>(pm, r1, v1, 8 * F * 2, ldsw, b);
      burst<i, 6>(pm, r2, vo, 8 * D * 2, ldsw + 8192, b6);
    }
  };
  static_assert(QRDY > 4 && QB0 + (NG - 1) * QBS / 4 + QRDY < QSTEP && OBS >= QBS && OB0 >= QB0 + QRDY && MB0 > OB0 + 4 &&
                MB0 + (NG - 1) * MBS / 4 > OB0 + (NG - 1) * OBS / 4 + 5 && MB0 + (NG - 1) * MBS / 4 <= CHKS && CHKS >= 52 && CHKS <= 63, "barrier order, staging area reuse");
  unsigned tx;
  constexpr int S0 = 2;
  sfor<QSTEP - S0>([&](auto I) { constexpr int i = I + S0; if constexpr (i == 11) ESTAMP(7); if constexpr (i == 2) ESTAMP(8); if constexpr (i == 3) { SB(); xt[1] = (unsigned)__builtin_amdgcn_s_memrealtime(); SB(); } hook(IC(i));
    PACEN(WN + (TCOM && i > TSTEP && i < TSTEP + 4 ? 1 : 0)); ldk(IC(i));
    if constexpr (TCOM && i == TSTEP) ald32sc(tx, mk_rsrc(ws + WS_T, 32), lane * 4);
    if constexpr (TCOM && i == TSTEP + 4) {
      PIN(tx);
      int dt = (int)(tx - (unsigned)tclk);
      dt = (dt > -400 && dt < 400) ? dt : 0;
      dt += (int)dppu<XOR1>(dt); dt += (int)dppu<XOR2>(dt); dt += (int)dppu<HMIR>(dt);
      tst += __builtin_amdgcn_readfirstlane((dt * TFRAC) >> 5);
      SB();
    }
  });
  PIN(tch);
  SB(); xt[2] = (unsigned)__builtin_amdgcn_s_memrealtime(); SB();
  unsigned qp[4];
  {
    const f32x4 s4 = *(const LAS f32x4*)(L + L_RED + lane * 16) + *(const LAS f32x4*)(L + L_RED + 1024 + lane * 16);
    rows_bcast(cvtpk(s4[0] + s4[2], s4[1] + s4[3]), qp);
  }
  SB();
  STAMP(2);
  float sc[2], xs[16];
  auto qk = [&](u32x4& kv) { PIN(kv); return dot2(kv[3], qp[3], dot2(kv[2], qp[2], dot2(kv[1], qp[1], dot2(kv[0], qp[0], 0.f)))); };
  static_assert(QSTEP <= 28, "");
  sfor<36 - QSTEP>([&](auto I) {
    constexpr int i = I + QSTEP;
    hook(IC(i));
    PACE();
    if constexpr (i < 32) ldk(IC(i)); else ldv(IC(i - 32));
    if constexpr (I < 3) sfor<(I < 2 ? 6 : 4)>([&](auto J) { xs[6 * I + J] = qk(K[6 * I + J]); PIN(xs[6 * I + J]); });
    if constexpr (I == 3) { sc[0] = transpose_reduce16(xs, c); PIN(sc[0]); }
    if constexpr (I >= 4) {
      constexpr int lo = (I == 4) ? 16 : (16 + 6 * (I - 4) < i - 4 ? 16 + 6 * (I - 4) : i - 4);
      constexpr int hi = (16 + 6 * (I - 3) < i - 3) ? 16 + 6 * (I - 3) : i - 3;
      sfor<hi - lo>([&](auto J) { xs[lo - 16 + J] = qk(K[lo + J]); PIN(xs[lo - 16 + J]); });
    }
    SB();
  });
  sc[1] = transpose_reduce16(xs, c);
  SB();
  hook(IC(36)); PACE(); ldv(IC(4));
  unsigned pp[2];
  float mw, e1;
  {
    const float s0 = rbf(rbf(sc[0]) * 0.08838834764831845f), s1 = rbf(rbf(sc[1]) * 0.08838834764831845f);
    mw = wave_max(fmaxf(s0, s1));
    const float e0 = __builtin_amdgcn_exp2f((s0 - mw) * 1.4426950408889634f);
    e1 = __builtin_amdgcn_exp2f((s1 - mw) * 1.4426950408889634f);
    pp[0] = cvtpk(e0, e0); pp[1] = cvtpk(e1, e1);
    SB();
    hook(IC(37)); PACE(); ldv(IC(5));
    *(LAS f32x2*)(L + L_ML + w * 8) = f32x2{mw, wave_sum(e0 + e1)};
  }
  SB();
  STAMP(3);
  f32x4 acc = {0.f, 0.f, 0.f, 0.f};
  u32x4 sel;
#pragma unroll
  for (int k = 0; k < 4; ++k) sel[k] = ((c & 7) >> 1) == k ? ((c & 1) ? 0xffff0000u : 0xffffu) : 0u;
  auto pv = [&](auto I) { PIN(V[I]); const unsigned pb = dppu<BCAST + (I & 15)>(pp[I >> 4]); acc = mfma(V[I], sel & pb, acc); PIN(acc); };
  const int ocol = 64 * (h >> 2) + 16 * r + 4 * (h & 3) + (c & 3);
  unsigned b2v, xo;
  int lw2 = (w >> 1) * 1024 + lane * 16 + (w & 1) * 8;
  u32x4 blast;
  sfor<29>([&](auto I) {
    constexpr int i = I + 6;
    hook(IC(i + 32));
    if constexpr (i < 32) { PACE(); ldv(IC(i)); }
    else PACEN(35 - i);
    if constexpr (i == 24) { ald16(b2v, mk_rsrc(b2, D * 2), w == 0 ? ocol * 2 : OOB, 0); ald16(xo, mk_rsrc(x, NB * D * 2), w == 0 ? ocol * 2 : OOB, b * (D * 2)); PIN(lw2); }
    if constexpr (i == 6) { pv(IC(0)); pv(IC(1)); }
    if constexpr (i + 32 == CHKS) {
      BARRIER();
      const f32x2 ml = *(const LAS f32x2*)(L + L_ML + (lane & 7) * 8);
      float M = ml[0];
      M = fmaxf(M, dppf<XOR1>(M)); M = fmaxf(M, dppf<XOR2>(M)); M = fmaxf(M, dppf<HMIR>(M));
      float Ls = ml[1] * __builtin_amdgcn_exp2f((ml[0] - M) * 1.4426950408889634f);
      Ls += dppf<XOR1>(Ls); Ls += dppf<XOR2>(Ls); Ls += dppf<HMIR>(Ls);
      const float scale = __builtin_amdgcn_exp2f((mw - M) * 1.4426950408889634f) / Ls;
      acc *= scale;
      pp[1] = cvtpk(e1 * scale, e1 * scale);
    }
    pv(IC(i - 4));
    SB();
  });
  blast = sel & dppu<BCAST + 15>(pp[1]);
  PIN(blast);
  PACEN(0);
  STAMP(4); TSTAMP(1);
  PIN(V[31]);
  acc = mfma(V[31], blast, acc);
  *(LAS f32x2*)(L + L_RED2 + lw2) = (c & 8) ? f32x2{acc[2], acc[3]} : f32x2{acc[0], acc[1]};
  BARRIER();
  SB(); xt[3] = (unsigned)__builtin_amdgcn_s_memrealtime(); SB();
  PIN(b2v); PIN(xo);
  if (w == 0) {
    const rsrc_t rws = mk_rsrc(ws, WS_BYTES);
    int vin = blk * IN_BYTES + lane * 16;
    if (SENTP) {
      u32x4 sent4 = {SENT, SENT, SENT, SENT};
      PIN(sent4);
      __builtin_amdgcn_raw_buffer_store_b128(sent4, rws, vin, IN_P2, AUX_SC1);
      __builtin_amdgcn_raw_buffer_store_b128(sent4, rws, vin, IN_P2 + 1024, AUX_SC1);
      asm volatile("s_nop 1" :: "v"(sent4) : "memory");
    }
    int vhs = HST == 0 ? (b * 32 + c) * IN_BYTES + IN_H + h * 32 + r * 8 : (b * 32 + (lane >> 1)) * IN_BYTES + IN_H + h * 32 + (lane & 1) * 16;
    int vout = c < 4 ? (b * D + ocol) * 2 : OOB;
    b2v <<= 16; xo <<= 16;
    PIN(vin); PIN(vhs); PIN(vout); PIN(b2v); PIN(xo);
    if (DLY0) __builtin_amdgcn_s_sleep(DLY0);
    PACEN(0);
    const float hf = asf(xo) + rbf(gather16(rws, vin + IN_P1));
    const unsigned hq = min(cvtpk(hf, dppf<XOR1>(hf)), SENT - 1u);
    unsigned hb[4];
    rows_bcast(dppu<0x88>(hq), hb);
    __builtin_amdgcn_raw_buffer_store_b128(u32x4{hb[0], hb[1], hb[2], hb[3]}, rws, vhs, 0, AUX_SC1);
    SB();
    STAMP(7); TSTAMP(5);
    BARRIER();
    float hres = asf(hq << 16);
    PIN(hres);
    if (DLY1) __builtin_amdgcn_s_sleep(DLY1);
    u32x4 hr;
    for (unsigned spins = 0; spins < SPIN_CAP; ++spins) {
      asm volatile("" ::: "memory");
      hr = __builtin_amdgcn_raw_buffer_load_b128(rws, vin, IN_H, AUX_SC1);
      if (__builtin_amdgcn_ballot_w64(max4(hr) == SENT) == 0) break;
      if ((spins & 63u) == 63u) {
        const int vr = (b * 32 + (lane >> 2)) * IN_BYTES + IN_P1 + h * 64 + (lane & 3) * 16;
        __builtin_amdgcn_raw_buffer_store_b128(*(const LAS u32x4*)(L + L_Y + lane * 16), rws, vr, 0, AUX_SC1);
        __builtin_amdgcn_raw_buffer_store_b128(*(const LAS u32x4*)(L + L_Y + 1024 + lane * 16), rws, vr, 16 * IN_BYTES, AUX_SC1);
      }
    }
    TSTAMP(7);
    {
      const float s1 = dot2(hr[3], 0x3f803f80u, dot2(hr[2], 0x3f803f80u, dot2(hr[1], 0x3f803f80u, dot2(hr[0], 0x3f803f80u, 0.f))));
      const float s2 = dot2(hr[3], hr[3], dot2(hr[2], hr[2], dot2(hr[1], hr[1], dot2(hr[0], hr[0], 0.f))));
      float t = tsum32(s1, s2);
      t = row_sum(tsum16(t, t));
      const float mean = asf(__builtin_amdgcn_readlane(fbits(t), 0)) * (1.f / D);
      const float var = fmaxf(fmaf(-mean, mean, asf(__builtin_amdgcn_readlane(fbits(t), 32)) * (1.f / D)), 0.f);
      const float den = rbf(__builtin_amdgcn_sqrtf(rbf(rbf(var) + 1e-5f)));
      *(LAS f32x2*)(L + L_ST) = f32x2{rbf(mean), __builtin_amdgcn_rcpf(den)};
    }
    if (TAILST == 1) TSTAMP(9);
    BARRIER();
    if (DLY2) __builtin_amdgcn_s_sleep(DLY2);
    const float res = hres + rbf(rbf(gather16(rws, vin + IN_P2)) + asf(b2v));
    __builtin_amdgcn_raw_buffer_store_b16(__builtin_bit_cast(unsigned short, (__bf16)res), mk_rsrc(out, NB * D * 2), vout, 0, 0);
    { SB(); xt[4] = (unsigned)__builtin_amdgcn_s_memrealtime();
      __builtin_amdgcn_s_sleep(100);
      const rsrc_t rd = mk_rsrc(ws + (8 << 20), 8192);
      for (int k = 0; k < 5; ++k) __builtin_amdgcn_raw_buffer_store_b32(xt[k], rd, lane == 0 ? (k * 256 + blk) * 4 : OOB, 0, AUX_SC1);
      if (blk == 0) {
        while ((int)((unsigned)__builtin_amdgcn_s_memrealtime() - (xt[0] + 4000u)) < 0) {}
        float mn[5], mx[5], av[5], rv[5], rx[5];
        u32x4 s0 = __builtin_amdgcn_raw_buffer_load_b128(rd, lane * 16, 0, AUX_SC1);
        for (int k = 0; k < 5; ++k) {
          const u32x4 v = __builtin_amdgcn_raw_buffer_load_b128(rd, lane * 16, k * 1024, AUX_SC1);
          float a = 1e9f, c2_ = -1e9f, sm = 0.f, sr = 0.f, mr = -1e9f;
          for (int j = 0; j < 4; ++j) { const float f = (float)(int)(v[j] - xt[0]) * 0.01f, g = (float)(int)(v[j] - s0[j]) * 0.01f; a = fminf(a, f); c2_ = fmaxf(c2_, f); sm += f; sr += g; mr = fmaxf(mr, g); }
          mn[k] = -wave_max(-a); mx[k] = wave_max(c2_); av[k] = wave_sum(sm) * (1.f / 256); rv[k] = wave_sum(sr) * (1.f / 256); rx[k] = wave_max(mr);
        }
        auto q = [](float v, float base, float unit) { return (int)fminf(fmaxf((v - base) / unit + 0.5f, 0.f), 15.f); };
        const int f1 = q(mx[4] - mn[0], 23.0f, 0.2f);
        const int f2 = DV == 1 ? q(mx[0] - mn[0], 0.f, 0.15f) : DV == 2 ? q(rv[1], 0.5f, 0.1f) : DV == 3 ? q(rv[2], 8.5f, 0.15f) : q(mx[3] - av[3], 0.f, 0.15f);
        const int f3 = DV == 1 ? q(av[0] - mn[0], 0.f, 0.1f) : DV == 2 ? q(rx[1], 0.5f, 0.2f) : DV == 3 ? q(rv[3], 19.0f, 0.15f) : q(mx[4] - mx[3], 2.2f, 0.1f);
        const unsigned tgt = xt[0] + 5000u + 200u * (unsigned)__builtin_amdgcn_readfirstlane(f1 * 256 + f2 * 16 + f3);
        while ((int)((unsigned)__builtin_amdgcn_s_memrealtime() - tgt) < 0) {}
      }
    }
  } else if (w >= 4) {
    const int u = w - 4, q4 = (lane >> 2) & 3;
    const rsrc_t rws = mk_rsrc(ws, WS_BYTES);
    __builtin_amdgcn_s_sleep(3);
    u32x8 W1[6];
    LAS unsigned char* p1 = L + L_W + u * 32768 + (lane & 3) * 8 + (4 * (r >> 1) + 32 * (r & 1) + q4) * 128;
    sfor<6>([&](auto J) { W1[J] = tr_read8<16 * 128, 8 * 128>(p1 + (J & 1) * 16384 + (J >> 1) * 32); });
    sfor<6>([&](auto J) { PIN(W1[J]); });
    __builtin_amdgcn_s_sleep(6);
    int vhl = blk * IN_BYTES + IN_H + u * 256 + r * 32 + (lane & 1) * 16, lwr = lane * 16 + u * 4;
    int idx = (lane & 1) ? 0xEEEEEEEE : 0x44444444;
    const f32x4 zero4 = {0.f, 0.f, 0.f, 0.f};
    f32x4 e[3];
    auto project = [&](u32x4 h0, u32x4 h1, int dst) {
      sfor<3>([&](auto T) { e[T] = smf(h0, W1[2 * T], e[T], idx); SB(); e[T] = smf(h1, W1[2 * T + 1], e[T], idx); SB(); });
      float a0 = e[0][0] + e[0][1], a1 = e[1][0] + e[1][1];
      a0 = r == 0 ? a0 : a1;
      PIN(a0);
      const float a2 = e[2][0] + e[2][1];
      KEEP(e[0]); KEEP(e[1]); KEEP(e[2]);
      *(LAS float*)(L + dst + lwr) = r >= 2 ? a2 : a0;
    };
    const u32x4 ones = {0x3f803f80u, 0x3f803f80u, 0x3f803f80u, 0x3f803f80u};
    sfor<3>([&](auto T) { e[T] = zero4; });
    project(ones, ones, L_EX);
    sfor<3>([&](auto T) { e[T] = zero4; PIN(e[T]); });
    PIN(vhl); PIN(lwr); PIN(idx);
    STAMP(6);
    BARRIER();
    if (DLY1) __builtin_amdgcn_s_sleep(DLY1);
    u32x4 h0, h1;
    for (unsigned spins = 0; spins < SPIN_CAP; ++spins) {
      asm volatile("" ::: "memory");
      h0 = __builtin_amdgcn_raw_buffer_load_b128(rws, vhl, 0, AUX_SC1);
      h1 = __builtin_amdgcn_raw_buffer_load_b128(rws, vhl, 128, AUX_SC1);
      if (__builtin_amdgcn_ballot_w64(max(max4(h0), max4(h1)) == SENT) == 0) break;
    }
    SB();
    STAMP(8); TSTAMP(6);
    project(h0, h1, L_EX2);
    if (TAILST == 1) TSTAMP(8);
    BARRIER();
  }
  }
}

extern "C" void kernel_launch(void* const* d_in, const int* in_sizes, int n_in, void* d_out, int out_size, void* d_ws,
                              size_t ws_size, hipStream_t stream) {
  fused_block<<<NB * NH, 768, 0, stream>>>((const unsigned char*)d_in[3], (const unsigned char*)d_in[1], (const unsigned char*)d_in[4],
                                      (const unsigned char*)d_in[2], (const unsigned short*)d_in[0], (const unsigned char*)d_in[6],
                                      (const unsigned char*)d_in[7], (const unsigned char*)d_in[8], (const unsigned short*)d_in[9],
                                      (unsigned short*)d_out, (unsigned char*)d_ws);
}
```

```cpp
#define PEND 1900
#include <hip/hip_runtime.h>
#include <hip/hip_bf16.h>
#include <stdint.h>

#define STAMPS 0
#define PFCODE 24
#define WN 3
#define BWN 3
#define QB0 4
#define QRDY 9
#define QSTEP 28
#define OB0 14
#define MB0 35
#define QBS 8
#define OBS 12
#define MBS 12
#define NG 8
#define OWD 5
#define INB 8192
#define SENTH 0
#define SENTP 1
#define CHKS 61
#define PTAU 24
#define PBUR 90
#define PBUM 110
#define DITH 32
#define PSH 1
#define DITG 0
#define TSTEP 11
#define NREP 32
#define TOFF 175
#define OTOUCH 1
#define COLD 0
#define TFRAC 4
#define TCOM 1
#define SKEW 0
#define PFROM 16
#define PTO 60
#define PEV 2
#define SENTE 1
#define LOSE -1
#define EARLYST 0
#define TOUCH 0
#define SENTW 0
#define TAILST 0
#define HST 1
#define EXPM 0
#define DLY0 20
#define DLY1 11
#define DLY2 18

typedef unsigned u32x4 __attribute__((ext_vector_type(4)));
typedef unsigned u32x2 __attribute__((ext_vector_type(2)));
typedef float f32x2 __attribute__((ext_vector_type(2)));
typedef float f32x4 __attribute__((ext_vector_type(4)));
typedef __bf16 bf16x2 __attribute__((ext_vector_type(2)));
typedef short s16x4 __attribute__((ext_vector_type(4)));
typedef unsigned u32x8 __attribute__((ext_vector_type(8)));
typedef __bf16 bf16x8 __attribute__((ext_vector_type(8)));
typedef __bf16 bf16x16 __attribute__((ext_vector_type(16)));
typedef __amdgpu_buffer_rsrc_t rsrc_t;
#define LAS __attribute__((address_space(3)))
#define DI __device__ __forceinline__

constexpr int NB = 8, D = 512, NH = 32, SK = 4096, DH = 128, WIN = 1024, F = 1376;
constexpr int FS = 48;
constexpr int AUX_NT = 2, AUX_SC1 = 16;
constexpr int OOB = 0x7ffffff0;
constexpr unsigned SENT = 0xffffffffu;
constexpr unsigned SPIN_CAP = 1u << 17;
constexpr int IN_P1 = 0;
constexpr int IN_P2 = 3072;
constexpr int IN_H = 2048;
constexpr int IN_BYTES = INB;
constexpr int WS_T = 256 * IN_BYTES;
constexpr int WS_BYTES = WS_T + 128;
constexpr int WS_STAMP = 4 * 1024 * 1024;
constexpr int L_RED = 0;
constexpr int L_RED2 = L_RED + 2048;
constexpr int L_EX = L_RED2 + 4096;
constexpr int L_EX2 = L_EX + 1024;
constexpr int L_ML = L_EX2 + 1024;
constexpr int L_ST = L_ML + 64;
constexpr int L_T = L_ST + 16;
constexpr int L_Y = 9216;
constexpr int L_DUMP = L_Y + 2048;
constexpr int L_W = L_DUMP + 1024;
constexpr int L_BYTES = L_W + 128 * 1024;

DI float dot2(unsigned a, unsigned b, float acc) {
  return __builtin_amdgcn_fdot2_f32_bf16(__builtin_bit_cast(bf16x2, a), __builtin_bit_cast(bf16x2, b), acc, false);
}
DI unsigned fbits(float f) { return __builtin_bit_cast(unsigned, f); }
DI float asf(unsigned u) { return __builtin_bit_cast(float, u); }
DI unsigned cvtpk(float lo, float hi) {
  bf16x2 r = {(__bf16)lo, (__bf16)hi};
  return __builtin_bit_cast(unsigned, r);
}
DI float rbf(float f) { return asf((unsigned)__builtin_bit_cast(unsigned short, (__bf16)f) << 16); }
template <int CTRL> DI unsigned dppu(unsigned x) { return __builtin_amdgcn_mov_dpp(x, CTRL, 0xf, 0xf, true); }
template <int CTRL> DI float dppf(float x) { return asf(dppu<CTRL>(fbits(x))); }
constexpr int XOR1 = 0xB1, XOR2 = 0x4E, HMIR = 0x141, ROR4 = 0x124, ROR8 = 0x128, BCAST = 0x150;
DI float tsum32(float a, float b) {
  auto s = __builtin_amdgcn_permlane32_swap(fbits(a), fbits(b), false, false);
  return asf(s[0]) + asf(s[1]);
}
DI float tsum16(float a, float b) {
  auto s = __builtin_amdgcn_permlane16_swap(fbits(a), fbits(b), false, false);
  return asf(s[0]) + asf(s[1]);
}
DI float row_sum(float x) { x += dppf<XOR1>(x); x += dppf<XOR2>(x); x += dppf<ROR4>(x); x += dppf<ROR8>(x); return x; }
DI float row_max(float x) { x = fmaxf(x, dppf<XOR1>(x)); x = fmaxf(x, dppf<XOR2>(x)); x = fmaxf(x, dppf<ROR4>(x)); x = fmaxf(x, dppf<ROR8>(x)); return x; }
DI float wave_sum(float x) { x = row_sum(x); x = tsum16(x, x); return tsum32(x, x); }
DI float wave_max(float x) {
  x = row_max(x);
  auto s = __builtin_amdgcn_permlane16_swap(fbits(x), fbits(x), false, false);
  x = fmaxf(asf(s[0]), asf(s[1]));
  auto t = __builtin_amdgcn_permlane32_swap(fbits(x), fbits(x), false, false);
  return fmaxf(asf(t[0]), asf(t[1]));
}
DI void rows_bcast(unsigned x, unsigned (&o)[4]) {
  auto s = __builtin_amdgcn_permlane16_swap(x, x, false, false);
  auto a = __builtin_amdgcn_permlane32_swap(s[0], s[0], false, false);
  auto b = __builtin_amdgcn_permlane32_swap(s[1], s[1], false, false);
  o[0] = a[0]; o[2] = a[1]; o[1] = b[0]; o[3] = b[1];
}
DI f32x4 smf(u32x4 a, u32x8 b, f32x4 c, int idx) {
  return __builtin_amdgcn_smfmac_f32_16x16x64_bf16(__builtin_bit_cast(bf16x8, a), __builtin_bit_cast(bf16x16, b), c, idx, 0, 0);
}
DI f32x4 mfma(u32x4 a, u32x4 b, f32x4 c) {
  return __builtin_amdgcn_mfma_f32_16x16x32_bf16(__builtin_bit_cast(bf16x8, a), __builtin_bit_cast(bf16x8, b), c, 0, 0, 0);
}
constexpr int SHL = 0x100;
template <class T> DI void pin_(T& x) { asm volatile("" : "+v"(x)); }
#define PIN(x) pin_(x)
template <class T> DI void keep_(const T& x) { asm volatile("" :: "v"(x)); }
#define KEEP(x) keep_(x)
template <class T> DI void pins_(T& x) { asm volatile("" : "+s"(x)); }
#define PINS(x) pins_(x)
#define SB() __builtin_amdgcn_sched_barrier(0)
#define PACEN(n) do { asm volatile("s_waitcnt vmcnt(%0)" :: "i"(n) : "memory"); SB(); } while (0)
#define PACE() PACEN(WN)
#define BARRIER() do { asm volatile("s_waitcnt lgkmcnt(0)\n\ts_barrier" ::: "memory"); SB(); } while (0)
DI rsrc_t mk_rsrc(const void* p, int bytes) { return __builtin_amdgcn_make_buffer_rsrc((void*)p, 0, bytes, 0x00020000); }

template <int G, int N, int CNT = N> DI void burst(int grp, rsrc_t r, int voff, int stride, int lds, int j0) {
  int keep, j, cnt, t;
  asm volatile(
      "s_cmp_lg_u32 %4, %10\n\t"
      "s_cbranch_scc1 BE%=\n\t"
      "s_mov_b32 %0, m0\n\t"
      "s_mov_b32 %1, %9\n\t"
      "s_mov_b32 %2, %12\n"
      "BL%=:\n\t"
      "s_lshl_b32 %3, %1, 10\n\t"
      "s_add_u32 m0, %3, %8\n\t"
      "s_mul_i32 %3, %1, %7\n\t"
      "s_add_u32 %1, %1, 1\n\t"
      "s_waitcnt vmcnt(%13)\n\t"
      "buffer_load_dwordx4 %6, %5, %3 offen lds\n\t"
      "s_cmp_eq_u32 %1, %11\n\t"
      "s_cselect_b32 %1, 0, %1\n\t"
      "s_sub_u32 %2, %2, 1\n\t"
      "s_cmp_lg_u32 %2, 0\n\t"
      "s_cbranch_scc1 BL%=\n\t"
      "s_mov_b32 m0, %0\n"
      "BE%=:"
      : "=&s"(keep), "=&s"(j), "=&s"(cnt), "=&s"(t)
      : "s"(grp), "s"(r), "v"(voff), "s"(stride), "s"(lds), "s"(j0), "i"(G), "i"(N), "i"(CNT), "i"(BWN)
      : "memory", "scc");
  SB();
}
DI void ald128nt(u32x4& d, rsrc_t r, int voff, int soff) { asm volatile("buffer_load_dwordx4 %0, %1, %2, %3 offen nt" : "=v"(d) : "v"(voff), "s"(r), "s"(soff) : "memory"); SB(); }
DI void ald128(u32x4& d, rsrc_t r, int voff, int soff) { asm volatile("buffer_load_dwordx4 %0, %1, %2, %3 offen" : "=v"(d) : "v"(voff), "s"(r), "s"(soff) : "memory"); SB(); }
DI void ald64sc(u32x2& d, rsrc_t r, int voff, int soff) { asm volatile("buffer_load_dwordx2 %0, %1, %2, %3 offen sc1" : "=v"(d) : "v"(voff), "s"(r), "s"(soff) : "memory"); SB(); }
DI void ald32sc(unsigned& d, rsrc_t r, int voff) { asm volatile("buffer_load_dword %0, %1, %2, 0 offen sc1" : "=v"(d) : "v"(voff), "s"(r) : "memory"); SB(); }
DI void ald32(unsigned& d, rsrc_t r, int voff) { asm volatile("buffer_load_dword %0, %1, %2, 0 offen" : "=v"(d) : "v"(voff), "s"(r) : "memory"); SB(); }
DI void ald16(unsigned& d, rsrc_t r, int voff, int soff) { asm volatile("buffer_load_ushort %0, %1, %2, %3 offen" : "=v"(d) : "v"(voff), "s"(r), "s"(soff) : "memory"); SB(); }
template <bool NT = false> DI void dma1(rsrc_t r, int voff, int soff, int lds) {
  int keep;
  if constexpr (NT)
    asm volatile("s_mov_b32 %0, m0\n\ts_mov_b32 m0, %4\n\ts_nop 0\n\tbuffer_load_dwordx4 %1, %2, %3 offen nt lds\n\ts_mov_b32 m0, %0"
                 : "=&s"(keep) : "v"(voff), "s"(r), "s"(soff), "s"(lds) : "memory");
  else
    asm volatile("s_mov_b32 %0, m0\n\ts_mov_b32 m0, %4\n\ts_nop 0\n\tbuffer_load_dwordx4 %1, %2, %3 offen lds\n\ts_mov_b32 m0, %0"
                 : "=&s"(keep) : "v"(voff), "s"(r), "s"(soff), "s"(lds) : "memory");
  SB();
}
template <int G> DI void cond_barrier(int grp) {
  asm volatile("s_cmp_lg_u32 %0, %1\n\ts_cbranch_scc1 CB%=\n\ts_barrier\nCB%=:" :: "s"(grp), "i"(G) : "memory", "scc");
  SB();
}
DI float gather16(rsrc_t r, int voff);
DI u32x2 tr_read(LAS unsigned char* p) {
  return __builtin_bit_cast(u32x2, __builtin_amdgcn_ds_read_tr16_b64_v4i16((LAS s16x4*)p));
}
template <int M1, int M2> DI u32x8 tr_read8(LAS unsigned char* p) {
  const u32x2 a = tr_read(p), b = tr_read(p + M1), c = tr_read(p + M2), d = tr_read(p + M1 + M2);
  return u32x8{a[0], a[1], b[0], b[1], c[0], c[1], d[0], d[1]};
}
DI f32x2 fold_rows8(const float (&acc)[8]) {
  const float t0 = tsum32(acc[0], acc[4]), t1 = tsum32(acc[1], acc[5]), t2 = tsum32(acc[2], acc[6]), t3 = tsum32(acc[3], acc[7]);
  return f32x2{tsum16(t0, t2), tsum16(t1, t3)};
}
DI float fold_rows4(float a0, float a1, float a2, float a3) { return tsum16(tsum32(a0, a2), tsum32(a1, a3)); }
DI unsigned max4(u32x4 v) { return max(max(v[0], v[1]), max(v[2], v[3])); }

DI float gather16(rsrc_t r, int voff) {
  u32x4 A, Bv;
  for (unsigned spins = 0; spins < SPIN_CAP; ++spins) {
    asm volatile("" ::: "memory");
    A = __builtin_amdgcn_raw_buffer_load_b128(r, voff, 0, AUX_SC1);
    Bv = __builtin_amdgcn_raw_buffer_load_b128(r, voff, 1024, AUX_SC1);
    if (__builtin_amdgcn_ballot_w64(max(max4(A), max4(Bv)) == SENT) == 0) break;
  }
  const f32x4 s = __builtin_bit_cast(f32x4, A) + __builtin_bit_cast(f32x4, Bv);
  float t = fold_rows4(s[0], s[1], s[2], s[3]);
  t += dppf<ROR4>(t); t += dppf<ROR8>(t);
  return t;
}

template <int I> DI void pv_step(float (&acc)[8], unsigned plo, unsigned phi, u32x4 v) {
  const unsigned lo = dppu<BCAST + I>(plo), hi = dppu<BCAST + I>(phi);
#pragma unroll
  for (int k = 0; k < 4; ++k) { acc[2 * k] = dot2(lo, v[k], acc[2 * k]); acc[2 * k + 1] = dot2(hi, v[k], acc[2 * k + 1]); }
}
DI void pin8(float (&a)[8]) {
  asm volatile("" : "+v"(a[0]), "+v"(a[1]), "+v"(a[2]), "+v"(a[3]), "+v"(a[4]), "+v"(a[5]), "+v"(a[6]), "+v"(a[7]));
}
template <int... Is> struct iseq {};
template <int N, int... Is> struct mkseq : mkseq<N - 1, N - 1, Is...> {};
template <int... Is> struct mkseq<0, Is...> { typedef iseq<Is...> type; };
template <class Fn, int... Is> DI void sfor_(Fn&& f, iseq<Is...>) { (f(std::integral_constant<int, Is>{}), ...); }
template <int N, class Fn> DI void sfor(Fn&& f) { sfor_(f, typename mkseq<N>::type{}); }

DI float transpose_reduce16(float (&x)[16], int c) {
#define TR_STEP(HALF, CTRL, BIT) _Pragma("unroll") for (int h_ = 0; h_ < (HALF); ++h_) { \
    const float lo_ = x[h_], hi_ = x[h_ + (HALF)]; const float t1_ = lo_ + dppf<CTRL>(lo_), t2_ = hi_ + dppf<CTRL>(hi_); \
    x[h_] = (c & (BIT)) ? t2_ : t1_; }
  TR_STEP(8, ROR8, 8) TR_STEP(4, HMIR, 4) TR_STEP(2, XOR2, 2) TR_STEP(1, XOR1, 1)
#undef TR_STEP
  return x[0];
}

#define STAMP(i) do {} while (0)
#define ESTAMP(i) do {} while (0)
#define TSTAMP(i) do {} while (0)

extern "C" __global__ void __launch_bounds__(768, 3)
fused_block(const unsigned char* __restrict__ wq, const unsigned char* __restrict__ kc, const unsigned char* __restrict__ wo,
            const unsigned char* __restrict__ vc, const unsigned short* __restrict__ x, const unsigned char* __restrict__ w1,
            const unsigned char* __restrict__ b1, const unsigned char* __restrict__ w2, const unsigned short* __restrict__ b2,
            unsigned short* __restrict__ out, unsigned char* ws) {
  __shared__ __attribute__((aligned(16))) unsigned char smem[L_BYTES];
  LAS unsigned char* L = (LAS unsigned char*)smem;
  if (SKEW && (blockIdx.x & 7) == 5) __builtin_amdgcn_s_sleep(SKEW);
  const unsigned long long xt0 = __builtin_amdgcn_s_memrealtime();
  const int blk = blockIdx.x, b = blk >> 5, h = blk & 31;
  const int tid = threadIdx.x, lane = tid & 63, r = lane >> 4, c = lane & 15;
  const int w = __builtin_amdgcn_readfirstlane(tid >> 6);
  const int slice = (h & 7) * 4 + (h >> 3), col0 = slice * FS;
#define IC(n) std::integral_constant<int, (n)>{}
  if (LOSE >= 0 && (blk == LOSE || blk == LOSE + 5)) for (int i = 0; i < (blk == LOSE ? 12 : 30); ++i) __builtin_amdgcn_s_sleep(127);
  {
    int w2 = w;
    PINS(w2);
    if (w2 >= 8) {
      if (COLD) { asm volatile("s_waitcnt lgkmcnt(0)"); __builtin_amdgcn_s_sleep(COLD); }
      const rsrc_t r0 = mk_rsrc(ws, WS_BYTES);
      u32x4 s4 = {SENT, SENT, SENT, SENT};
      PIN(s4);
      if (w2 >= 9) {
        if (!(EXPM & 1)) __builtin_amdgcn_raw_buffer_store_b128(s4, r0, lane * 16, blk * IN_BYTES + (w2 - 9) * 1024, AUX_SC1);
      } else if (TCOM && (blk & 0xd0) == 0x80) {
        unsigned pr = __builtin_amdgcn_raw_buffer_load_b32(mk_rsrc(x, 4), lane * 4, 0, 0);
        PIN(pr);
        SB();
        __builtin_amdgcn_raw_buffer_store_b32((unsigned)__builtin_amdgcn_s_memrealtime(), r0, lane == 0 ? (((blk >> 1) & 16) | (blk & 15)) * 4 : OOB, WS_T, AUX_SC1);
      }
      SB(); KEEP(s4);
    }
    SB();
  }
  if (w >= 8) {
  const int u = w - 8;
  const rsrc_t rws = mk_rsrc(ws, WS_BYTES);
  const u32x4 xrow = *(const u32x4*)(x + b * D + lane * 8);
  const float xe0 = asf((unsigned)x[b * D + 128 * u + 4 * c + r] << 16), xe1 = asf((unsigned)x[b * D + 128 * u + 64 + 4 * c + r] << 16);
  float b1v = asf((unsigned)__builtin_amdgcn_raw_buffer_load_b16(mk_rsrc(b1, F * 2), lane < FS ? (col0 + lane) * 2 : OOB, 0, 0) << 16);
  if (PFCODE && (blk >> 3) == PFCODE && u >= 1) {
    unsigned long long pc; int o0, o1;
    asm volatile("s_getpc_b64 %0\nKPC0:\n\ts_mov_b32 %1, KPC0-KPCA\n\ts_mov_b32 %2, KPCB-KPCA" : "=s"(pc), "=s"(o0), "=s"(o1));
    const rsrc_t rc = mk_rsrc((const void*)(pc - (long long)o0), o1);
    unsigned p0 = __builtin_amdgcn_raw_buffer_load_b32(rc, lane * 128, (u - 1) * 8192, 0);
    PIN(p0);
  }
  SB();
  {
    float xf[8];
#pragma unroll
    for (int k = 0; k < 4; ++k) { xf[2 * k] = asf(xrow[k] << 16); xf[2 * k + 1] = asf(xrow[k] & 0xffff0000u); }
    const float mean = wave_sum(((xf[0] + xf[1]) + (xf[2] + xf[3])) + ((xf[4] + xf[5]) + (xf[6] + xf[7]))) * (1.f / D);
    float vs = 0.f;
#pragma unroll
    for (int k = 0; k < 8; ++k) { const float d = xf[k] - mean; vs = fmaf(d, d, vs); }
    const float var = wave_sum(vs) * (1.f / D);
    const float den = rbf(sqrtf(rbf(rbf(var) + 1e-5f))), mb = rbf(mean);
    const unsigned xp = cvtpk(rbf(xe0 - mb) / den, rbf(xe1 - mb) / den);
    unsigned xlo[2] = {xp & 0xffffu, xp >> 16}, xhi[2] = {xp << 16, xp & 0xffff0000u};
    PIN(xlo[0]); PIN(xlo[1]); PIN(xhi[0]); PIN(xhi[1]); PIN(b1v);
    PACEN(0);
    ESTAMP(5);
    BARRIER();
    ESTAMP(6);
    float acc[8] = {0.f, 0.f, 0.f, 0.f, 0.f, 0.f, 0.f, 0.f};
    sfor<32>([&](auto I) { pv_step<I & 15>(acc, xlo[I >> 4], xhi[I >> 4], *(const LAS u32x4*)(L + L_W + u * 32768 + I * 1024 + lane * 16)); });
    *(LAS f32x2*)(L + L_RED + (u >> 1) * 1024 + lane * 16 + (u & 1) * 8) = fold_rows8(acc);
  }
  STAMP(11);
  BARRIER();
  BARRIER();
  u32x8 WO[16];
  const int q4 = (lane >> 2) & 3;
  LAS unsigned char* lw = L + L_W + u * 32768 + (lane & 3) * 8;
  {
    LAS unsigned char* p = lw + (32 * (r >> 1) + 4 * (r & 1) + (q4 >> 1) + 8 * (q4 & 1)) * 128;
    sfor<16>([&](auto J) { WO[J] = tr_read8<2 * 128, 64 * 128>(p + (J >> 3) * 16384 + ((J >> 1) & 3) * 32 + (J & 1) * 16 * 128); });
    sfor<16>([&](auto J) { PIN(WO[J]); });
  }
  BARRIER();
  BARRIER();
  int vp = lane < 16 ? (b * 32 + 8 * u + (lane >> 2)) * IN_BYTES + (lane & 3) * 16 : OOB;
  int lrd = lane * 16;
  int idx = (lane & 1) ? 0xEEEEEEEE : 0x44444444;
  PIN(vp); PIN(lrd); PIN(idx);
  const f32x4 zero4 = {0.f, 0.f, 0.f, 0.f};
  f32x4 d[8];
  d[0] = zero4; d[1] = zero4; PIN(d[0]); PIN(d[1]);
  int so1 = IN_P1 + h * 64, so2 = IN_P1 + h * 64 + 4 * IN_BYTES;
  PINS(so1); PINS(so2);
  BARRIER();
  TSTAMP(2);
  {
    f32x4 s4 = *(const LAS f32x4*)(L + L_RED2 + lrd) + *(const LAS f32x4*)(L + L_RED2 + 1024 + lrd);
    s4 += *(const LAS f32x4*)(L + L_RED2 + 2048 + lrd) + *(const LAS f32x4*)(L + L_RED2 + 3072 + lrd);
    const unsigned op = cvtpk(s4[0] + s4[2], s4[1] + s4[3]);
    const u32x4 a0 = {op, dppu<SHL + 2>(op), dppu<SHL + 4>(op), dppu<SHL + 6>(op)};
    const u32x4 a1 = {dppu<SHL + 8>(op), dppu<SHL + 10>(op), dppu<SHL + 12>(op), dppu<SHL + 14>(op)};
    float y[8];
    auto fin = [&](auto T) { y[T] = d[T][0] + d[T][1]; PIN(y[T]); KEEP(d[T]); };
    sfor<8>([&](auto T) {
      d[T] = smf(a0, WO[2 * T], d[T], idx);
      if constexpr (T > 0) SB();
      if constexpr (T + 2 < 8 && T > 0) { d[T + 2] = zero4; PIN(d[T + 2]); }
      SB();
      d[T] = smf(a1, WO[2 * T + 1], d[T], idx);
      SB();
      if constexpr (T == 0) { d[2] = zero4; PIN(d[2]); }
      if constexpr (T >= 2) fin(IC(T - 2));
      if constexpr (T == 6)
        __builtin_amdgcn_raw_buffer_store_b128(u32x4{fbits(y[0]), fbits(y[1]), fbits(y[2]), fbits(y[3])}, rws, (EXPM & 8) ? OOB : vp, so1, AUX_SC1);
      SB();
    });
    fin(IC(6)); fin(IC(7));
    __builtin_amdgcn_raw_buffer_store_b128(u32x4{fbits(y[4]), fbits(y[5]), fbits(y[6]), fbits(y[7])}, rws, (EXPM & 8) ? OOB : vp, so2, AUX_SC1);
    SB();
    if (lrd < 256) {
      *(LAS f32x4*)(L + L_Y + u * 512 + lrd) = f32x4{y[0], y[1], y[2], y[3]};
      *(LAS f32x4*)(L + L_Y + u * 512 + 256 + lrd) = f32x4{y[4], y[5], y[6], y[7]};
    }
  }
  SB();
  STAMP(5); TSTAMP(3);
  u32x8 W2[8];
  {
    const int ln = __builtin_amdgcn_mbcnt_hi(~0u, __builtin_amdgcn_mbcnt_lo(~0u, 0u)), rr = ln >> 4, qq = (ln >> 2) & 3;
    LAS unsigned char* p2 = L + L_W + u * 32768 + (ln & 3) * 8 + 8192 + (16 * (rr >> 1) + 8 * (rr & 1) + (qq >> 1) + 2 * (qq & 1)) * 128;
    LAS unsigned char* p3 = p2 + (32 - 16 * (rr >> 1)) * 128;
    sfor<8>([&](auto J) {
      constexpr int o = (J >> 2) * 16384 + (J & 3) * 32;
      const u32x2 t0 = tr_read(p2 + o), t1 = tr_read(p2 + o + 4 * 128), t2 = tr_read(p3 + o), t3 = tr_read(p3 + o + 4 * 128);
      W2[J] = u32x8{t0[0], t0[1], t1[0], t1[1], t2[0], t2[1], t3[0], t3[1]};
    });
    sfor<8>([&](auto J) { PIN(W2[J]); d[J] = zero4; PIN(d[J]); });
  }
  so1 = IN_P2 + slice * 64; so2 = IN_P2 + slice * 64 + 4 * IN_BYTES;
  int lst = L_ST;
  float gk = -2.f * 1.4426950408889634f * 0.7978845608028654f;
  unsigned gmask = lrd < FS * 16 ? ~0u : 0u;
  PINS(so1); PINS(so2); PIN(lst); PIN(gk); PIN(gmask);
  SB();
  STAMP(6);
  BARRIER();
  float c1;
  { const f32x4 t = *(const LAS f32x4*)(L + L_EX + lrd); c1 = (t[0] + t[1]) + (t[2] + t[3]); }
  PIN(c1);
  BARRIER();
  if (TAILST == 2) TSTAMP(8);
  {
    const f32x4 t4 = *(const LAS f32x4*)(L + L_EX2 + lrd);
    const f32x2 ms = *(const LAS f32x2*)(L + lst);
    const float tot = (t4[0] + t4[1]) + (t4[2] + t4[3]);
    const float uu = rbf(rbf(fmaf(-ms[0], c1, tot) * ms[1]) + b1v);
    const float z = uu * fmaf(uu * uu, -2.f * 1.4426950408889634f * 0.044715f * 0.7978845608028654f, gk);
    const float g = asf(fbits(uu) & gmask) * __builtin_amdgcn_rcpf(1.f + __builtin_amdgcn_exp2f(z));
    const unsigned gp = cvtpk(g, dppf<XOR2>(g));
    const u32x4 ag = {gp, dppu<SHL + 4>(gp), dppu<SHL + 8>(gp), dppu<SHL + 12>(gp)};
    if (TAILST == 2) { KEEP(ag); TSTAMP(9); }
    float y[8];
    auto fin = [&](auto T) { y[T] = d[T][0] + d[T][1]; PIN(y[T]); KEEP(d[T]); SB(); };
    sfor<8>([&](auto T) { d[T] = smf(ag, W2[T], d[T], idx); SB(); });
    fin(IC(0)); fin(IC(1)); fin(IC(2)); fin(IC(3));
    __builtin_amdgcn_raw_buffer_store_b128(u32x4{fbits(y[0]), fbits(y[1]), fbits(y[2]), fbits(y[3])}, rws, vp, so1, AUX_SC1);
    SB();
    fin(IC(4)); fin(IC(5)); fin(IC(6)); fin(IC(7));
    KEEP(y[0]); KEEP(y[1]); KEEP(y[2]); KEEP(y[3]);
    __builtin_amdgcn_raw_buffer_store_b128(u32x4{fbits(y[4]), fbits(y[5]), fbits(y[6]), fbits(y[7])}, rws, vp, so2, AUX_SC1);
  }
  asm volatile("KPCB:");
  SB();
  STAMP(9); TSTAMP(10);
  } else {
  const int grp = NG == 8 ? (b + 2 * (h >> 3)) & 7 : (NG == 16 ? (2 * b + 4 * (h >> 4) + ((h >> 3) & 1)) & 15 : 4 * b + (h >> 3));
  const int pq = QB0 + ((grp * QBS) >> 2), po = OB0 + ((grp * OBS) >> 2), pm = MB0 + ((grp * MBS) >> 2);
  const size_t kvoff = ((size_t)blk * SK + (SK - WIN)) * (DH * 2);
  const rsrc_t rk = mk_rsrc(kc + kvoff, WIN * DH * 2), rv = mk_rsrc(vc + kvoff, WIN * DH * 2);
  const int vkv = tid * 16;
  const int lds0 = (int)(unsigned)(size_t)L, ldsw = lds0 + L_W + w * 16384, ldsd = lds0 + L_DUMP;

  u32x4 K[32], V[32];
  auto ldk = [&](auto I) { ald128nt(K[I], rk, vkv, I * 8192); };
  auto ldv = [&](auto I) { ald128nt(V[I], rv, vkv, I * 8192); };
  ldk(IC(0)); ldk(IC(1));
  const unsigned long long tclk = __builtin_amdgcn_s_memrealtime();
  SB();
  asm volatile("KPCA:");
  const rsrc_t rq = mk_rsrc(wq + h * (DH * 2), D * NH * DH * 2 - h * (DH * 2));
  const int vq = (64 * w + r) * (NH * DH * 2) + c * 16;
  dma1(rq, vq, 2 * b * (4 * NH * DH * 2), ldsw + 2 * b * 1024);
  dma1(rq, vq, (2 * b + 1) * (4 * NH * DH * 2), ldsw + (2 * b + 1) * 1024);
  unsigned tch = 0;
  if (TOUCH) {
    const unsigned char* p = w == 0 ? vc + kvoff : w == 1 ? wo + (size_t)h * (DH * D * 2) : w == 2 ? w1 + 256 * F * 2 : w == 3 ? w2 + (size_t)min(col0, F - 1) * (D * 2)
                           : w == 4 ? ws + blk * IN_BYTES + 5120 : w == 5 ? (const unsigned char*)out : w == 6 ? (const unsigned char*)b2 : b1;
    ald32(tch, mk_rsrc(p, 4), lane == 0 ? 0 : OOB);
  }
  const rsrc_t ro = mk_rsrc(wo + (size_t)h * (DH * D * 2), DH * D * 2);
  const int vo = (lane >> 3) * (D * 2) + w * 128 + (lane & 7) * 16;
  const rsrc_t r1 = mk_rsrc(w1, D * F * 2);
  const int v1 = ((lane & 7) < 6 && col0 + (lane & 7) * 8 < F) ? (64 * w + (lane >> 3)) * (F * 2) + col0 * 2 + (lane & 7) * 16 : OOB;
  const int c2 = min(col0, F);
  const rsrc_t r2 = mk_rsrc(w2 + (size_t)c2 * (D * 2), (F - c2) * (D * 2));
  const int b6 = b < 6 ? b : b - 6;
  const int dith = (((w - 4) * DITH) >> 3) + ((((blk >> 3) - 16) * DITG) >> 5);
  unsigned tst = PEND ? (unsigned)tclk + TOFF + dith : 0u;
  auto hook = [&](auto I) {
    constexpr int i = I;
    if constexpr (PEND && i >= PFROM && i <= PTO && i % PEV == 0) {
      PINS(tst);
      const int pend = (i <= pq ? PBUR : 0) + (i <= po ? PBUR : 0) + (i <= pm ? PBUM : 0);
      constexpr int extra = (i <= OB0 - OWD ? 2 : 0) + (i <= MB0 - OWD ? 2 : 0);
      const unsigned tgt = tst + PEND - (67 - i + extra) * PTAU - pend;
      int tmp;
      asm volatile("s_memrealtime vcc\n\ts_waitcnt lgkmcnt(0)\n\ts_sub_i32 %0, %1, vcc_lo\n\ts_ashr_i32 %0, %0, %2\n\ts_cmp_lt_i32 %0, 3\n\ts_cbranch_scc1 PE%=\n\t"
                   "s_add_i32 %0, %0, vcc_lo\n"
                   "PC%=:\n\ts_memrealtime vcc\n\ts_waitcnt lgkmcnt(0)\n\ts_sub_i32 vcc_lo, vcc_lo, %0\n\ts_cmp_lt_i32 vcc_lo, 0\n\ts_cbranch_scc1 PC%=\nPE%=:"
                   : "=&s"(tmp) : "s"(tgt), "i"(PSH) : "scc", "vcc", "memory");
      SB();
    }
    constexpr auto site = [](int i_, int p0, int sp) { for (int g = 0; g < NG; ++g) if (p0 + ((g * sp) >> 2) == i_) return true; return false; };
    if constexpr (site(i - 4, QB0, QBS)) cond_barrier<i - 4>(pq);
    if constexpr (site(i - QRDY, QB0, QBS)) cond_barrier<i - QRDY>(pq);
    if constexpr (site(i - 4, OB0, OBS)) cond_barrier<i - 4>(po);
    if constexpr (site(i, MB0, MBS)) cond_barrier<i>(pm);
    if constexpr (site(i, QB0, QBS)) burst<i, 16, 14>(pq, rq, vq, 4 * NH * DH * 2, ldsw, (2 * b + 2) & 15);
    if constexpr (i == OB0 - OWD) { PACE(); dma1(ro, vo, 2 * b * 8192, ldsd); PACE(); dma1(ro, vo, (2 * b + 1) * 8192, ldsd); }
    if constexpr (site(i, OB0, OBS)) burst<i, 16>(po, ro, vo, 8 * D * 2, ldsw, 2 * b);
    if constexpr (i == MB0 - OWD) { PACE(); dma1(r1, v1, b * (8 * F * 2), ldsd); PACE(); dma1(r2, vo, b * (8 * D * 2), ldsd); }
    if constexpr (site(i, MB0, MBS)) {
      burst<i, 8>(pm, r1, v1, 8 * F * 2, ldsw, b);
      burst<i, 6>(pm, r2, vo, 8 * D * 2, ldsw + 8192, b6);
    }
  };
  static_assert(QRDY > 4 && QB0 + (NG - 1) * QBS / 4 + QRDY < QSTEP && OBS >= QBS && OB0 >= QB0 + QRDY && MB0 > OB0 + 4 &&
                MB0 + (NG - 1) * MBS / 4 > OB0 + (NG - 1) * OBS / 4 + 5 && MB0 + (NG - 1) * MBS / 4 <= CHKS && CHKS >= 52 && CHKS <= 63, "barrier order, staging area reuse");
  unsigned tx;
  constexpr int S0 = 2;
  sfor<QSTEP - S0>([&](auto I) { constexpr int i = I + S0; if constexpr (i == 11) ESTAMP(7); if constexpr (i == 2) ESTAMP(8); if constexpr (i == 3) ESTAMP(9); hook(IC(i));
    PACEN(WN + (TCOM && i > TSTEP && i < TSTEP + 4 ? 1 : 0)); ldk(IC(i));
    if constexpr (TCOM && i == TSTEP) ald32sc(tx, mk_rsrc(ws + WS_T, NREP * 4), (lane & 31) * 4);
    if constexpr (TCOM && i == TSTEP + 4) {
      PIN(tx);
      const int dt = (int)(tx - (unsigned)tclk) - TOFF;
      const bool ok = dt > -400 && dt < 400;
      float sm = ok ? (lane < 32 ? (float)dt : 1.f) : 0.f;
      sm = row_sum(tsum16(sm, sm));
      const float cnt = asf(__builtin_amdgcn_readlane(fbits(sm), 32));
      int adj = (int)(sm * __builtin_amdgcn_rcpf(fmaxf(cnt, 1.f)));
      PIN(adj);
      tst += __builtin_amdgcn_readfirstlane(adj);
      SB();
    }
  });
  PIN(tch);
  STAMP(1);
  unsigned qp[4];
  {
    const f32x4 s4 = *(const LAS f32x4*)(L + L_RED + lane * 16) + *(const LAS f32x4*)(L + L_RED + 1024 + lane * 16);
    rows_bcast(cvtpk(s4[0] + s4[2], s4[1] + s4[3]), qp);
  }
  SB();
  STAMP(2);
  float sc[2], xs[16];
  auto qk = [&](u32x4& kv) { PIN(kv); return dot2(kv[3], qp[3], dot2(kv[2], qp[2], dot2(kv[1], qp[1], dot2(kv[0], qp[0], 0.f)))); };
  static_assert(QSTEP <= 28, "");
  sfor<36 - QSTEP>([&](auto I) {
    constexpr int i = I + QSTEP;
    hook(IC(i));
    PACE();
    if constexpr (i < 32) ldk(IC(i)); else ldv(IC(i - 32));
    if constexpr (I < 3) sfor<(I < 2 ? 6 : 4)>([&](auto J) { xs[6 * I + J] = qk(K[6 * I + J]); PIN(xs[6 * I + J]); });
    if constexpr (I == 3) { sc[0] = transpose_reduce16(xs, c); PIN(sc[0]); }
    if constexpr (I >= 4) {
      constexpr int lo = (I == 4) ? 16 : (16 + 6 * (I - 4) < i - 4 ? 16 + 6 * (I - 4) : i - 4);
      constexpr int hi = (16 + 6 * (I - 3) < i - 3) ? 16 + 6 * (I - 3) : i - 3;
      sfor<hi - lo>([&](auto J) { xs[lo - 16 + J] = qk(K[lo + J]); PIN(xs[lo - 16 + J]); });
    }
    SB();
  });
  sc[1] = transpose_reduce16(xs, c);
  SB();
  hook(IC(36)); PACE(); ldv(IC(4));
  unsigned pp[2];
  float mw, e1;
  {
    const float s0 = rbf(rbf(sc[0]) * 0.08838834764831845f), s1 = rbf(rbf(sc[1]) * 0.08838834764831845f);
    mw = wave_max(fmaxf(s0, s1));
    const float e0 = __builtin_amdgcn_exp2f((s0 - mw) * 1.4426950408889634f);
    e1 = __builtin_amdgcn_exp2f((s1 - mw) * 1.4426950408889634f);
    pp[0] = cvtpk(e0, e0); pp[1] = cvtpk(e1, e1);
    SB();
    hook(IC(37)); PACE(); ldv(IC(5));
    *(LAS f32x2*)(L + L_ML + w * 8) = f32x2{mw, wave_sum(e0 + e1)};
  }
  SB();
  STAMP(3);
  f32x4 acc = {0.f, 0.f, 0.f, 0.f};
  u32x4 sel;
#pragma unroll
  for (int k = 0; k < 4; ++k) sel[k] = ((c & 7) >> 1) == k ? ((c & 1) ? 0xffff0000u : 0xffffu) : 0u;
  auto pv = [&](auto I) { PIN(V[I]); const unsigned pb = dppu<BCAST + (I & 15)>(pp[I >> 4]); acc = mfma(V[I], sel & pb, acc); PIN(acc); };
  const int ocol = 64 * (h >> 2) + 16 * r + 4 * (h & 3) + (c & 3);
  unsigned b2v, xo;
  int lw2 = (w >> 1) * 1024 + lane * 16 + (w & 1) * 8;
  u32x4 blast;
  sfor<29>([&](auto I) {
    constexpr int i = I + 6;
    hook(IC(i + 32));
    if constexpr (i < 32) { PACE(); ldv(IC(i)); }
    else PACEN(35 - i);
    if constexpr (i == 24) { ald16(b2v, mk_rsrc(b2, D * 2), w == 0 ? ocol * 2 : OOB, 0); ald16(xo, mk_rsrc(x, NB * D * 2), w == 0 ? ocol * 2 : OOB, b * (D * 2)); PIN(lw2); }
    if constexpr (OTOUCH && i == 8) { __builtin_amdgcn_raw_buffer_store_b16((unsigned short)0, mk_rsrc(out, NB * D * 2), (w == 0 && c < 4) ? (b * D + ocol) * 2 : OOB, 0, 0); SB(); }
    if constexpr (i == 6) { pv(IC(0)); pv(IC(1)); }
    if constexpr (i + 32 == CHKS) {
      BARRIER();
      const f32x2 ml = *(const LAS f32x2*)(L + L_ML + (lane & 7) * 8);
      float M = ml[0];
      M = fmaxf(M, dppf<XOR1>(M)); M = fmaxf(M, dppf<XOR2>(M)); M = fmaxf(M, dppf<HMIR>(M));
      float Ls = ml[1] * __builtin_amdgcn_exp2f((ml[0] - M) * 1.4426950408889634f);
      Ls += dppf<XOR1>(Ls); Ls += dppf<XOR2>(Ls); Ls += dppf<HMIR>(Ls);
      const float scale = __builtin_amdgcn_exp2f((mw - M) * 1.4426950408889634f) / Ls;
      acc *= scale;
      pp[1] = cvtpk(e1 * scale, e1 * scale);
    }
    pv(IC(i - 4));
    SB();
  });
  blast = sel & dppu<BCAST + 15>(pp[1]);
  PIN(blast);
  PACEN(0);
  STAMP(4); TSTAMP(1);
  PIN(V[31]);
  acc = mfma(V[31], blast, acc);
  *(LAS f32x2*)(L + L_RED2 + lw2) = (c & 8) ? f32x2{acc[2], acc[3]} : f32x2{acc[0], acc[1]};
  BARRIER();
  PIN(b2v); PIN(xo);
  if (w == 0) {
    const rsrc_t rws = mk_rsrc(ws, WS_BYTES);
    int vin = blk * IN_BYTES + lane * 16;
    if (SENTP) {
      u32x4 sent4 = {SENT, SENT, SENT, SENT};
      PIN(sent4);
      __builtin_amdgcn_raw_buffer_store_b128(sent4, rws, vin, IN_P2, AUX_SC1);
      __builtin_amdgcn_raw_buffer_store_b128(sent4, rws, vin, IN_P2 + 1024, AUX_SC1);
      asm volatile("s_nop 1" :: "v"(sent4) : "memory");
    }
    int vhs = HST == 0 ? (b * 32 + c) * IN_BYTES + IN_H + h * 32 + r * 8 : (b * 32 + (lane >> 1)) * IN_BYTES + IN_H + h * 32 + (lane & 1) * 16;
    int vout = c < 4 ? (b * D + ocol) * 2 : OOB;
    b2v <<= 16; xo <<= 16;
    PIN(vin); PIN(vhs); PIN(vout); PIN(b2v); PIN(xo);
    if (DLY0) __builtin_amdgcn_s_sleep(DLY0);
    PACEN(0);
    const float hf = asf(xo) + rbf(gather16(rws, vin + IN_P1));
    const unsigned hq = min(cvtpk(hf, dppf<XOR1>(hf)), SENT - 1u);
    unsigned hb[4];
    rows_bcast(dppu<0x88>(hq), hb);
    __builtin_amdgcn_raw_buffer_store_b128(u32x4{hb[0], hb[1], hb[2], hb[3]}, rws, vhs, 0, AUX_SC1);
    SB();
    STAMP(7); TSTAMP(5);
    BARRIER();
    float hres = asf(hq << 16);
    PIN(hres);
    if (DLY1) __builtin_amdgcn_s_sleep(DLY1);
    u32x4 hr;
    for (unsigned spins = 0; spins < SPIN_CAP; ++spins) {
      asm volatile("" ::: "memory");
      hr = __builtin_amdgcn_raw_buffer_load_b128(rws, vin, IN_H, AUX_SC1);
      if (__builtin_amdgcn_ballot_w64(max4(hr) == SENT) == 0) break;
      if ((spins & 63u) == 63u) {
        const int vr = (b * 32 + (lane >> 2)) * IN_BYTES + IN_P1 + h * 64 + (lane & 3) * 16;
        __builtin_amdgcn_raw_buffer_store_b128(*(const LAS u32x4*)(L + L_Y + lane * 16), rws, vr, 0, AUX_SC1);
        __builtin_amdgcn_raw_buffer_store_b128(*(const LAS u32x4*)(L + L_Y + 1024 + lane * 16), rws, vr, 16 * IN_BYTES, AUX_SC1);
      }
    }
    TSTAMP(7);
    {
      const float s1 = dot2(hr[3], 0x3f803f80u, dot2(hr[2], 0x3f803f80u, dot2(hr[1], 0x3f803f80u, dot2(hr[0], 0x3f803f80u, 0.f))));
      const float s2 = dot2(hr[3], hr[3], dot2(hr[2], hr[2], dot2(hr[1], hr[1], dot2(hr[0], hr[0], 0.f))));
      float t = tsum32(s1, s2);
      t = row_sum(tsum16(t, t));
      const float mean = asf(__builtin_amdgcn_readlane(fbits(t), 0)) * (1.f / D);
      const float var = fmaxf(fmaf(-mean, mean, asf(__builtin_amdgcn_readlane(fbits(t), 32)) * (1.f / D)), 0.f);
      const float den = rbf(__builtin_amdgcn_sqrtf(rbf(rbf(var) + 1e-5f)));
      *(LAS f32x2*)(L + L_ST) = f32x2{rbf(mean), __builtin_amdgcn_rcpf(den)};
    }
    if (TAILST == 1) TSTAMP(9);
    BARRIER();
    if (DLY2) __builtin_amdgcn_s_sleep(DLY2);
    const float res = hres + rbf(rbf(gather16(rws, vin + IN_P2)) + asf(b2v));
    __builtin_amdgcn_raw_buffer_store_b16(__builtin_bit_cast(unsigned short, (__bf16)res), mk_rsrc(out, NB * D * 2), vout, 0, 0);
    { SB(); const unsigned long long xt1 = __builtin_amdgcn_s_memrealtime(); const unsigned long long tgt = xt1 + 9 * (xt1 - xt0); while (__builtin_amdgcn_s_memrealtime() < tgt) {} }
  } else if (w >= 4) {
    const int u = w - 4, q4 = (lane >> 2) & 3;
    const rsrc_t rws = mk_rsrc(ws, WS_BYTES);
    __builtin_amdgcn_s_sleep(3);
    u32x8 W1[6];
    LAS unsigned char* p1 = L + L_W + u * 32768 + (lane & 3) * 8 + (4 * (r >> 1) + 32 * (r & 1) + q4) * 128;
    sfor<6>([&](auto J) { W1[J] = tr_read8<16 * 128, 8 * 128>(p1 + (J & 1) * 16384 + (J >> 1) * 32); });
    sfor<6>([&](auto J) { PIN(W1[J]); });
    __builtin_amdgcn_s_sleep(6);
    int vhl = blk * IN_BYTES + IN_H + u * 256 + r * 32 + (lane & 1) * 16, lwr = lane * 16 + u * 4;
    int idx = (lane & 1) ? 0xEEEEEEEE : 0x44444444;
    const f32x4 zero4 = {0.f, 0.f, 0.f, 0.f};
    f32x4 e[3];
    auto project = [&](u32x4 h0, u32x4 h1, int dst) {
      sfor<3>([&](auto T) { e[T] = smf(h0, W1[2 * T], e[T], idx); SB(); e[T] = smf(h1, W1[2 * T + 1], e[T], idx); SB(); });
      float a0 = e[0][0] + e[0][1], a1 = e[1][0] + e[1][1];
      a0 = r == 0 ? a0 : a1;
      PIN(a0);
      const float a2 = e[2][0] + e[2][1];
      KEEP(e[0]); KEEP(e[1]); KEEP(e[2]);
      *(LAS float*)(L + dst + lwr) = r >= 2 ? a2 : a0;
    };
    const u32x4 ones = {0x3f803f80u, 0x3f803f80u, 0x3f803f80u, 0x3f803f80u};
    sfor<3>([&](auto T) { e[T] = zero4; });
    project(ones, ones, L_EX);
    sfor<3>([&](auto T) { e[T] = zero4; PIN(e[T]); });
    PIN(vhl); PIN(lwr); PIN(idx);
    STAMP(6);
    BARRIER();
    if (DLY1) __builtin_amdgcn_s_sleep(DLY1);
    u32x4 h0, h1;
    for (unsigned spins = 0; spins < SPIN_CAP; ++spins) {
      asm volatile("" ::: "memory");
      h0 = __builtin_amdgcn_raw_buffer_load_b128(rws, vhl, 0, AUX_SC1);
      h1 = __builtin_amdgcn_raw_buffer_load_b128(rws, vhl, 128, AUX_SC1);
      if (__builtin_amdgcn_ballot_w64(max(max4(h0), max4(h1)) == SENT) == 0) break;
    }
    SB();
    STAMP(8); TSTAMP(6);
    project(h0, h1, L_EX2);
    if (TAILST == 1) TSTAMP(8);
    BARRIER();
  }
  }
}

extern "C" void kernel_launch(void* const* d_in, const int* in_sizes, int n_in, void* d_out, int out_size, void* d_ws,
                              size_t ws_size, hipStream_t stream) {
  fused_block<<<NB * NH, 768, 0, stream>>>((const unsigned char*)d_in[3], (const unsigned char*)d_in[1], (const unsigned char*)d_in[4],
                                      (const unsigned char*)d_in[2], (const unsigned short*)d_in[0], (const unsigned char*)d_in[6],
                                      (const unsigned char*)d_in[7], (const unsigned char*)d_in[8], (const unsigned short*)d_in[9],
                                      (unsigned short*)d_out, (unsigned char*)d_ws);
}
```

```cpp
#define PEND 1830
#define TOFF 215
#include <hip/hip_runtime.h>
#include <hip/hip_bf16.h>
#include <stdint.h>

#define STAMPS 0
#define PFCODE 24
#define WN 3
#define BWN 3
#define QB0 4
#define QRDY 9
#define QSTEP 28
#define OB0 14
#define MB0 35
#define QBS 8
#define OBS 12
#define MBS 12
#define NG 8
#define OWD 5
#define INB 8192
#define SENTH 0
#define SENTP 1
#define CHKS 61
#define PTAU 24
#define PBUR 90
#define PBUM 110
#define DITH 32
#define PSH 1
#define DITG 0
#define TSTEP 14
#define DUMMY 0
#define DUMW 8
#define MSTEP 6
#define MWAVE 3
#define NREP 32
#define OTOUCH 1
#define COLD 0
#define TFRAC 4
#define TCOM 1
#define SKEW 0
#define PFROM 10
#define PTO 60
#define PEV 2
#define SENTE 1
#define LOSE -1
#define EARLYST 0
#define TOUCH 0
#define SENTW 0
#define TAILST 0
#define HST 1
#define EXPM 0
#define DLY0 20
#define DLY1 11
#define DLY2 18

typedef unsigned u32x4 __attribute__((ext_vector_type(4)));
typedef unsigned u32x2 __attribute__((ext_vector_type(2)));
typedef float f32x2 __attribute__((ext_vector_type(2)));
typedef float f32x4 __attribute__((ext_vector_type(4)));
typedef __bf16 bf16x2 __attribute__((ext_vector_type(2)));
typedef short s16x4 __attribute__((ext_vector_type(4)));
typedef unsigned u32x8 __attribute__((ext_vector_type(8)));
typedef __bf16 bf16x8 __attribute__((ext_vector_type(8)));
typedef __bf16 bf16x16 __attribute__((ext_vector_type(16)));
typedef __amdgpu_buffer_rsrc_t rsrc_t;
#define LAS __attribute__((address_space(3)))
#define DI __device__ __forceinline__

constexpr int NB = 8, D = 512, NH = 32, SK = 4096, DH = 128, WIN = 1024, F = 1376;
constexpr int FS = 48;
constexpr int AUX_NT = 2, AUX_SC1 = 16;
constexpr int OOB = 0x7ffffff0;
constexpr unsigned SENT = 0xffffffffu;
constexpr unsigned SPIN_CAP = 1u << 17;
constexpr int IN_P1 = 0;
constexpr int IN_P2 = 3072;
constexpr int IN_H = 2048;
constexpr int IN_BYTES = INB;
constexpr int WS_T = 256 * IN_BYTES;
constexpr int WS_BYTES = WS_T + 128;
constexpr int WS_STAMP = 4 * 1024 * 1024;
constexpr int L_RED = 0;
constexpr int L_RED2 = L_RED + 2048;
constexpr int L_EX = L_RED2 + 4096;
constexpr int L_EX2 = L_EX + 1024;
constexpr int L_ML = L_EX2 + 1024;
constexpr int L_ST = L_ML + 64;
constexpr int L_T = L_ST + 16;
constexpr int L_Y = 9216;
constexpr int L_DUMP = L_Y + 2048;
constexpr int L_W = L_DUMP + 1024;
constexpr int L_BYTES = L_W + 128 * 1024;

DI float dot2(unsigned a, unsigned b, float acc) {
  return __builtin_amdgcn_fdot2_f32_bf16(__builtin_bit_cast(bf16x2, a), __builtin_bit_cast(bf16x2, b), acc, false);
}
DI unsigned fbits(float f) { return __builtin_bit_cast(unsigned, f); }
DI float asf(unsigned u) { return __builtin_bit_cast(float, u); }
DI unsigned cvtpk(float lo, float hi) {
  bf16x2 r = {(__bf16)lo, (__bf16)hi};
  return __builtin_bit_cast(unsigned, r);
}
DI float rbf(float f) { return asf((unsigned)__builtin_bit_cast(unsigned short, (__bf16)f) << 16); }
template <int CTRL> DI unsigned dppu(unsigned x) { return __builtin_amdgcn_mov_dpp(x, CTRL, 0xf, 0xf, true); }
template <int CTRL> DI float dppf(float x) { return asf(dppu<CTRL>(fbits(x))); }
constexpr int XOR1 = 0xB1, XOR2 = 0x4E, HMIR = 0x141, ROR4 = 0x124, ROR8 = 0x128, BCAST = 0x150;
DI float tsum32(float a, float b) {
  auto s = __builtin_amdgcn_permlane32_swap(fbits(a), fbits(b), false, false);
  return asf(s[0]) + asf(s[1]);
}
DI float tsum16(float a, float b) {
  auto s = __builtin_amdgcn_permlane16_swap(fbits(a), fbits(b), false, false);
  return asf(s[0]) + asf(s[1]);
}
DI float row_sum(float x) { x += dppf<XOR1>(x); x += dppf<XOR2>(x); x += dppf<ROR4>(x); x += dppf<ROR8>(x); return x; }
DI float row_max(float x) { x = fmaxf(x, dppf<XOR1>(x)); x = fmaxf(x, dppf<XOR2>(x)); x = fmaxf(x, dppf<ROR4>(x)); x = fmaxf(x, dppf<ROR8>(x)); return x; }
DI float wave_sum(float x) { x = row_sum(x); x = tsum16(x, x); return tsum32(x, x); }
DI float wave_max(float x) {
  x = row_max(x);
  auto s = __builtin_amdgcn_permlane16_swap(fbits(x), fbits(x), false, false);
  x = fmaxf(asf(s[0]), asf(s[1]));
  auto t = __builtin_amdgcn_permlane32_swap(fbits(x), fbits(x), false, false);
  return fmaxf(asf(t[0]), asf(t[1]));
}
DI void rows_bcast(unsigned x, unsigned (&o)[4]) {
  auto s = __builtin_amdgcn_permlane16_swap(x, x, false, false);
  auto a = __builtin_amdgcn_permlane32_swap(s[0], s[0], false, false);
  auto b = __builtin_amdgcn_permlane32_swap(s[1], s[1], false, false);
  o[0] = a[0]; o[2] = a[1]; o[1] = b[0]; o[3] = b[1];
}
DI f32x4 smf(u32x4 a, u32x8 b, f32x4 c, int idx) {
  return __builtin_amdgcn_smfmac_f32_16x16x64_bf16(__builtin_bit_cast(bf16x8, a), __builtin_bit_cast(bf16x16, b), c, idx, 0, 0);
}
DI f32x4 mfma(u32x4 a, u32x4 b, f32x4 c) {
  return __builtin_amdgcn_mfma_f32_16x16x32_bf16(__builtin_bit_cast(bf16x8, a), __builtin_bit_cast(bf16x8, b), c, 0, 0, 0);
}
constexpr int SHL = 0x100;
template <class T> DI void pin_(T& x) { asm volatile("" : "+v"(x)); }
#define PIN(x) pin_(x)
template <class T> DI void keep_(const T& x) { asm volatile("" :: "v"(x)); }
#define KEEP(x) keep_(x)
template <class T> DI void pins_(T& x) { asm volatile("" : "+s"(x)); }
#define PINS(x) pins_(x)
#define SB() __builtin_amdgcn_sched_barrier(0)
#define PACEN(n) do { asm volatile("s_waitcnt vmcnt(%0)" :: "i"(n) : "memory"); SB(); } while (0)
#define PACE() PACEN(WN)
#define BARRIER() do { asm volatile("s_waitcnt lgkmcnt(0)\n\ts_barrier" ::: "memory"); SB(); } while (0)
DI rsrc_t mk_rsrc(const void* p, int bytes) { return __builtin_amdgcn_make_buffer_rsrc((void*)p, 0, bytes, 0x00020000); }

template <int G, int N, int CNT = N> DI void burst(int grp, rsrc_t r, int voff, int stride, int lds, int j0) {
  int keep, j, cnt, t;
  asm volatile(
      "s_cmp_lg_u32 %4, %10\n\t"
      "s_cbranch_scc1 BE%=\n\t"
      "s_mov_b32 %0, m0\n\t"
      "s_mov_b32 %1, %9\n\t"
      "s_mov_b32 %2, %12\n"
      "BL%=:\n\t"
      "s_lshl_b32 %3, %1, 10\n\t"
      "s_add_u32 m0, %3, %8\n\t"
      "s_mul_i32 %3, %1, %7\n\t"
      "s_add_u32 %1, %1, 1\n\t"
      "s_waitcnt vmcnt(%13)\n\t"
      "buffer_load_dwordx4 %6, %5, %3 offen lds\n\t"
      "s_cmp_eq_u32 %1, %11\n\t"
      "s_cselect_b32 %1, 0, %1\n\t"
      "s_sub_u32 %2, %2, 1\n\t"
      "s_cmp_lg_u32 %2, 0\n\t"
      "s_cbranch_scc1 BL%=\n\t"
      "s_mov_b32 m0, %0\n"
      "BE%=:"
      : "=&s"(keep), "=&s"(j), "=&s"(cnt), "=&s"(t)
      : "s"(grp), "s"(r), "v"(voff), "s"(stride), "s"(lds), "s"(j0), "i"(G), "i"(N), "i"(CNT), "i"(BWN)
      : "memory", "scc");
  SB();
}
DI void ald128nt(u32x4& d, rsrc_t r, int voff, int soff) { asm volatile("buffer_load_dwordx4 %0, %1, %2, %3 offen nt" : "=v"(d) : "v"(voff), "s"(r), "s"(soff) : "memory"); SB(); }
DI void ald128(u32x4& d, rsrc_t r, int voff, int soff) { asm volatile("buffer_load_dwordx4 %0, %1, %2, %3 offen" : "=v"(d) : "v"(voff), "s"(r), "s"(soff) : "memory"); SB(); }
DI void ald64sc(u32x2& d, rsrc_t r, int voff, int soff) { asm volatile("buffer_load_dwordx2 %0, %1, %2, %3 offen sc1" : "=v"(d) : "v"(voff), "s"(r), "s"(soff) : "memory"); SB(); }
DI void ald32sc(unsigned& d, rsrc_t r, int voff) { asm volatile("buffer_load_dword %0, %1, %2, 0 offen sc1" : "=v"(d) : "v"(voff), "s"(r) : "memory"); SB(); }
DI void ald32(unsigned& d, rsrc_t r, int voff) { asm volatile("buffer_load_dword %0, %1, %2, 0 offen" : "=v"(d) : "v"(voff), "s"(r) : "memory"); SB(); }
DI void ald16(unsigned& d, rsrc_t r, int voff, int soff) { asm volatile("buffer_load_ushort %0, %1, %2, %3 offen" : "=v"(d) : "v"(voff), "s"(r), "s"(soff) : "memory"); SB(); }
template <bool NT = false> DI void dma1(rsrc_t r, int voff, int soff, int lds) {
  int keep;
  if constexpr (NT)
    asm volatile("s_mov_b32 %0, m0\n\ts_mov_b32 m0, %4\n\ts_nop 0\n\tbuffer_load_dwordx4 %1, %2, %3 offen nt lds\n\ts_mov_b32 m0, %0"
                 : "=&s"(keep) : "v"(voff), "s"(r), "s"(soff), "s"(lds) : "memory");
  else
    asm volatile("s_mov_b32 %0, m0\n\ts_mov_b32 m0, %4\n\ts_nop 0\n\tbuffer_load_dwordx4 %1, %2, %3 offen lds\n\ts_mov_b32 m0, %0"
                 : "=&s"(keep) : "v"(voff), "s"(r), "s"(soff), "s"(lds) : "memory");
  SB();
}
template <int G> DI void cond_barrier(int grp) {
  asm volatile("s_cmp_lg_u32 %0, %1\n\ts_cbranch_scc1 CB%=\n\ts_barrier\nCB%=:" :: "s"(grp), "i"(G) : "memory", "scc");
  SB();
}
DI float gather16(rsrc_t r, int voff);
DI u32x2 tr_read(LAS unsigned char* p) {
  return __builtin_bit_cast(u32x2, __builtin_amdgcn_ds_read_tr16_b64_v4i16((LAS s16x4*)p));
}
template <int M1, int M2> DI u32x8 tr_read8(LAS unsigned char* p) {
  const u32x2 a = tr_read(p), b = tr_read(p + M1), c = tr_read(p + M2), d = tr_read(p + M1 + M2);
  return u32x8{a[0], a[1], b[0], b[1], c[0], c[1], d[0], d[1]};
}
DI f32x2 fold_rows8(const float (&acc)[8]) {
  const float t0 = tsum32(acc[0], acc[4]), t1 = tsum32(acc[1], acc[5]), t2 = tsum32(acc[2], acc[6]), t3 = tsum32(acc[3], acc[7]);
  return f32x2{tsum16(t0, t2), tsum16(t1, t3)};
}
DI float fold_rows4(float a0, float a1, float a2, float a3) { return tsum16(tsum32(a0, a2), tsum32(a1, a3)); }
DI unsigned max4(u32x4 v) { return max(max(v[0], v[1]), max(v[2], v[3])); }

DI float gather16(rsrc_t r, int voff) {
  u32x4 A, Bv;
  for (unsigned spins = 0; spins < SPIN_CAP; ++spins) {
    asm volatile("" ::: "memory");
    A = __builtin_amdgcn_raw_buffer_load_b128(r, voff, 0, AUX_SC1);
    Bv = __builtin_amdgcn_raw_buffer_load_b128(r, voff, 1024, AUX_SC1);
    if (__builtin_amdgcn_ballot_w64(max(max4(A), max4(Bv)) == SENT) == 0) break;
  }
  const f32x4 s = __builtin_bit_cast(f32x4, A) + __builtin_bit_cast(f32x4, Bv);
  float t = fold_rows4(s[0], s[1], s[2], s[3]);
  t += dppf<ROR4>(t); t += dppf<ROR8>(t);
  return t;
}

template <int I> DI void pv_step(float (&acc)[8], unsigned plo, unsigned phi, u32x4 v) {
  const unsigned lo = dppu<BCAST + I>(plo), hi = dppu<BCAST + I>(phi);
#pragma unroll
  for (int k = 0; k < 4; ++k) { acc[2 * k] = dot2(lo, v[k], acc[2 * k]); acc[2 * k + 1] = dot2(hi, v[k], acc[2 * k + 1]); }
}
DI void pin8(float (&a)[8]) {
  asm volatile("" : "+v"(a[0]), "+v"(a[1]), "+v"(a[2]), "+v"(a[3]), "+v"(a[4]), "+v"(a[5]), "+v"(a[6]), "+v"(a[7]));
}
template <int... Is> struct iseq {};
template <int N, int... Is> struct mkseq : mkseq<N - 1, N - 1, Is...> {};
template <int... Is> struct mkseq<0, Is...> { typedef iseq<Is...> type; };
template <class Fn, int... Is> DI void sfor_(Fn&& f, iseq<Is...>) { (f(std::integral_constant<int, Is>{}), ...); }
template <int N, class Fn> DI void sfor(Fn&& f) { sfor_(f, typename mkseq<N>::type{}); }

DI float transpose_reduce16(float (&x)[16], int c) {
#define TR_STEP(HALF, CTRL, BIT) _Pragma("unroll") for (int h_ = 0; h_ < (HALF); ++h_) { \
    const float lo_ = x[h_], hi_ = x[h_ + (HALF)]; const float t1_ = lo_ + dppf<CTRL>(lo_), t2_ = hi_ + dppf<CTRL>(hi_); \
    x[h_] = (c & (BIT)) ? t2_ : t1_; }
  TR_STEP(8, ROR8, 8) TR_STEP(4, HMIR, 4) TR_STEP(2, XOR2, 2) TR_STEP(1, XOR1, 1)
#undef TR_STEP
  return x[0];
}

#define STAMP(i) do {} while (0)
#define ESTAMP(i) do {} while (0)
#define TSTAMP(i) do {} while (0)

extern "C" __global__ void __launch_bounds__(768, 3)
fused_block(const unsigned char* __restrict__ wq, const unsigned char* __restrict__ kc, const unsigned char* __restrict__ wo,
            const unsigned char* __restrict__ vc, const unsigned short* __restrict__ x, const unsigned char* __restrict__ w1,
            const unsigned char* __restrict__ b1, const unsigned char* __restrict__ w2, const unsigned short* __restrict__ b2,
            unsigned short* __restrict__ out, unsigned char* ws) {
  __shared__ __attribute__((aligned(16))) unsigned char smem[L_BYTES];
  LAS unsigned char* L = (LAS unsigned char*)smem;
  if (SKEW && (blockIdx.x & 7) == 5) __builtin_amdgcn_s_sleep(SKEW);
  const unsigned long long xt0 = __builtin_amdgcn_s_memrealtime();
  const int blk = blockIdx.x, b = blk >> 5, h = blk & 31;
  const int tid = threadIdx.x, lane = tid & 63, r = lane >> 4, c = lane & 15;
  const int w = __builtin_amdgcn_readfirstlane(tid >> 6);
  const int slice = (h & 7) * 4 + (h >> 3), col0 = slice * FS;
#define IC(n) std::integral_constant<int, (n)>{}
  if (LOSE >= 0 && (blk == LOSE || blk == LOSE + 5)) for (int i = 0; i < (blk == LOSE ? 12 : 30); ++i) __builtin_amdgcn_s_sleep(127);
  {
    int w2 = w;
    PINS(w2);
    if (w2 >= 9) {
      if (COLD) { asm volatile("s_waitcnt lgkmcnt(0)"); __builtin_amdgcn_s_sleep(COLD); }
      const rsrc_t r0 = mk_rsrc(ws, WS_BYTES);
      u32x4 s4 = {SENT, SENT, SENT, SENT};
      PIN(s4);
      if (!(EXPM & 1)) __builtin_amdgcn_raw_buffer_store_b128(s4, r0, lane * 16, blk * IN_BYTES + (w2 - 9) * 1024, AUX_SC1);
      SB(); KEEP(s4);
    }
    SB();
  }
  if (w >= 8) {
  const int u = w - 8;
  const rsrc_t rws = mk_rsrc(ws, WS_BYTES);
  const u32x4 xrow = *(const u32x4*)(x + b * D + lane * 8);
  const float xe0 = asf((unsigned)x[b * D + 128 * u + 4 * c + r] << 16), xe1 = asf((unsigned)x[b * D + 128 * u + 64 + 4 * c + r] << 16);
  float b1v = asf((unsigned)__builtin_amdgcn_raw_buffer_load_b16(mk_rsrc(b1, F * 2), lane < FS ? (col0 + lane) * 2 : OOB, 0, 0) << 16);
  if (PFCODE && (blk >> 3) == PFCODE && u >= 1) {
    unsigned long long pc; int o0, o1;
    asm volatile("s_getpc_b64 %0\nKPC0:\n\ts_mov_b32 %1, KPC0-KPCA\n\ts_mov_b32 %2, KPCB-KPCA" : "=s"(pc), "=s"(o0), "=s"(o1));
    const rsrc_t rc = mk_rsrc((const void*)(pc - (long long)o0), o1);
    unsigned p0 = __builtin_amdgcn_raw_buffer_load_b32(rc, lane * 128, (u - 1) * 8192, 0);
    PIN(p0);
  }
  SB();
  {
    float xf[8];
#pragma unroll
    for (int k = 0; k < 4; ++k) { xf[2 * k] = asf(xrow[k] << 16); xf[2 * k + 1] = asf(xrow[k] & 0xffff0000u); }
    const float mean = wave_sum(((xf[0] + xf[1]) + (xf[2] + xf[3])) + ((xf[4] + xf[5]) + (xf[6] + xf[7]))) * (1.f / D);
    float vs = 0.f;
#pragma unroll
    for (int k = 0; k < 8; ++k) { const float d = xf[k] - mean; vs = fmaf(d, d, vs); }
    const float var = wave_sum(vs) * (1.f / D);
    const float den = rbf(sqrtf(rbf(rbf(var) + 1e-5f))), mb = rbf(mean);
    const unsigned xp = cvtpk(rbf(xe0 - mb) / den, rbf(xe1 - mb) / den);
    unsigned xlo[2] = {xp & 0xffffu, xp >> 16}, xhi[2] = {xp << 16, xp & 0xffff0000u};
    PIN(xlo[0]); PIN(xlo[1]); PIN(xhi[0]); PIN(xhi[1]); PIN(b1v);
    PACEN(0);
    ESTAMP(5);
    BARRIER();
    ESTAMP(6);
    float acc[8] = {0.f, 0.f, 0.f, 0.f, 0.f, 0.f, 0.f, 0.f};
    sfor<32>([&](auto I) { pv_step<I & 15>(acc, xlo[I >> 4], xhi[I >> 4], *(const LAS u32x4*)(L + L_W + u * 32768 + I * 1024 + lane * 16)); });
    *(LAS f32x2*)(L + L_RED + (u >> 1) * 1024 + lane * 16 + (u & 1) * 8) = fold_rows8(acc);
  }
  STAMP(11);
  BARRIER();
  BARRIER();
  u32x8 WO[16];
  const int q4 = (lane >> 2) & 3;
  LAS unsigned char* lw = L + L_W + u * 32768 + (lane & 3) * 8;
  {
    LAS unsigned char* p = lw + (32 * (r >> 1) + 4 * (r & 1) + (q4 >> 1) + 8 * (q4 & 1)) * 128;
    sfor<16>([&](auto J) { WO[J] = tr_read8<2 * 128, 64 * 128>(p + (J >> 3) * 16384 + ((J >> 1) & 3) * 32 + (J & 1) * 16 * 128); });
    sfor<16>([&](auto J) { PIN(WO[J]); });
  }
  BARRIER();
  BARRIER();
  int vp = lane < 16 ? (b * 32 + 8 * u + (lane >> 2)) * IN_BYTES + (lane & 3) * 16 : OOB;
  int lrd = lane * 16;
  int idx = (lane & 1) ? 0xEEEEEEEE : 0x44444444;
  PIN(vp); PIN(lrd); PIN(idx);
  const f32x4 zero4 = {0.f, 0.f, 0.f, 0.f};
  f32x4 d[8];
  d[0] = zero4; d[1] = zero4; PIN(d[0]); PIN(d[1]);
  int so1 = IN_P1 + h * 64, so2 = IN_P1 + h * 64 + 4 * IN_BYTES;
  PINS(so1); PINS(so2);
  BARRIER();
  TSTAMP(2);
  {
    f32x4 s4 = *(const LAS f32x4*)(L + L_RED2 + lrd) + *(const LAS f32x4*)(L + L_RED2 + 1024 + lrd);
    s4 += *(const LAS f32x4*)(L + L_RED2 + 2048 + lrd) + *(const LAS f32x4*)(L + L_RED2 + 3072 + lrd);
    const unsigned op = cvtpk(s4[0] + s4[2], s4[1] + s4[3]);
    const u32x4 a0 = {op, dppu<SHL + 2>(op), dppu<SHL + 4>(op), dppu<SHL + 6>(op)};
    const u32x4 a1 = {dppu<SHL + 8>(op), dppu<SHL + 10>(op), dppu<SHL + 12>(op), dppu<SHL + 14>(op)};
    float y[8];
    auto fin = [&](auto T) { y[T] = d[T][0] + d[T][1]; PIN(y[T]); KEEP(d[T]); };
    sfor<8>([&](auto T) {
      d[T] = smf(a0, WO[2 * T], d[T], idx);
      if constexpr (T > 0) SB();
      if constexpr (T + 2 < 8 && T > 0) { d[T + 2] = zero4; PIN(d[T + 2]); }
      SB();
      d[T] = smf(a1, WO[2 * T + 1], d[T], idx);
      SB();
      if constexpr (T == 0) { d[2] = zero4; PIN(d[2]); }
      if constexpr (T >= 2) fin(IC(T - 2));
      if constexpr (T == 6)
        __builtin_amdgcn_raw_buffer_store_b128(u32x4{fbits(y[0]), fbits(y[1]), fbits(y[2]), fbits(y[3])}, rws, (EXPM & 8) ? OOB : vp, so1, AUX_SC1);
      SB();
    });
    fin(IC(6)); fin(IC(7));
    __builtin_amdgcn_raw_buffer_store_b128(u32x4{fbits(y[4]), fbits(y[5]), fbits(y[6]), fbits(y[7])}, rws, (EXPM & 8) ? OOB : vp, so2, AUX_SC1);
    SB();
    if (lrd < 256) {
      *(LAS f32x4*)(L + L_Y + u * 512 + lrd) = f32x4{y[0], y[1], y[2], y[3]};
      *(LAS f32x4*)(L + L_Y + u * 512 + 256 + lrd) = f32x4{y[4], y[5], y[6], y[7]};
    }
  }
  SB();
  STAMP(5); TSTAMP(3);
  u32x8 W2[8];
  {
    const int ln = __builtin_amdgcn_mbcnt_hi(~0u, __builtin_amdgcn_mbcnt_lo(~0u, 0u)), rr = ln >> 4, qq = (ln >> 2) & 3;
    LAS unsigned char* p2 = L + L_W + u * 32768 + (ln & 3) * 8 + 8192 + (16 * (rr >> 1) + 8 * (rr & 1) + (qq >> 1) + 2 * (qq & 1)) * 128;
    LAS unsigned char* p3 = p2 + (32 - 16 * (rr >> 1)) * 128;
    sfor<8>([&](auto J) {
      constexpr int o = (J >> 2) * 16384 + (J & 3) * 32;
      const u32x2 t0 = tr_read(p2 + o), t1 = tr_read(p2 + o + 4 * 128), t2 = tr_read(p3 + o), t3 = tr_read(p3 + o + 4 * 128);
      W2[J] = u32x8{t0[0], t0[1], t1[0], t1[1], t2[0], t2[1], t3[0], t3[1]};
    });
    sfor<8>([&](auto J) { PIN(W2[J]); d[J] = zero4; PIN(d[J]); });
  }
  so1 = IN_P2 + slice * 64; so2 = IN_P2 + slice * 64 + 4 * IN_BYTES;
  int lst = L_ST;
  float gk = -2.f * 1.4426950408889634f * 0.7978845608028654f;
  unsigned gmask = lrd < FS * 16 ? ~0u : 0u;
  PINS(so1); PINS(so2); PIN(lst); PIN(gk); PIN(gmask);
  SB();
  STAMP(6);
  BARRIER();
  float c1;
  { const f32x4 t = *(const LAS f32x4*)(L + L_EX + lrd); c1 = (t[0] + t[1]) + (t[2] + t[3]); }
  PIN(c1);
  BARRIER();
  if (TAILST == 2) TSTAMP(8);
  {
    const f32x4 t4 = *(const LAS f32x4*)(L + L_EX2 + lrd);
    const f32x2 ms = *(const LAS f32x2*)(L + lst);
    const float tot = (t4[0] + t4[1]) + (t4[2] + t4[3]);
    const float uu = rbf(rbf(fmaf(-ms[0], c1, tot) * ms[1]) + b1v);
    const float z = uu * fmaf(uu * uu, -2.f * 1.4426950408889634f * 0.044715f * 0.7978845608028654f, gk);
    const float g = asf(fbits(uu) & gmask) * __builtin_amdgcn_rcpf(1.f + __builtin_amdgcn_exp2f(z));
    const unsigned gp = cvtpk(g, dppf<XOR2>(g));
    const u32x4 ag = {gp, dppu<SHL + 4>(gp), dppu<SHL + 8>(gp), dppu<SHL + 12>(gp)};
    if (TAILST == 2) { KEEP(ag); TSTAMP(9); }
    float y[8];
    auto fin = [&](auto T) { y[T] = d[T][0] + d[T][1]; PIN(y[T]); KEEP(d[T]); SB(); };
    sfor<8>([&](auto T) { d[T] = smf(ag, W2[T], d[T], idx); SB(); });
    fin(IC(0)); fin(IC(1)); fin(IC(2)); fin(IC(3));
    __builtin_amdgcn_raw_buffer_store_b128(u32x4{fbits(y[0]), fbits(y[1]), fbits(y[2]), fbits(y[3])}, rws, vp, so1, AUX_SC1);
    SB();
    fin(IC(4)); fin(IC(5)); fin(IC(6)); fin(IC(7));
    KEEP(y[0]); KEEP(y[1]); KEEP(y[2]); KEEP(y[3]);
    __builtin_amdgcn_raw_buffer_store_b128(u32x4{fbits(y[4]), fbits(y[5]), fbits(y[6]), fbits(y[7])}, rws, vp, so2, AUX_SC1);
  }
  asm volatile("KPCB:");
  SB();
  STAMP(9); TSTAMP(10);
  } else {
  const int grp = NG == 8 ? (b + 2 * (h >> 3)) & 7 : (NG == 16 ? (2 * b + 4 * (h >> 4) + ((h >> 3) & 1)) & 15 : 4 * b + (h >> 3));
  const int pq = QB0 + ((grp * QBS) >> 2), po = OB0 + ((grp * OBS) >> 2), pm = MB0 + ((grp * MBS) >> 2);
  const size_t kvoff = ((size_t)blk * SK + (SK - WIN)) * (DH * 2);
  const rsrc_t rk = mk_rsrc(kc + kvoff, WIN * DH * 2), rv = mk_rsrc(vc + kvoff, WIN * DH * 2);
  const int vkv = tid * 16;
  const int lds0 = (int)(unsigned)(size_t)L, ldsw = lds0 + L_W + w * 16384, ldsd = lds0 + L_DUMP;

  u32x4 K[32], V[32];
  auto ldk = [&](auto I) { ald128nt(K[I], rk, vkv, I * 8192); };
  auto ldv = [&](auto I) { ald128nt(V[I], rv, vkv, I * 8192); };
  ldk(IC(0)); ldk(IC(1));
  const unsigned long long tclk = __builtin_amdgcn_s_memrealtime();
  SB();
  asm volatile("KPCA:");
  const rsrc_t rq = mk_rsrc(wq + h * (DH * 2), D * NH * DH * 2 - h * (DH * 2));
  const int vq = (64 * w + r) * (NH * DH * 2) + c * 16;
  dma1(rq, vq, 2 * b * (4 * NH * DH * 2), ldsw + 2 * b * 1024);
  dma1(rq, vq, (2 * b + 1) * (4 * NH * DH * 2), ldsw + (2 * b + 1) * 1024);
  unsigned tch = 0;
  if (TOUCH) {
    const unsigned char* p = w == 0 ? vc + kvoff : w == 1 ? wo + (size_t)h * (DH * D * 2) : w == 2 ? w1 + 256 * F * 2 : w == 3 ? w2 + (size_t)min(col0, F - 1) * (D * 2)
                           : w == 4 ? ws + blk * IN_BYTES + 5120 : w == 5 ? (const unsigned char*)out : w == 6 ? (const unsigned char*)b2 : b1;
    ald32(tch, mk_rsrc(p, 4), lane == 0 ? 0 : OOB);
  }
  const rsrc_t ro = mk_rsrc(wo + (size_t)h * (DH * D * 2), DH * D * 2);
  const int vo = (lane >> 3) * (D * 2) + w * 128 + (lane & 7) * 16;
  const rsrc_t r1 = mk_rsrc(w1, D * F * 2);
  const int v1 = ((lane & 7) < 6 && col0 + (lane & 7) * 8 < F) ? (64 * w + (lane >> 3)) * (F * 2) + col0 * 2 + (lane & 7) * 16 : OOB;
  const int c2 = min(col0, F);
  const rsrc_t r2 = mk_rsrc(w2 + (size_t)c2 * (D * 2), (F - c2) * (D * 2));
  const int b6 = b < 6 ? b : b - 6;
  const int dith = (((w - 4) * DITH) >> 3) + ((((blk >> 3) - 16) * DITG) >> 5);
  unsigned tst = PEND ? (unsigned)tclk + TOFF + dith : 0u;
  auto hook = [&](auto I) {
    constexpr int i = I;
    if constexpr (PEND && i >= PFROM && i <= PTO && i % PEV == 0) {
      PINS(tst);
      const int pend = (i <= pq ? PBUR : 0) + (i <= po ? PBUR : 0) + (i <= pm ? PBUM : 0);
      constexpr int extra = (i <= OB0 - OWD ? 2 : 0) + (i <= MB0 - OWD ? 2 : 0);
      const unsigned tgt = tst + PEND - (67 - i + extra) * PTAU - pend;
      int tmp;
      asm volatile("s_memrealtime vcc\n\ts_waitcnt lgkmcnt(0)\n\ts_sub_i32 %0, %1, vcc_lo\n\ts_ashr_i32 %0, %0, %2\n\ts_cmp_lt_i32 %0, 3\n\ts_cbranch_scc1 PE%=\n\t"
                   "s_add_i32 %0, %0, vcc_lo\n"
                   "PC%=:\n\ts_memrealtime vcc\n\ts_waitcnt lgkmcnt(0)\n\ts_sub_i32 vcc_lo, vcc_lo, %0\n\ts_cmp_lt_i32 vcc_lo, 0\n\ts_cbranch_scc1 PC%=\nPE%=:"
                   : "=&s"(tmp) : "s"(tgt), "i"(PSH) : "scc", "vcc", "memory");
      SB();
    }
    constexpr auto site = [](int i_, int p0, int sp) { for (int g = 0; g < NG; ++g) if (p0 + ((g * sp) >> 2) == i_) return true; return false; };
    if constexpr (site(i - 4, QB0, QBS)) cond_barrier<i - 4>(pq);
    if constexpr (site(i - QRDY, QB0, QBS)) cond_barrier<i - QRDY>(pq);
    if constexpr (site(i - 4, OB0, OBS)) cond_barrier<i - 4>(po);
    if constexpr (site(i, MB0, MBS)) cond_barrier<i>(pm);
    if constexpr (site(i, QB0, QBS)) burst<i, 16, 14>(pq, rq, vq, 4 * NH * DH * 2, ldsw, (2 * b + 2) & 15);
    if constexpr (i == OB0 - OWD) { PACE(); dma1(ro, vo, 2 * b * 8192, ldsd); PACE(); dma1(ro, vo, (2 * b + 1) * 8192, ldsd); }
    if constexpr (site(i, OB0, OBS)) burst<i, 16>(po, ro, vo, 8 * D * 2, ldsw, 2 * b);
    if constexpr (i == MB0 - OWD) { PACE(); dma1(r1, v1, b * (8 * F * 2), ldsd); PACE(); dma1(r2, vo, b * (8 * D * 2), ldsd); }
    if constexpr (site(i, MB0, MBS)) {
      burst<i, 8>(pm, r1, v1, 8 * F * 2, ldsw, b);
      burst<i, 6>(pm, r2, vo, 8 * D * 2, ldsw + 8192, b6);
    }
  };
  static_assert(QRDY > 4 && QB0 + (NG - 1) * QBS / 4 + QRDY < QSTEP && OBS >= QBS && OB0 >= QB0 + QRDY && MB0 > OB0 + 4 &&
                MB0 + (NG - 1) * MBS / 4 > OB0 + (NG - 1) * OBS / 4 + 5 && MB0 + (NG - 1) * MBS / 4 <= CHKS && CHKS >= 52 && CHKS <= 63, "barrier order, staging area reuse");
  unsigned tx;
  constexpr int S0 = 2;
  sfor<QSTEP - S0>([&](auto I) { constexpr int i = I + S0; if constexpr (i == 11) ESTAMP(7); if constexpr (i == 2) ESTAMP(8); if constexpr (i == 3) ESTAMP(9); hook(IC(i));
    PACEN(WN + (TCOM && ((i > TSTEP && i < TSTEP + 4) || (i > MSTEP && i < MSTEP + 4)) ? 1 : 0)); ldk(IC(i));
    if constexpr (TCOM && i == MSTEP) {
      __builtin_amdgcn_raw_buffer_store_b32((unsigned)__builtin_amdgcn_s_memrealtime(), mk_rsrc(ws + WS_T, NREP * 4),
                                            (tid == 64 * MWAVE && (blk & 0xd0) == 0x80) ? (((blk >> 1) & 16) | (blk & 15)) * 4 : OOB, 0, AUX_SC1);
      SB();
    }
    if constexpr (TCOM && i == TSTEP) ald32sc(tx, mk_rsrc(ws + WS_T, NREP * 4), (lane & 31) * 4);
    if constexpr (TCOM && i == TSTEP + 4) {
      PIN(tx);
      const int dt = (int)(tx - (unsigned)tclk) - TOFF;
      const bool ok = dt > -400 && dt < 400;
      float sm = ok ? (lane < 32 ? (float)dt : 1.f) : 0.f;
      sm = row_sum(tsum16(sm, sm));
      const float cnt = asf(__builtin_amdgcn_readlane(fbits(sm), 32));
      int adj = (int)(sm * __builtin_amdgcn_rcpf(fmaxf(cnt, 1.f)));
      PIN(adj);
      tst += __builtin_amdgcn_readfirstlane(adj);
      SB();
    }
  });
  PIN(tch);
  STAMP(1);
  unsigned qp[4];
  {
    const f32x4 s4 = *(const LAS f32x4*)(L + L_RED + lane * 16) + *(const LAS f32x4*)(L + L_RED + 1024 + lane * 16);
    rows_bcast(cvtpk(s4[0] + s4[2], s4[1] + s4[3]), qp);
  }
  SB();
  STAMP(2);
  float sc[2], xs[16];
  auto qk = [&](u32x4& kv) { PIN(kv); return dot2(kv[3], qp[3], dot2(kv[2], qp[2], dot2(kv[1], qp[1], dot2(kv[0], qp[0], 0.f)))); };
  static_assert(QSTEP <= 28, "");
  sfor<36 - QSTEP>([&](auto I) {
    constexpr int i = I + QSTEP;
    hook(IC(i));
    PACE();
    if constexpr (i < 32) ldk(IC(i)); else ldv(IC(i - 32));
    if constexpr (I < 3) sfor<(I < 2 ? 6 : 4)>([&](auto J) { xs[6 * I + J] = qk(K[6 * I + J]); PIN(xs[6 * I + J]); });
    if constexpr (I == 3) { sc[0] = transpose_reduce16(xs, c); PIN(sc[0]); }
    if constexpr (I >= 4) {
      constexpr int lo = (I == 4) ? 16 : (16 + 6 * (I - 4) < i - 4 ? 16 + 6 * (I - 4) : i - 4);
      constexpr int hi = (16 + 6 * (I - 3) < i - 3) ? 16 + 6 * (I - 3) : i - 3;
      sfor<hi - lo>([&](auto J) { xs[lo - 16 + J] = qk(K[lo + J]); PIN(xs[lo - 16 + J]); });
    }
    SB();
  });
  sc[1] = transpose_reduce16(xs, c);
  SB();
  hook(IC(36)); PACE(); ldv(IC(4));
  unsigned pp[2];
  float mw, e1;
  {
    const float s0 = rbf(rbf(sc[0]) * 0.08838834764831845f), s1 = rbf(rbf(sc[1]) * 0.08838834764831845f);
    mw = wave_max(fmaxf(s0, s1));
    const float e0 = __builtin_amdgcn_exp2f((s0 - mw) * 1.4426950408889634f);
    e1 = __builtin_amdgcn_exp2f((s1 - mw) * 1.4426950408889634f);
    pp[0] = cvtpk(e0, e0); pp[1] = cvtpk(e1, e1);
    SB();
    hook(IC(37)); PACE(); ldv(IC(5));
    *(LAS f32x2*)(L + L_ML + w * 8) = f32x2{mw, wave_sum(e0 + e1)};
  }
  SB();
  STAMP(3);
  f32x4 acc = {0.f, 0.f, 0.f, 0.f};
  u32x4 sel;
#pragma unroll
  for (int k = 0; k < 4; ++k) sel[k] = ((c & 7) >> 1) == k ? ((c & 1) ? 0xffff0000u : 0xffffu) : 0u;
  auto pv = [&](auto I) { PIN(V[I]); const unsigned pb = dppu<BCAST + (I & 15)>(pp[I >> 4]); acc = mfma(V[I], sel & pb, acc); PIN(acc); };
  const int ocol = 64 * (h >> 2) + 16 * r + 4 * (h & 3) + (c & 3);
  unsigned b2v, xo;
  int lw2 = (w >> 1) * 1024 + lane * 16 + (w & 1) * 8;
  u32x4 blast;
  const rsrc_t rdum = mk_rsrc(kc + kvoff - 262144, 262144);
  sfor<29>([&](auto I) {
    constexpr int i = I + 6;
    hook(IC(i + 32));
    if constexpr (i < 32) { PACE(); ldv(IC(i)); }
    else if constexpr (DUMMY) { PACE(); dma1<true>(rdum, w < DUMW ? vkv : OOB, (i - 32) * 8192, ldsd); }
    else PACEN(35 - i);
    if constexpr (i == 24) { ald16(b2v, mk_rsrc(b2, D * 2), w == 0 ? ocol * 2 : OOB, 0); ald16(xo, mk_rsrc(x, NB * D * 2), w == 0 ? ocol * 2 : OOB, b * (D * 2)); PIN(lw2); }
    if constexpr (OTOUCH && i == 8) { __builtin_amdgcn_raw_buffer_store_b16((unsigned short)0, mk_rsrc(out, NB * D * 2), (w == 0 && c < 4) ? (b * D + ocol) * 2 : OOB, 0, 0); SB(); }
    if constexpr (i == 6) { pv(IC(0)); pv(IC(1)); }
    if constexpr (i + 32 == CHKS) {
      BARRIER();
      const f32x2 ml = *(const LAS f32x2*)(L + L_ML + (lane & 7) * 8);
      float M = ml[0];
      M = fmaxf(M, dppf<XOR1>(M)); M = fmaxf(M, dppf<XOR2>(M)); M = fmaxf(M, dppf<HMIR>(M));
      float Ls = ml[1] * __builtin_amdgcn_exp2f((ml[0] - M) * 1.4426950408889634f);
      Ls += dppf<XOR1>(Ls); Ls += dppf<XOR2>(Ls); Ls += dppf<HMIR>(Ls);
      const float scale = __builtin_amdgcn_exp2f((mw - M) * 1.4426950408889634f) / Ls;
      acc *= scale;
      pp[1] = cvtpk(e1 * scale, e1 * scale);
    }
    pv(IC(i - 4));
    SB();
  });
  blast = sel & dppu<BCAST + 15>(pp[1]);
  PIN(blast);
  PACEN(DUMMY ? 3 : 0);
  STAMP(4); TSTAMP(1);
  PIN(V[31]);
  acc = mfma(V[31], blast, acc);
  *(LAS f32x2*)(L + L_RED2 + lw2) = (c & 8) ? f32x2{acc[2], acc[3]} : f32x2{acc[0], acc[1]};
  BARRIER();
  PIN(b2v); PIN(xo);
  if constexpr (DUMMY > 3) sfor<(DUMMY > 3 ? DUMMY - 3 : 0)>([&](auto J) { PACE(); dma1<true>(rdum, w < DUMW ? vkv : OOB, (J + 3) * 8192, ldsd); });
  if (w == 0) {
    const rsrc_t rws = mk_rsrc(ws, WS_BYTES);
    int vin = blk * IN_BYTES + lane * 16;
    if (SENTP) {
      u32x4 sent4 = {SENT, SENT, SENT, SENT};
      PIN(sent4);
      __builtin_amdgcn_raw_buffer_store_b128(sent4, rws, vin, IN_P2, AUX_SC1);
      __builtin_amdgcn_raw_buffer_store_b128(sent4, rws, vin, IN_P2 + 1024, AUX_SC1);
      asm volatile("s_nop 1" :: "v"(sent4) : "memory");
    }
    int vhs = HST == 0 ? (b * 32 + c) * IN_BYTES + IN_H + h * 32 + r * 8 : (b * 32 + (lane >> 1)) * IN_BYTES + IN_H + h * 32 + (lane & 1) * 16;
    int vout = c < 4 ? (b * D + ocol) * 2 : OOB;
    b2v <<= 16; xo <<= 16;
    PIN(vin); PIN(vhs); PIN(vout); PIN(b2v); PIN(xo);
    if (DLY0) __builtin_amdgcn_s_sleep(DLY0);
    PACEN(0);
    const float hf = asf(xo) + rbf(gather16(rws, vin + IN_P1));
    const unsigned hq = min(cvtpk(hf, dppf<XOR1>(hf)), SENT - 1u);
    unsigned hb[4];
    rows_bcast(dppu<0x88>(hq), hb);
    __builtin_amdgcn_raw_buffer_store_b128(u32x4{hb[0], hb[1], hb[2], hb[3]}, rws, vhs, 0, AUX_SC1);
    SB();
    STAMP(7); TSTAMP(5);
    BARRIER();
    float hres = asf(hq << 16);
    PIN(hres);
    if (DLY1) __builtin_amdgcn_s_sleep(DLY1);
    u32x4 hr;
    for (unsigned spins = 0; spins < SPIN_CAP; ++spins) {
      asm volatile("" ::: "memory");
      hr = __builtin_amdgcn_raw_buffer_load_b128(rws, vin, IN_H, AUX_SC1);
      if (__builtin_amdgcn_ballot_w64(max4(hr) == SENT) == 0) break;
      if ((spins & 63u) == 63u) {
        const int vr = (b * 32 + (lane >> 2)) * IN_BYTES + IN_P1 + h * 64 + (lane & 3) * 16;
        __builtin_amdgcn_raw_buffer_store_b128(*(const LAS u32x4*)(L + L_Y + lane * 16), rws, vr, 0, AUX_SC1);
        __builtin_amdgcn_raw_buffer_store_b128(*(const LAS u32x4*)(L + L_Y + 1024 + lane * 16), rws, vr, 16 * IN_BYTES, AUX_SC1);
      }
    }
    TSTAMP(7);
    {
      const float s1 = dot2(hr[3], 0x3f803f80u, dot2(hr[2], 0x3f803f80u, dot2(hr[1], 0x3f803f80u, dot2(hr[0], 0x3f803f80u, 0.f))));
      const float s2 = dot2(hr[3], hr[3], dot2(hr[2], hr[2], dot2(hr[1], hr[1], dot2(hr[0], hr[0], 0.f))));
      float t = tsum32(s1, s2);
      t = row_sum(tsum16(t, t));
      const float mean = asf(__builtin_amdgcn_readlane(fbits(t), 0)) * (1.f / D);
      const float var = fmaxf(fmaf(-mean, mean, asf(__builtin_amdgcn_readlane(fbits(t), 32)) * (1.f / D)), 0.f);
      const float den = rbf(__builtin_amdgcn_sqrtf(rbf(rbf(var) + 1e-5f)));
      *(LAS f32x2*)(L + L_ST) = f32x2{rbf(mean), __builtin_amdgcn_rcpf(den)};
    }
    if (TAILST == 1) TSTAMP(9);
    BARRIER();
    if (DLY2) __builtin_amdgcn_s_sleep(DLY2);
    const float res = hres + rbf(rbf(gather16(rws, vin + IN_P2)) + asf(b2v));
    __builtin_amdgcn_raw_buffer_store_b16(__builtin_bit_cast(unsigned short, (__bf16)res), mk_rsrc(out, NB * D * 2), vout, 0, 0);
    { SB(); const unsigned long long xt1 = __builtin_amdgcn_s_memrealtime(); const unsigned long long tgt = xt1 + 9 * (xt1 - xt0); while (__builtin_amdgcn_s_memrealtime() < tgt) {} }
  } else if (w >= 4) {
    const int u = w - 4, q4 = (lane >> 2) & 3;
    const rsrc_t rws = mk_rsrc(ws, WS_BYTES);
    __builtin_amdgcn_s_sleep(3);
    u32x8 W1[6];
    LAS unsigned char* p1 = L + L_W + u * 32768 + (lane & 3) * 8 + (4 * (r >> 1) + 32 * (r & 1) + q4) * 128;
    sfor<6>([&](auto J) { W1[J] = tr_read8<16 * 128, 8 * 128>(p1 + (J & 1) * 16384 + (J >> 1) * 32); });
    sfor<6>([&](auto J) { PIN(W1[J]); });
    __builtin_amdgcn_s_sleep(6);
    int vhl = blk * IN_BYTES + IN_H + u * 256 + r * 32 + (lane & 1) * 16, lwr = lane * 16 + u * 4;
    int idx = (lane & 1) ? 0xEEEEEEEE : 0x44444444;
    const f32x4 zero4 = {0.f, 0.f, 0.f, 0.f};
    f32x4 e[3];
    auto project = [&](u32x4 h0, u32x4 h1, int dst) {
      sfor<3>([&](auto T) { e[T] = smf(h0, W1[2 * T], e[T], idx); SB(); e[T] = smf(h1, W1[2 * T + 1], e[T], idx); SB(); });
      float a0 = e[0][0] + e[0][1], a1 = e[1][0] + e[1][1];
      a0 = r == 0 ? a0 : a1;
      PIN(a0);
      const float a2 = e[2][0] + e[2][1];
      KEEP(e[0]); KEEP(e[1]); KEEP(e[2]);
      *(LAS float*)(L + dst + lwr) = r >= 2 ? a2 : a0;
    };
    const u32x4 ones = {0x3f803f80u, 0x3f803f80u, 0x3f803f80u, 0x3f803f80u};
    sfor<3>([&](auto T) { e[T] = zero4; });
    project(ones, ones, L_EX);
    sfor<3>([&](auto T) { e[T] = zero4; PIN(e[T]); });
    PIN(vhl); PIN(lwr); PIN(idx);
    STAMP(6);
    BARRIER();
    if (DLY1) __builtin_amdgcn_s_sleep(DLY1);
    u32x4 h0, h1;
    for (unsigned spins = 0; spins < SPIN_CAP; ++spins) {
      asm volatile("" ::: "memory");
      h0 = __builtin_amdgcn_raw_buffer_load_b128(rws, vhl, 0, AUX_SC1);
      h1 = __builtin_amdgcn_raw_buffer_load_b128(rws, vhl, 128, AUX_SC1);
      if (__builtin_amdgcn_ballot_w64(max(max4(h0), max4(h1)) == SENT) == 0) break;
    }
    SB();
    STAMP(8); TSTAMP(6);
    project(h0, h1, L_EX2);
    if (TAILST == 1) TSTAMP(8);
    BARRIER();
  }
  }
}

extern "C" void kernel_launch(void* const* d_in, const int* in_sizes, int n_in, void* d_out, int out_size, void* d_ws,
                              size_t ws_size, hipStream_t stream) {
  fused_block<<<NB * NH, 768, 0, stream>>>((const unsigned char*)d_in[3], (const unsigned char*)d_in[1], (const unsigned char*)d_in[4],
                                      (const unsigned char*)d_in[2], (const unsigned short*)d_in[0], (const unsigned char*)d_in[6],
                                      (const unsigned char*)d_in[7], (const unsigned char*)d_in[8], (const unsigned short*)d_in[9],
                                      (unsigned short*)d_out, (unsigned char*)d_ws);
}
```

```cpp
#define DV 5
#define WAKE 1
#include <hip/hip_runtime.h>
#include <hip/hip_bf16.h>
#include <stdint.h>

#define STAMPS 0
#define PFCODE 0
#define WN 3
#define BWN 3
#define QB0 4
#define QRDY 9
#define QSTEP 28
#define OB0 14
#define MB0 35
#define QBS 8
#define OBS 12
#define MBS 12
#define NG 8
#define OWD 5
#define INB 8192
#define SENTH 0
#define SENTP 1
#define CHKS 61
#define PEND 1850
#define PTAU 24
#define PBUR 90
#define PBUM 110
#define DITH 32
#define PSH 1
#define DITG 0
#define TSTEP 14
#define PRIO 0
#define UD1 10
#define UD2 1
#define W2B 1
#define QOWN 2
#define HDLY 0
#define SDLY 0
#define QNT false
#define PADS 1
#define MSTEP 6
#define MWAVE 3
#define NREP 32
#define TOFF 215
#define OTOUCH 1
#define COLD 0
#define TFRAC 4
#define TCOM 1
#define SKEW 0
#define PFROM 10
#define PTO 60
#define PEV 2
#define SENTE 1
#define LOSE -1
#define EARLYST 0
#define TOUCH 0
#define SENTW 0
#define TAILST 0
#define HST 1
#define EXPM 0
#define DLY0 17
#define DLY1 11
#define DLY2 18

typedef unsigned u32x4 __attribute__((ext_vector_type(4)));
typedef unsigned u32x2 __attribute__((ext_vector_type(2)));
typedef float f32x2 __attribute__((ext_vector_type(2)));
typedef float f32x4 __attribute__((ext_vector_type(4)));
typedef __bf16 bf16x2 __attribute__((ext_vector_type(2)));
typedef short s16x4 __attribute__((ext_vector_type(4)));
typedef unsigned u32x8 __attribute__((ext_vector_type(8)));
typedef __bf16 bf16x8 __attribute__((ext_vector_type(8)));
typedef __bf16 bf16x16 __attribute__((ext_vector_type(16)));
typedef __amdgpu_buffer_rsrc_t rsrc_t;
#define LAS __attribute__((address_space(3)))
#define DI __device__ __forceinline__

constexpr int NB = 8, D = 512, NH = 32, SK = 4096, DH = 128, WIN = 1024, F = 1376;
constexpr int FS = 48;
constexpr int AUX_NT = 2, AUX_SC1 = 16;
constexpr int OOB = 0x7ffffff0;
constexpr unsigned SENT = 0xffffffffu;
constexpr unsigned SPIN_CAP = 1u << 17;
constexpr int IN_P1 = 0;
constexpr int IN_P2 = 3072;
constexpr int IN_H = 2048;
constexpr int IN_BYTES = INB;
constexpr int WS_T = 256 * IN_BYTES;
constexpr int WS_BYTES = WS_T + 128;
constexpr int WS_STAMP = 4 * 1024 * 1024;
constexpr int L_RED = 0;
constexpr int L_RED2 = L_RED + 2048;
constexpr int L_EX = L_RED2 + 4096;
constexpr int L_EX2 = L_EX + 1024;
constexpr int L_ML = L_EX2 + 1024;
constexpr int L_ST = L_ML + 64;
constexpr int L_T = L_ST + 16;
constexpr int L_Y = 9216;
constexpr int L_DUMP = L_Y + 2048;
constexpr int L_W = L_DUMP + 1024;
constexpr int L_BYTES = L_W + 128 * 1024;

DI float dot2(unsigned a, unsigned b, float acc) {
  return __builtin_amdgcn_fdot2_f32_bf16(__builtin_bit_cast(bf16x2, a), __builtin_bit_cast(bf16x2, b), acc, false);
}
DI unsigned fbits(float f) { return __builtin_bit_cast(unsigned, f); }
DI float asf(unsigned u) { return __builtin_bit_cast(float, u); }
DI unsigned cvtpk(float lo, float hi) {
  bf16x2 r = {(__bf16)lo, (__bf16)hi};
  return __builtin_bit_cast(unsigned, r);
}
DI float rbf(float f) { return asf((unsigned)__builtin_bit_cast(unsigned short, (__bf16)f) << 16); }
template <int CTRL> DI unsigned dppu(unsigned x) { return __builtin_amdgcn_mov_dpp(x, CTRL, 0xf, 0xf, true); }
template <int CTRL> DI float dppf(float x) { return asf(dppu<CTRL>(fbits(x))); }
constexpr int XOR1 = 0xB1, XOR2 = 0x4E, HMIR = 0x141, ROR4 = 0x124, ROR8 = 0x128, BCAST = 0x150;
DI float tsum32(float a, float b) {
  auto s = __builtin_amdgcn_permlane32_swap(fbits(a), fbits(b), false, false);
  return asf(s[0]) + asf(s[1]);
}
DI float tsum16(float a, float b) {
  auto s = __builtin_amdgcn_permlane16_swap(fbits(a), fbits(b), false, false);
  return asf(s[0]) + asf(s[1]);
}
DI float row_sum(float x) { x += dppf<XOR1>(x); x += dppf<XOR2>(x); x += dppf<ROR4>(x); x += dppf<ROR8>(x); return x; }
DI float row_max(float x) { x = fmaxf(x, dppf<XOR1>(x)); x = fmaxf(x, dppf<XOR2>(x)); x = fmaxf(x, dppf<ROR4>(x)); x = fmaxf(x, dppf<ROR8>(x)); return x; }
DI float wave_sum(float x) { x = row_sum(x); x = tsum16(x, x); return tsum32(x, x); }
DI float wave_max(float x) {
  x = row_max(x);
  auto s = __builtin_amdgcn_permlane16_swap(fbits(x), fbits(x), false, false);
  x = fmaxf(asf(s[0]), asf(s[1]));
  auto t = __builtin_amdgcn_permlane32_swap(fbits(x), fbits(x), false, false);
  return fmaxf(asf(t[0]), asf(t[1]));
}
DI void rows_bcast(unsigned x, unsigned (&o)[4]) {
  auto s = __builtin_amdgcn_permlane16_swap(x, x, false, false);
  auto a = __builtin_amdgcn_permlane32_swap(s[0], s[0], false, false);
  auto b = __builtin_amdgcn_permlane32_swap(s[1], s[1], false, false);
  o[0] = a[0]; o[2] = a[1]; o[1] = b[0]; o[3] = b[1];
}
DI f32x4 smf(u32x4 a, u32x8 b, f32x4 c, int idx) {
  return __builtin_amdgcn_smfmac_f32_16x16x64_bf16(__builtin_bit_cast(bf16x8, a), __builtin_bit_cast(bf16x16, b), c, idx, 0, 0);
}
DI f32x4 mfma(u32x4 a, u32x4 b, f32x4 c) {
  return __builtin_amdgcn_mfma_f32_16x16x32_bf16(__builtin_bit_cast(bf16x8, a), __builtin_bit_cast(bf16x8, b), c, 0, 0, 0);
}
constexpr int SHL = 0x100;
template <class T> DI void pin_(T& x) { asm volatile("" : "+v"(x)); }
#define PIN(x) pin_(x)
template <class T> DI void keep_(const T& x) { asm volatile("" :: "v"(x)); }
#define KEEP(x) keep_(x)
template <class T> DI void pins_(T& x) { asm volatile("" : "+s"(x)); }
#define PINS(x) pins_(x)
#define SB() __builtin_amdgcn_sched_barrier(0)
#define NEW_PAGE() do { if (PADS) { SB(); asm volatile("s_branch 1f\n\t.p2align 12\n1:"); SB(); } } while (0)
#define PAD(id, next) do { if (PADS) { SB(); asm volatile("PAD" #id ":\n\ts_cbranch_execz PAD" #next); SB(); } } while (0)
#define VISIT(u, p0, p1, p2, p3, ret) do { if (PADS) { SB(); asm volatile( \
    "s_mov_b64 exec, 0\n\ts_cmp_eq_u32 %0, 0\n\ts_cbranch_scc1 PAD" #p0 "\n\ts_cmp_eq_u32 %0, 1\n\ts_cbranch_scc1 PAD" #p1 "\n\t" \
    "s_cmp_eq_u32 %0, 2\n\ts_cbranch_scc1 PAD" #p2 "\n\ts_branch PAD" #p3 "\nPAD" #ret ":\n\ts_mov_b64 exec, -1" :: "s"(u) : "scc"); SB(); } } while (0)
#define PACEN(n) do { asm volatile("s_waitcnt vmcnt(%0)" :: "i"(n) : "memory"); SB(); } while (0)
#define PACE() PACEN(WN)
#define BARRIER() do { asm volatile("s_waitcnt lgkmcnt(0)\n\ts_barrier" ::: "memory"); SB(); } while (0)
DI rsrc_t mk_rsrc(const void* p, int bytes) { return __builtin_amdgcn_make_buffer_rsrc((void*)p, 0, bytes, 0x00020000); }

template <int G, int N, int CNT = N> DI void burst(int grp, rsrc_t r, int voff, int stride, int lds, int j0) {
  int keep, j, cnt, t;
  asm volatile(
      "s_cmp_lg_u32 %4, %10\n\t"
      "s_cbranch_scc1 BE%=\n\t"
      "s_mov_b32 %0, m0\n\t"
      "s_mov_b32 %1, %9\n\t"
      "s_mov_b32 %2, %12\n"
      "BL%=:\n\t"
      "s_lshl_b32 %3, %1, 10\n\t"
      "s_add_u32 m0, %3, %8\n\t"
      "s_mul_i32 %3, %1, %7\n\t"
      "s_add_u32 %1, %1, 1\n\t"
      "s_waitcnt vmcnt(%13)\n\t"
      "buffer_load_dwordx4 %6, %5, %3 offen lds\n\t"
      "s_cmp_eq_u32 %1, %11\n\t"
      "s_cselect_b32 %1, 0, %1\n\t"
      "s_sub_u32 %2, %2, 1\n\t"
      "s_cmp_lg_u32 %2, 0\n\t"
      "s_cbranch_scc1 BL%=\n\t"
      "s_mov_b32 m0, %0\n"
      "BE%=:"
      : "=&s"(keep), "=&s"(j), "=&s"(cnt), "=&s"(t)
      : "s"(grp), "s"(r), "v"(voff), "s"(stride), "s"(lds), "s"(j0), "i"(G), "i"(N), "i"(CNT), "i"(BWN)
      : "memory", "scc");
  SB();
}
DI void ald128nt(u32x4& d, rsrc_t r, int voff, int soff) { asm volatile("buffer_load_dwordx4 %0, %1, %2, %3 offen nt" : "=v"(d) : "v"(voff), "s"(r), "s"(soff) : "memory"); SB(); }
DI void ald128(u32x4& d, rsrc_t r, int voff, int soff) { asm volatile("buffer_load_dwordx4 %0, %1, %2, %3 offen" : "=v"(d) : "v"(voff), "s"(r), "s"(soff) : "memory"); SB(); }
DI void ald64sc(u32x2& d, rsrc_t r, int voff, int soff) { asm volatile("buffer_load_dwordx2 %0, %1, %2, %3 offen sc1" : "=v"(d) : "v"(voff), "s"(r), "s"(soff) : "memory"); SB(); }
DI void ald32sc(unsigned& d, rsrc_t r, int voff) { asm volatile("buffer_load_dword %0, %1, %2, 0 offen sc1" : "=v"(d) : "v"(voff), "s"(r) : "memory"); SB(); }
DI void ald32(unsigned& d, rsrc_t r, int voff) { asm volatile("buffer_load_dword %0, %1, %2, 0 offen" : "=v"(d) : "v"(voff), "s"(r) : "memory"); SB(); }
DI void ald16(unsigned& d, rsrc_t r, int voff, int soff) { asm volatile("buffer_load_ushort %0, %1, %2, %3 offen" : "=v"(d) : "v"(voff), "s"(r), "s"(soff) : "memory"); SB(); }
template <bool NT = false> DI void dma1(rsrc_t r, int voff, int soff, int lds) {
  int keep;
  if constexpr (NT)
    asm volatile("s_mov_b32 %0, m0\n\ts_mov_b32 m0, %4\n\ts_nop 0\n\tbuffer_load_dwordx4 %1, %2, %3 offen nt lds\n\ts_mov_b32 m0, %0"
                 : "=&s"(keep) : "v"(voff), "s"(r), "s"(soff), "s"(lds) : "memory");
  else
    asm volatile("s_mov_b32 %0, m0\n\ts_mov_b32 m0, %4\n\ts_nop 0\n\tbuffer_load_dwordx4 %1, %2, %3 offen lds\n\ts_mov_b32 m0, %0"
                 : "=&s"(keep) : "v"(voff), "s"(r), "s"(soff), "s"(lds) : "memory");
  SB();
}
template <int G> DI void cond_barrier(int grp) {
  asm volatile("s_cmp_lg_u32 %0, %1\n\ts_cbranch_scc1 CB%=\n\ts_barrier\nCB%=:" :: "s"(grp), "i"(G) : "memory", "scc");
  SB();
}
DI float gather16(rsrc_t r, int voff);
DI u32x2 tr_read(LAS unsigned char* p) {
  return __builtin_bit_cast(u32x2, __builtin_amdgcn_ds_read_tr16_b64_v4i16((LAS s16x4*)p));
}
template <int M1, int M2> DI u32x8 tr_read8(LAS unsigned char* p) {
  const u32x2 a = tr_read(p), b = tr_read(p + M1), c = tr_read(p + M2), d = tr_read(p + M1 + M2);
  return u32x8{a[0], a[1], b[0], b[1], c[0], c[1], d[0], d[1]};
}
DI f32x2 fold_rows8(const float (&acc)[8]) {
  const float t0 = tsum32(acc[0], acc[4]), t1 = tsum32(acc[1], acc[5]), t2 = tsum32(acc[2], acc[6]), t3 = tsum32(acc[3], acc[7]);
  return f32x2{tsum16(t0, t2), tsum16(t1, t3)};
}
DI float fold_rows4(float a0, float a1, float a2, float a3) { return tsum16(tsum32(a0, a2), tsum32(a1, a3)); }
DI unsigned max4(u32x4 v) { return max(max(v[0], v[1]), max(v[2], v[3])); }

DI bool any_nan(float t) { return __builtin_amdgcn_ballot_w64(t != t) != 0; }
DI bool any_sent(unsigned m) { return __builtin_amdgcn_ballot_w64(m == SENT) != 0; }
DI float gather16(rsrc_t r, int voff) {
  u32x4 A, Bv;
  auto fetch = [&]() {
    asm volatile("" ::: "memory");
    A = __builtin_amdgcn_raw_buffer_load_b128(r, voff, 0, AUX_SC1);
    Bv = __builtin_amdgcn_raw_buffer_load_b128(r, voff + 1024, 0, AUX_SC1);
  };
  auto total = [&]() {
    const f32x4 s = __builtin_bit_cast(f32x4, A) + __builtin_bit_cast(f32x4, Bv);
    float t = fold_rows4(s[0], s[1], s[2], s[3]);
    t += dppf<ROR4>(t); t += dppf<ROR8>(t);
    return t;
  };
  fetch();
  float t = total();
  if (any_nan(t)) {
#pragma unroll 1
    for (unsigned spins = 0; spins < SPIN_CAP; ++spins) {
      fetch();
      if (!any_sent(max(max4(A), max4(Bv)))) break;
    }
    t = total();
  }
  return t;
}

template <int I> DI void pv_step(float (&acc)[8], unsigned plo, unsigned phi, u32x4 v) {
  const unsigned lo = dppu<BCAST + I>(plo), hi = dppu<BCAST + I>(phi);
#pragma unroll
  for (int k = 0; k < 4; ++k) { acc[2 * k] = dot2(lo, v[k], acc[2 * k]); acc[2 * k + 1] = dot2(hi, v[k], acc[2 * k + 1]); }
}
DI void pin8(float (&a)[8]) {
  asm volatile("" : "+v"(a[0]), "+v"(a[1]), "+v"(a[2]), "+v"(a[3]), "+v"(a[4]), "+v"(a[5]), "+v"(a[6]), "+v"(a[7]));
}
template <int... Is> struct iseq {};
template <int N, int... Is> struct mkseq : mkseq<N - 1, N - 1, Is...> {};
template <int... Is> struct mkseq<0, Is...> { typedef iseq<Is...> type; };
template <class Fn, int... Is> DI void sfor_(Fn&& f, iseq<Is...>) { (f(std::integral_constant<int, Is>{}), ...); }
template <int N, class Fn> DI void sfor(Fn&& f) { sfor_(f, typename mkseq<N>::type{}); }

DI float transpose_reduce16(float (&x)[16], int c) {
#define TR_STEP(HALF, CTRL, BIT) _Pragma("unroll") for (int h_ = 0; h_ < (HALF); ++h_) { \
    const float lo_ = x[h_], hi_ = x[h_ + (HALF)]; const float t1_ = lo_ + dppf<CTRL>(lo_), t2_ = hi_ + dppf<CTRL>(hi_); \
    x[h_] = (c & (BIT)) ? t2_ : t1_; }
  TR_STEP(8, ROR8, 8) TR_STEP(4, HMIR, 4) TR_STEP(2, XOR2, 2) TR_STEP(1, XOR1, 1)
#undef TR_STEP
  return x[0];
}

#define STAMP(i) do {} while (0)
#define ESTAMP(i) do {} while (0)
#define TSTAMP(i) do {} while (0)

__device__ unsigned char wake_area[1 << 20];
extern "C" __global__ void __launch_bounds__(768, 3) __attribute__((aligned(4096)))
fused_block(const unsigned char* __restrict__ wq, const unsigned char* __restrict__ kc, const unsigned char* __restrict__ wo,
            const unsigned char* __restrict__ vc, const unsigned short* __restrict__ x, const unsigned char* __restrict__ w1,
            const unsigned char* __restrict__ b1, const unsigned char* __restrict__ w2, const unsigned short* __restrict__ b2,
            unsigned short* __restrict__ out, unsigned char* ws) {
  __shared__ __attribute__((aligned(16))) unsigned char smem[L_BYTES];
  LAS unsigned char* L = (LAS unsigned char*)smem;
  if (SKEW && (blockIdx.x & 7) == 5) __builtin_amdgcn_s_sleep(SKEW);
  unsigned xt[5]; xt[0] = (unsigned)__builtin_amdgcn_s_memrealtime();
  unsigned wk;
  { const unsigned char* wp = wake_area + (blockIdx.x * 4096);
    asm volatile("global_load_dword %0, %1, %2 nt" : "=v"(wk) : "v"((threadIdx.x >= 704 && threadIdx.x < 720) ? (threadIdx.x & 15) * 256 : 0), "s"(wp) : "memory"); SB(); }
  const int blk = blockIdx.x, b = blk >> 5, h = blk & 31;
  const int tid = threadIdx.x, lane = tid & 63, r = lane >> 4, c = lane & 15;
  const int w = __builtin_amdgcn_readfirstlane(tid >> 6);
  const int slice = (h & 7) * 4 + (h >> 3), col0 = slice * FS;
#define IC(n) std::integral_constant<int, (n)>{}
  if (LOSE >= 0 && (blk == LOSE || blk == LOSE + 5)) for (int i = 0; i < (blk == LOSE ? 12 : 30); ++i) __builtin_amdgcn_s_sleep(127);
  {
    int w2 = w;
    PINS(w2);
    if (w2 >= (SENTP ? 9 : 8)) {
      if (COLD || SDLY) { asm volatile("s_waitcnt lgkmcnt(0)"); __builtin_amdgcn_s_sleep(COLD + SDLY); }
      const rsrc_t r0 = mk_rsrc(ws, WS_BYTES);
      u32x4 s4 = {SENT, SENT, SENT, SENT};
      PIN(s4);
      if (!(EXPM & 1)) __builtin_amdgcn_raw_buffer_store_b128(s4, r0, lane * 16, blk * IN_BYTES + (w2 - (SENTP ? 9 : 8)) * 1024, AUX_SC1);
      if (!SENTP) __builtin_amdgcn_raw_buffer_store_b128(s4, r0, w2 == 8 ? lane * 16 : OOB, blk * IN_BYTES + 4096, AUX_SC1);
      SB(); KEEP(s4);
    }
    SB();
  }
  if (w >= 8) {
  const int u = w - 8;
  if (HDLY) { __builtin_amdgcn_s_sleep(HDLY); SB(); }
  const rsrc_t rws = mk_rsrc(ws, WS_BYTES);
  u32x4 xrow; unsigned xr0, xr1, b1r;
  {
    const rsrc_t rx = mk_rsrc(x + b * D, D * 2);
    ald128(xrow, rx, lane * 16, 0);
    ald16(xr0, rx, (4 * c + r) * 2, u * 256);
    ald16(xr1, rx, (4 * c + r) * 2, u * 256 + 128);
    ald16(b1r, mk_rsrc(b1, F * 2), lane < FS ? (col0 + lane) * 2 : OOB, 0);
  }
  if (PFCODE && (blk >> 3) == PFCODE) {
    unsigned long long pc; int o0, o1;
    asm volatile("s_getpc_b64 %0\nKPC0:\n\ts_mov_b32 %1, KPC0-KPCA\n\ts_mov_b32 %2, KPCB-KPCA" : "=s"(pc), "=s"(o0), "=s"(o1));
    const rsrc_t rc = mk_rsrc((const void*)(pc - (long long)o0), o1);
    unsigned p0 = __builtin_amdgcn_raw_buffer_load_b32(rc, lane * 128, u * 8192, 0);
    PIN(p0);
  }
  VISIT(u, 10, 11, 12, 13, R1);
  PACEN(0);
  PIN(xrow); PIN(xr0); PIN(xr1); PIN(b1r);
  PIN(wk);
  const float xe0 = asf(xr0 << 16), xe1 = asf(xr1 << 16);
  float b1v = asf(b1r << 16);
  {
    float xf[8];
#pragma unroll
    for (int k = 0; k < 4; ++k) { xf[2 * k] = asf(xrow[k] << 16); xf[2 * k + 1] = asf(xrow[k] & 0xffff0000u); }
    const float mean = wave_sum(((xf[0] + xf[1]) + (xf[2] + xf[3])) + ((xf[4] + xf[5]) + (xf[6] + xf[7]))) * (1.f / D);
    float vs = 0.f;
#pragma unroll
    for (int k = 0; k < 8; ++k) { const float d = xf[k] - mean; vs = fmaf(d, d, vs); }
    const float var = wave_sum(vs) * (1.f / D);
    const float den = rbf(sqrtf(rbf(rbf(var) + 1e-5f))), mb = rbf(mean);
    const unsigned xp = cvtpk(rbf(xe0 - mb) / den, rbf(xe1 - mb) / den);
    unsigned xlo[2] = {xp & 0xffffu, xp >> 16}, xhi[2] = {xp << 16, xp & 0xffff0000u};
    PIN(xlo[0]); PIN(xlo[1]); PIN(xhi[0]); PIN(xhi[1]); PIN(b1v);
    ESTAMP(5);
    NEW_PAGE(); PAD(12, 14);
    BARRIER();
    ESTAMP(6);
    float acc[8] = {0.f, 0.f, 0.f, 0.f, 0.f, 0.f, 0.f, 0.f};
    sfor<32>([&](auto I) { pv_step<I & 15>(acc, xlo[I >> 4], xhi[I >> 4], *(const LAS u32x4*)(L + L_W + u * 32768 + I * 1024 + lane * 16)); if constexpr (I == 15) PAD(13, 20); });
    *(LAS f32x2*)(L + L_RED + (u >> 1) * 1024 + lane * 16 + (u & 1) * 8) = fold_rows8(acc);
  }
  STAMP(11);
  BARRIER();
  BARRIER();
  u32x8 WO[16];
  const int q4 = (lane >> 2) & 3;
  LAS unsigned char* lw = L + L_W + u * 32768 + (lane & 3) * 8;
  {
    LAS unsigned char* p = lw + (32 * (r >> 1) + 4 * (r & 1) + (q4 >> 1) + 8 * (q4 & 1)) * 128;
    sfor<16>([&](auto J) { WO[J] = tr_read8<2 * 128, 64 * 128>(p + (J >> 3) * 16384 + ((J >> 1) & 3) * 32 + (J & 1) * 16 * 128); });
    sfor<16>([&](auto J) { PIN(WO[J]); });
  }
  VISIT(u, 30, 31, 32, 33, R2);
  NEW_PAGE();
  BARRIER();
  BARRIER();
  int vp = lane < 16 ? (b * 32 + 8 * u + (lane >> 2)) * IN_BYTES + (lane & 3) * 16 : OOB;
  int lrd = lane * 16;
  int idx = (lane & 1) ? 0xEEEEEEEE : 0x44444444;
  PIN(vp); PIN(lrd); PIN(idx);
  const f32x4 zero4 = {0.f, 0.f, 0.f, 0.f};
  f32x4 d[8];
  d[0] = zero4; d[1] = zero4; PIN(d[0]); PIN(d[1]);
  int so1 = IN_P1 + h * 64, so2 = IN_P1 + h * 64 + 4 * IN_BYTES;
  PINS(so1); PINS(so2);
  if (PRIO) __builtin_amdgcn_s_setprio(2);
  BARRIER();
  TSTAMP(2);
  {
    f32x4 s4 = *(const LAS f32x4*)(L + L_RED2 + lrd) + *(const LAS f32x4*)(L + L_RED2 + 1024 + lrd);
    s4 += *(const LAS f32x4*)(L + L_RED2 + 2048 + lrd) + *(const LAS f32x4*)(L + L_RED2 + 3072 + lrd);
    const f32x2 o2 = f32x2{s4[0], s4[1]} + f32x2{s4[2], s4[3]};
    const unsigned op = cvtpk(o2[0], o2[1]);
    const u32x4 a0 = {op, dppu<SHL + 2>(op), dppu<SHL + 4>(op), dppu<SHL + 6>(op)};
    u32x4 a1;
    a1[0] = dppu<SHL + 8>(op); a1[1] = dppu<SHL + 10>(op);
    SB();
    d[0] = smf(a0, WO[0], d[0], idx);
    SB();
    a1[2] = dppu<SHL + 12>(op); a1[3] = dppu<SHL + 14>(op);
    SB();
    d[1] = smf(a0, WO[2], d[1], idx);
    SB();
    d[2] = zero4; PIN(d[2]);
    SB();
    d[0] = smf(a1, WO[1], d[0], idx);
    SB();
    d[3] = zero4; PIN(d[3]);
    SB();
    d[1] = smf(a1, WO[3], d[1], idx);
    SB();
    float y[8];
    auto fin = [&](auto T) { y[T] = d[T][0] + d[T][1]; PIN(y[T]); KEEP(d[T]); };
    sfor<6>([&](auto J) {
      constexpr int T = J + 2;
      d[T] = smf(a0, WO[2 * T], d[T], idx);
      SB();
      if constexpr (T + 2 < 8) { d[T + 2] = zero4; PIN(d[T + 2]); }
      if constexpr (T == 6)
        __builtin_amdgcn_raw_buffer_store_b128(u32x4{fbits(y[0]), fbits(y[1]), fbits(y[2]), fbits(y[3])}, rws, (EXPM & 8) ? OOB : vp, so1, AUX_SC1);
      SB();
      d[T] = smf(a1, WO[2 * T + 1], d[T], idx);
      SB();
      fin(IC(T - 2));
      SB();
    });
    fin(IC(6)); fin(IC(7));
    if (TAILST == 3) TSTAMP(4);
    __builtin_amdgcn_raw_buffer_store_b128(u32x4{fbits(y[4]), fbits(y[5]), fbits(y[6]), fbits(y[7])}, rws, (EXPM & 8) ? OOB : vp, so2, AUX_SC1);
    SB();
    if (TAILST == 3) TSTAMP(5); else TSTAMP(3);
    if (lrd < 256) {
      *(LAS f32x4*)(L + L_Y + u * 512 + lrd) = f32x4{y[0], y[1], y[2], y[3]};
      *(LAS f32x4*)(L + L_Y + u * 512 + 256 + lrd) = f32x4{y[4], y[5], y[6], y[7]};
    }
  }
  SB();
  STAMP(5);
  u32x8 W2[8];
  if (W2B) BARRIER();
  {
    const int ln = __builtin_amdgcn_mbcnt_hi(~0u, __builtin_amdgcn_mbcnt_lo(~0u, 0u)), rr = ln >> 4, qq = (ln >> 2) & 3;
    LAS unsigned char* p2 = L + L_W + u * 32768 + (ln & 3) * 8 + 8192 + (16 * (rr >> 1) + 8 * (rr & 1) + (qq >> 1) + 2 * (qq & 1)) * 128;
    LAS unsigned char* p3 = p2 + (32 - 16 * (rr >> 1)) * 128;
    sfor<8>([&](auto J) {
      constexpr int o = (J >> 2) * 16384 + (J & 3) * 32;
      const u32x2 t0 = tr_read(p2 + o), t1 = tr_read(p2 + o + 4 * 128), t2 = tr_read(p3 + o), t3 = tr_read(p3 + o + 4 * 128);
      W2[J] = u32x8{t0[0], t0[1], t1[0], t1[1], t2[0], t2[1], t3[0], t3[1]};
    });
    sfor<8>([&](auto J) { PIN(W2[J]); d[J] = zero4; PIN(d[J]); });
  }
  PAD(35, R2);
  so1 = IN_P2 + slice * 64; so2 = IN_P2 + slice * 64 + 4 * IN_BYTES;
  int lst = L_ST;
  float gk = -2.f * 1.4426950408889634f * 0.7978845608028654f;
  unsigned gmask = lrd < FS * 16 ? ~0u : 0u;
  PINS(so1); PINS(so2); PIN(lst); PIN(gk); PIN(gmask);
  SB();
  STAMP(6);
  if (!W2B) BARRIER();
  float c1;
  { const f32x4 t = *(const LAS f32x4*)(L + L_EX + lrd); c1 = (t[0] + t[1]) + (t[2] + t[3]); }
  PIN(c1);
  BARRIER();
  if (TAILST == 2) TSTAMP(8);
  {
    const f32x4 t4 = *(const LAS f32x4*)(L + L_EX2 + lrd);
    const f32x2 ms = *(const LAS f32x2*)(L + lst);
    const float tot = (t4[0] + t4[1]) + (t4[2] + t4[3]);
    const float uu = rbf(rbf(fmaf(-ms[0], c1, tot) * ms[1]) + b1v);
    const float z = uu * fmaf(uu * uu, -2.f * 1.4426950408889634f * 0.044715f * 0.7978845608028654f, gk);
    const float g = asf(fbits(uu) & gmask) * __builtin_amdgcn_rcpf(1.f + __builtin_amdgcn_exp2f(z));
    const unsigned gp = cvtpk(g, dppf<XOR2>(g));
    const u32x4 ag = {gp, dppu<SHL + 4>(gp), dppu<SHL + 8>(gp), dppu<SHL + 12>(gp)};
    if (TAILST == 2) { KEEP(ag); TSTAMP(9); }
    float y[8];
    auto fin = [&](auto T) { y[T] = d[T][0] + d[T][1]; PIN(y[T]); KEEP(d[T]); SB(); };
    sfor<8>([&](auto T) { d[T] = smf(ag, W2[T], d[T], idx); SB(); });
    fin(IC(0)); fin(IC(1)); fin(IC(2)); fin(IC(3));
    __builtin_amdgcn_raw_buffer_store_b128(u32x4{fbits(y[0]), fbits(y[1]), fbits(y[2]), fbits(y[3])}, rws, vp, so1, AUX_SC1);
    SB();
    fin(IC(4)); fin(IC(5)); fin(IC(6)); fin(IC(7));
    KEEP(y[0]); KEEP(y[1]); KEEP(y[2]); KEEP(y[3]);
    __builtin_amdgcn_raw_buffer_store_b128(u32x4{fbits(y[4]), fbits(y[5]), fbits(y[6]), fbits(y[7])}, rws, vp, so2, AUX_SC1);
  }
  asm volatile("KPCB:");
  SB();
  STAMP(9); TSTAMP(10);
  } else {
  const int grp = NG == 8 ? (b + 2 * (h >> 3)) & 7 : (NG == 16 ? (2 * b + 4 * (h >> 4) + ((h >> 3) & 1)) & 15 : 4 * b + (h >> 3));
  const int pq = QB0 + ((grp * QBS) >> 2), po = OB0 + ((grp * OBS) >> 2), pm = MB0 + ((grp * MBS) >> 2);
  const size_t kvoff = ((size_t)blk * SK + (SK - WIN)) * (DH * 2);
  const rsrc_t rk = mk_rsrc(kc + kvoff, WIN * DH * 2), rv = mk_rsrc(vc + kvoff, WIN * DH * 2);
  const int vkv = tid * 16;
  const int lds0 = (int)(unsigned)(size_t)L, ldsw = lds0 + L_W + w * 16384, ldsd = lds0 + L_DUMP;

  u32x4 K[32], V[32];
  auto ldk = [&](auto I) { ald128nt(K[I], rk, vkv, I * 8192); };
  auto ldv = [&](auto I) { ald128nt(V[I], rv, vkv, I * 8192); };
  ldk(IC(0)); ldk(IC(1));
  if (QOWN > 2) { ldk(IC(2)); ldk(IC(3)); }
  const unsigned long long tclk = __builtin_amdgcn_s_memrealtime();
  SB();
  asm volatile("KPCA:");
  const rsrc_t rq = mk_rsrc(wq + h * (DH * 2), D * NH * DH * 2 - h * (DH * 2));
  const int vq = (64 * w + r) * (NH * DH * 2) + c * 16;
  if (QOWN == 2) {
    dma1<QNT>(rq, vq, 2 * b * (4 * NH * DH * 2), ldsw + 2 * b * 1024);
    dma1<QNT>(rq, vq, (2 * b + 1) * (4 * NH * DH * 2), ldsw + (2 * b + 1) * 1024);
  }
  unsigned tch = 0;
  if (TOUCH) {
    const unsigned char* p = w == 0 ? vc + kvoff : w == 1 ? wo + (size_t)h * (DH * D * 2) : w == 2 ? w1 : w == 3 ? w2 + (size_t)min(col0, F - 1) * (D * 2)
                           : w == 4 ? w1 + (D - 1) * F * 2 : w == 5 ? (const unsigned char*)x : w == 6 ? (const unsigned char*)b2 : b1;
    ald32(tch, mk_rsrc(p, 4), lane == 0 ? 0 : OOB);
  }
  const rsrc_t ro = mk_rsrc(wo + (size_t)h * (DH * D * 2), DH * D * 2);
  const int vo = (lane >> 3) * (D * 2) + w * 128 + (lane & 7) * 16;
  const rsrc_t r1 = mk_rsrc(w1, D * F * 2);
  const int v1 = ((lane & 7) < 6 && col0 + (lane & 7) * 8 < F) ? (64 * w + (lane >> 3)) * (F * 2) + col0 * 2 + (lane & 7) * 16 : OOB;
  const int c2 = min(col0, F);
  const rsrc_t r2 = mk_rsrc(w2 + (size_t)c2 * (D * 2), (F - c2) * (D * 2));
  const int b6 = b < 6 ? b : b - 6;
  const int dith = (((w - 4) * DITH) >> 3) + ((((blk >> 3) - 16) * DITG) >> 5);
  unsigned tst = PEND ? (unsigned)tclk + TOFF + dith : 0u;
  auto hook = [&](auto I) {
    constexpr int i = I;
    if constexpr (i == 32) PAD(11, 16);
    if constexpr (i == 35) PAD(14, R1);
    if constexpr (i == 38) PAD(15, R1);
    if constexpr (i == 42) { NEW_PAGE(); PAD(20, R1); }
    if constexpr (i == 49) PAD(16, R1);
    if constexpr (i == 56) PAD(36, 35);
    if constexpr (i == 61) { NEW_PAGE(); PAD(30, 36); }
    if constexpr (PEND && i >= PFROM && i <= PTO && i % PEV == 0) {
      PINS(tst);
      const int pend = (i <= pq ? PBUR : 0) + (i <= po ? PBUR : 0) + (i <= pm ? PBUM : 0);
      constexpr int extra = (i <= OB0 - OWD ? 2 : 0) + (i <= MB0 - OWD ? 2 : 0);
      const unsigned tgt = tst + PEND - (67 - i + extra) * PTAU - pend;
      int tmp;
      asm volatile("s_memrealtime vcc\n\ts_waitcnt lgkmcnt(0)\n\ts_sub_i32 %0, %1, vcc_lo\n\ts_ashr_i32 %0, %0, %2\n\ts_cmp_lt_i32 %0, 3\n\ts_cbranch_scc1 PE%=\n\t"
                   "s_add_i32 %0, %0, vcc_lo\n"
                   "PC%=:\n\ts_memrealtime vcc\n\ts_waitcnt lgkmcnt(0)\n\ts_sub_i32 vcc_lo, vcc_lo, %0\n\ts_cmp_lt_i32 vcc_lo, 0\n\ts_cbranch_scc1 PC%=\nPE%=:"
                   : "=&s"(tmp) : "s"(tgt), "i"(PSH) : "scc", "vcc", "memory");
      SB();
    }
    constexpr auto site = [](int i_, int p0, int sp) { for (int g = 0; g < NG; ++g) if (p0 + ((g * sp) >> 2) == i_) return true; return false; };
    if constexpr (site(i - 4, QB0, QBS)) cond_barrier<i - 4>(pq);
    if constexpr (site(i - QRDY, QB0, QBS)) cond_barrier<i - QRDY>(pq);
    if constexpr (site(i - 4, OB0, OBS)) cond_barrier<i - 4>(po);
    if constexpr (site(i, MB0, MBS)) cond_barrier<i>(pm);
    if constexpr (QOWN > 2 && i == QOWN) { PACE(); dma1<QNT>(rq, vq, 2 * b * (4 * NH * DH * 2), ldsw + 2 * b * 1024); PACE(); dma1<QNT>(rq, vq, (2 * b + 1) * (4 * NH * DH * 2), ldsw + (2 * b + 1) * 1024); }
    if constexpr (site(i, QB0, QBS)) burst<i, 16, 14>(pq, rq, vq, 4 * NH * DH * 2, ldsw, (2 * b + 2) & 15);
    if constexpr (i == OB0 - OWD) { PACE(); dma1(ro, vo, 2 * b * 8192, ldsd); PACE(); dma1(ro, vo, (2 * b + 1) * 8192, ldsd); }
    if constexpr (site(i, OB0, OBS)) burst<i, 16>(po, ro, vo, 8 * D * 2, ldsw, 2 * b);
    if constexpr (i == MB0 - OWD) { PACE(); dma1(r1, v1, b * (8 * F * 2), ldsd); PACE(); dma1(r2, vo, b * (8 * D * 2), ldsd); }
    if constexpr (site(i, MB0, MBS)) {
      burst<i, 8>(pm, r1, v1, 8 * F * 2, ldsw, b);
      burst<i, 6>(pm, r2, vo, 8 * D * 2, ldsw + 8192, b6);
    }
  };
  static_assert(QRDY > 4 && QB0 + (NG - 1) * QBS / 4 + QRDY < QSTEP && OBS >= QBS && OB0 >= QB0 + QRDY && MB0 > OB0 + 4 &&
                MB0 + (NG - 1) * MBS / 4 > OB0 + (NG - 1) * OBS / 4 + 5 && MB0 + (NG - 1) * MBS / 4 <= CHKS && CHKS >= 52 && CHKS <= 63, "barrier order, staging area reuse");
  unsigned tx;
  constexpr int S0 = QOWN > 2 ? 4 : 2;
  sfor<QSTEP - S0>([&](auto I) { constexpr int i = I + S0; if constexpr (i == 11) ESTAMP(7); if constexpr (i == 9) ESTAMP(8); if constexpr (i == 3) { SB(); xt[1] = (unsigned)__builtin_amdgcn_s_memrealtime(); SB(); } hook(IC(i));
    PACEN(WN + (TOUCH && i < 6 ? 1 : 0) + (TCOM && ((i > TSTEP && i < TSTEP + 4) || (i > MSTEP && i < MSTEP + 4)) ? 1 : 0)); ldk(IC(i));
    if constexpr (TCOM && i == MSTEP) {
      __builtin_amdgcn_raw_buffer_store_b32((unsigned)__builtin_amdgcn_s_memrealtime(), mk_rsrc(ws + WS_T, NREP * 4),
                                            (tid == 64 * MWAVE && (blk & 0xd0) == 0x80) ? (((blk >> 1) & 16) | (blk & 15)) * 4 : OOB, 0, AUX_SC1);
      SB();
    }
    if constexpr (TCOM && i == TSTEP) ald32sc(tx, mk_rsrc(ws + WS_T, NREP * 4), (lane & 31) * 4);
    if constexpr (TCOM && i == TSTEP + 4) {
      PIN(tx);
      const int dt = (int)(tx - (unsigned)tclk) - TOFF;
      const bool ok = dt > -400 && dt < 400;
      float sm = ok ? (lane < 32 ? (float)dt : 1.f) : 0.f;
      sm = row_sum(tsum16(sm, sm));
      const float cnt = asf(__builtin_amdgcn_readlane(fbits(sm), 32));
      int adj = (int)(sm * __builtin_amdgcn_rcpf(fmaxf(cnt, 1.f)));
      PIN(adj);
      tst += __builtin_amdgcn_readfirstlane(adj);
      SB();
    }
  });
  PIN(tch);
  PIN(wk);
  NEW_PAGE(); PAD(10, 15);
  SB(); xt[2] = DV == 5 ? (unsigned)tclk : (unsigned)__builtin_amdgcn_s_memrealtime(); SB();
  unsigned qp[4];
  {
    const f32x4 s4 = *(const LAS f32x4*)(L + L_RED + lane * 16) + *(const LAS f32x4*)(L + L_RED + 1024 + lane * 16);
    rows_bcast(cvtpk(s4[0] + s4[2], s4[1] + s4[3]), qp);
  }
  SB();
  STAMP(2);
  float sc[2], xs[16];
  auto qk = [&](u32x4& kv) { PIN(kv); return dot2(kv[3], qp[3], dot2(kv[2], qp[2], dot2(kv[1], qp[1], dot2(kv[0], qp[0], 0.f)))); };
  static_assert(QSTEP <= 28, "");
  sfor<36 - QSTEP>([&](auto I) {
    constexpr int i = I + QSTEP;
    hook(IC(i));
    PACE();
    if constexpr (i < 32) ldk(IC(i)); else ldv(IC(i - 32));
    if constexpr (I < 3) sfor<(I < 2 ? 6 : 4)>([&](auto J) { xs[6 * I + J] = qk(K[6 * I + J]); PIN(xs[6 * I + J]); });
    if constexpr (I == 3) { sc[0] = transpose_reduce16(xs, c); PIN(sc[0]); }
    if constexpr (I >= 4) {
      constexpr int lo = (I == 4) ? 16 : (16 + 6 * (I - 4) < i - 4 ? 16 + 6 * (I - 4) : i - 4);
      constexpr int hi = (16 + 6 * (I - 3) < i - 3) ? 16 + 6 * (I - 3) : i - 3;
      sfor<hi - lo>([&](auto J) { xs[lo - 16 + J] = qk(K[lo + J]); PIN(xs[lo - 16 + J]); });
    }
    SB();
  });
  sc[1] = transpose_reduce16(xs, c);
  SB();
  hook(IC(36)); PACE(); ldv(IC(4));
  unsigned pp[2];
  float mw, e1;
  {
    const float s0 = rbf(rbf(sc[0]) * 0.08838834764831845f), s1 = rbf(rbf(sc[1]) * 0.08838834764831845f);
    mw = wave_max(fmaxf(s0, s1));
    const float e0 = __builtin_amdgcn_exp2f((s0 - mw) * 1.4426950408889634f);
    e1 = __builtin_amdgcn_exp2f((s1 - mw) * 1.4426950408889634f);
    pp[0] = cvtpk(e0, e0); pp[1] = cvtpk(e1, e1);
    SB();
    hook(IC(37)); PACE(); ldv(IC(5));
    *(LAS f32x2*)(L + L_ML + w * 8) = f32x2{mw, wave_sum(e0 + e1)};
  }
  SB();
  STAMP(3);
  f32x4 acc = {0.f, 0.f, 0.f, 0.f};
  u32x4 sel;
#pragma unroll
  for (int k = 0; k < 4; ++k) sel[k] = ((c & 7) >> 1) == k ? ((c & 1) ? 0xffff0000u : 0xffffu) : 0u;
  auto pv = [&](auto I) { PIN(V[I]); const unsigned pb = dppu<BCAST + (I & 15)>(pp[I >> 4]); acc = mfma(V[I], sel & pb, acc); PIN(acc); };
  const int ocol = 64 * (h >> 2) + 16 * r + 4 * (h & 3) + (c & 3);
  unsigned b2v, xo;
  int lw2 = (w >> 1) * 1024 + lane * 16 + (w & 1) * 8;
  u32x4 blast;
  sfor<29>([&](auto I) {
    constexpr int i = I + 6;
    hook(IC(i + 32));
    if constexpr (i < 32) { PACE(); ldv(IC(i)); }
    else PACEN(35 - i);
    if constexpr (i == 24) { ald16(b2v, mk_rsrc(b2, D * 2), w == 0 ? ocol * 2 : OOB, 0); ald16(xo, mk_rsrc(x, NB * D * 2), w == 0 ? ocol * 2 : OOB, b * (D * 2)); PIN(lw2); }
    if constexpr (OTOUCH && i == 8) { __builtin_amdgcn_raw_buffer_store_b16((unsigned short)0, mk_rsrc(out, NB * D * 2), (w == 0 && c < 4) ? (b * D + ocol) * 2 : OOB, 0, 0); SB(); }
    if constexpr (i == 6) { pv(IC(0)); pv(IC(1)); }
    if constexpr (i + 32 == CHKS) {
      BARRIER();
      const f32x2 ml = *(const LAS f32x2*)(L + L_ML + (lane & 7) * 8);
      float M = ml[0];
      M = fmaxf(M, dppf<XOR1>(M)); M = fmaxf(M, dppf<XOR2>(M)); M = fmaxf(M, dppf<HMIR>(M));
      float Ls = ml[1] * __builtin_amdgcn_exp2f((ml[0] - M) * 1.4426950408889634f);
      Ls += dppf<XOR1>(Ls); Ls += dppf<XOR2>(Ls); Ls += dppf<HMIR>(Ls);
      const float scale = __builtin_amdgcn_exp2f((mw - M) * 1.4426950408889634f) / Ls;
      acc *= scale;
      pp[1] = cvtpk(e1 * scale, e1 * scale);
    }
    pv(IC(i - 4));
    SB();
  });
  blast = sel & dppu<BCAST + 15>(pp[1]);
  PIN(blast);
  PACEN(0);
  STAMP(4); TSTAMP(1);
  PIN(V[31]);
  acc = mfma(V[31], blast, acc);
  *(LAS f32x2*)(L + L_RED2 + lw2) = (c & 8) ? f32x2{acc[2], acc[3]} : f32x2{acc[0], acc[1]};
  BARRIER();
  SB(); xt[3] = (unsigned)__builtin_amdgcn_s_memrealtime(); SB();
  PIN(b2v); PIN(xo);
  if (w == 0) {
    PAD(32, R2);
    const rsrc_t rws = mk_rsrc(ws, WS_BYTES);
    int vin = blk * IN_BYTES + lane * 16;
    auto init_p2 = [&]() {
      u32x4 sent4 = {SENT, SENT, SENT, SENT};
      PIN(sent4);
      __builtin_amdgcn_raw_buffer_store_b128(sent4, rws, vin, IN_P2, AUX_SC1);
      __builtin_amdgcn_raw_buffer_store_b128(sent4, rws, vin, IN_P2 + 1024, AUX_SC1);
      asm volatile("s_nop 1" :: "v"(sent4) : "memory");
    };
    if (SENTP == 1) init_p2();
    int vhs = HST == 0 ? (b * 32 + c) * IN_BYTES + IN_H + h * 32 + r * 8 : (b * 32 + (lane >> 1)) * IN_BYTES + IN_H + h * 32 + (lane & 1) * 16;
    int vout = c < 4 ? (b * D + ocol) * 2 : OOB;
    b2v <<= 16; xo <<= 16;
    PIN(vin); PIN(vhs); PIN(vout); PIN(b2v); PIN(xo);
    if (SENTP >= 2) { __builtin_amdgcn_s_sleep(SENTP); init_p2(); __builtin_amdgcn_s_sleep(DLY0 - SENTP); }
    else if (DLY0) __builtin_amdgcn_s_sleep(DLY0);
    if (PRIO) __builtin_amdgcn_s_setprio(3);
    float g1 = gather16(rws, vin + IN_P1);
    if (TAILST && TAILST != 3) { PIN(g1); TSTAMP(4); }
    PACEN(0);
    const float hf = asf(xo) + rbf(g1);
    const unsigned hq = cvtpk(hf, dppf<XOR1>(hf));
    unsigned hb[4];
    rows_bcast(dppu<0x88>(hq), hb);
    __builtin_amdgcn_raw_buffer_store_b128(u32x4{hb[0], hb[1], hb[2], hb[3]}, rws, vhs, 0, AUX_SC1);
    SB();
    STAMP(7); if (TAILST != 3) TSTAMP(5);
    BARRIER();
    PAD(33, R2);
    float hres = asf(hq << 16);
    PIN(hres);
    if (DLY1) __builtin_amdgcn_s_sleep(DLY1);
    int lst = L_ST;
    PIN(lst);
#pragma unroll 1
    for (unsigned spins = 0;; ++spins) {
      asm volatile("" ::: "memory");
      const u32x4 hr = __builtin_amdgcn_raw_buffer_load_b128(rws, vin + IN_H, 0, AUX_SC1);
      float s1 = 0.f, s2 = 0.f;
      PIN(s1); PIN(s2);
#pragma unroll
      for (int k = 0; k < 4; ++k) { s1 = dot2(hr[k], 0x3b003b00u, s1); s2 = dot2(hr[k], hr[k], s2); }
      float t = tsum32(s1, s2);
      t = row_sum(tsum16(t, t));
      const auto ms = __builtin_amdgcn_permlane32_swap(fbits(t), fbits(t), false, false);
      const float mean = asf(ms[0]);
      const float var = fmaxf(fmaf(asf(ms[1]), 1.f / D, -(mean * mean)), 0.f);
      const float den = rbf(__builtin_amdgcn_sqrtf(rbf(rbf(var) + 1e-5f)));
      *(LAS f32x2*)(L + lst) = f32x2{rbf(mean), __builtin_amdgcn_rcpf(den)};
      if (__builtin_expect(!any_nan(t), 1)) break;
      if (!any_sent(max4(hr)) || spins >= SPIN_CAP) break;
      if ((spins & 63u) == 63u) {
        const int vr = (b * 32 + (lane >> 2)) * IN_BYTES + IN_P1 + h * 64 + (lane & 3) * 16;
        __builtin_amdgcn_raw_buffer_store_b128(*(const LAS u32x4*)(L + L_Y + lane * 16), rws, vr, 0, AUX_SC1);
        __builtin_amdgcn_raw_buffer_store_b128(*(const LAS u32x4*)(L + L_Y + 1024 + lane * 16), rws, vr, 16 * IN_BYTES, AUX_SC1);
      }
    }
    if (TAILST == 1) TSTAMP(9);
    BARRIER();
    if (DLY2) __builtin_amdgcn_s_sleep(DLY2);
    const float res = hres + rbf(rbf(gather16(rws, vin + IN_P2)) + asf(b2v));
    __builtin_amdgcn_raw_buffer_store_b16(__builtin_bit_cast(unsigned short, (__bf16)res), mk_rsrc(out, NB * D * 2), vout, 0, 0);
    { SB(); xt[4] = (unsigned)__builtin_amdgcn_s_memrealtime();
      __builtin_amdgcn_s_sleep(100);
      const rsrc_t rd = mk_rsrc(ws + (8 << 20), 8192);
      for (int k = 0; k < 5; ++k) __builtin_amdgcn_raw_buffer_store_b32(xt[k], rd, lane == 0 ? (k * 256 + blk) * 4 : OOB, 0, AUX_SC1);
      if (blk == 0) {
        while ((int)((unsigned)__builtin_amdgcn_s_memrealtime() - (xt[0] + 4000u)) < 0) {}
        float mn[5], mx[5], av[5], rv[5], rx[5];
        u32x4 s0 = __builtin_amdgcn_raw_buffer_load_b128(rd, lane * 16, 0, AUX_SC1);
        for (int k = 0; k < 5; ++k) {
          const u32x4 v = __builtin_amdgcn_raw_buffer_load_b128(rd, lane * 16, k * 1024, AUX_SC1);
          float a = 1e9f, c2_ = -1e9f, sm = 0.f, sr = 0.f, mr = -1e9f;
          for (int j = 0; j < 4; ++j) { const float f = (float)(int)(v[j] - xt[0]) * 0.01f, g = (float)(int)(v[j] - s0[j]) * 0.01f; a = fminf(a, f); c2_ = fmaxf(c2_, f); sm += f; sr += g; mr = fmaxf(mr, g); }
          mn[k] = -wave_max(-a); mx[k] = wave_max(c2_); av[k] = wave_sum(sm) * (1.f / 256); rv[k] = wave_sum(sr) * (1.f / 256); rx[k] = wave_max(mr);
        }
        auto q = [](float v, float base, float unit) { return (int)fminf(fmaxf((v - base) / unit + 0.5f, 0.f), 15.f); };
        const int f1 = q(mx[4] - mn[0], 22.6f, 0.2f);
        const int f2 = DV == 5 ? q(rv[2], 0.2f, 0.1f) : DV == 1 ? q(mx[0] - mn[0], 0.f, 0.15f) : DV == 2 ? q(rv[1], 0.5f, 0.1f) : DV == 3 ? q(rv[2], 8.5f, 0.15f) : q(mx[3] - av[3], 0.f, 0.15f);
        const int f3 = DV == 5 ? q(rv[1] - rv[2], 0.3f, 0.1f) : DV == 1 ? q(av[0] - mn[0], 0.f, 0.1f) : DV == 2 ? q(rx[1], 0.5f, 0.2f) : DV == 3 ? q(rv[3], 19.0f, 0.15f) : q(mx[4] - mx[3], 2.2f, 0.1f);
        const unsigned tgt = xt[0] + 5000u + 200u * (unsigned)__builtin_amdgcn_readfirstlane(f1 * 256 + f2 * 16 + f3);
        while ((int)((unsigned)__builtin_amdgcn_s_memrealtime() - tgt) < 0) {}
      }
    }
  } else if (w >= 4) {
    PAD(31, R2);
    const int u = w - 4, q4 = (lane >> 2) & 3;
    const rsrc_t rws = mk_rsrc(ws, WS_BYTES);
    __builtin_amdgcn_s_sleep(UD1); SB();
    u32x8 W1[6];
    LAS unsigned char* p1 = L + L_W + u * 32768 + (lane & 3) * 8 + (4 * (r >> 1) + 32 * (r & 1) + q4) * 128;
    sfor<6>([&](auto J) { W1[J] = tr_read8<16 * 128, 8 * 128>(p1 + (J & 1) * 16384 + (J >> 1) * 32); });
    sfor<6>([&](auto J) { PIN(W1[J]); });
    SB(); __builtin_amdgcn_s_sleep(UD2); SB();
    int vhl = blk * IN_BYTES + IN_H + u * 256 + r * 32 + (lane & 1) * 16, lwr = lane * 16 + u * 4;
    int idx = (lane & 1) ? 0xEEEEEEEE : 0x44444444;
    const f32x4 zero4 = {0.f, 0.f, 0.f, 0.f};
    f32x4 e[3];
    auto project = [&](u32x4 h0, u32x4 h1) {
      sfor<3>([&](auto T) { e[T] = smf(h0, W1[2 * T], e[T], idx); SB(); });
      sfor<3>([&](auto T) { e[T] = smf(h1, W1[2 * T + 1], e[T], idx); SB(); });
      float a0 = e[0][0] + e[0][1], a1 = e[1][0] + e[1][1];
      a0 = r == 0 ? a0 : a1;
      PIN(a0);
      const float a2 = e[2][0] + e[2][1];
      KEEP(e[0]); KEEP(e[1]); KEEP(e[2]);
      return r >= 2 ? a2 : a0;
    };
    const u32x4 ones = {0x3f803f80u, 0x3f803f80u, 0x3f803f80u, 0x3f803f80u};
    sfor<3>([&](auto T) { e[T] = zero4; });
    *(LAS float*)(L + L_EX + lwr) = project(ones, ones);
    sfor<3>([&](auto T) { e[T] = zero4; PIN(e[T]); });
    PIN(vhl); PIN(lwr); PIN(idx);
    STAMP(6);
    BARRIER();
    if (DLY1) __builtin_amdgcn_s_sleep(DLY1);
#pragma unroll 1
    for (unsigned spins = 0;; ++spins) {
      asm volatile("" ::: "memory");
      const u32x4 h0 = __builtin_amdgcn_raw_buffer_load_b128(rws, vhl, 0, AUX_SC1);
      const u32x4 h1 = __builtin_amdgcn_raw_buffer_load_b128(rws, vhl + 128, 0, AUX_SC1);
      const float a = project(h0, h1);
      *(LAS float*)(L + L_EX2 + lwr) = a;
      if (__builtin_expect(!any_nan(a), 1)) break;
      if (!any_sent(max(max4(h0), max4(h1))) || spins >= SPIN_CAP) break;
      sfor<3>([&](auto T) { e[T] = zero4; PIN(e[T]); });
    }
    if (TAILST == 1) TSTAMP(8);
    BARRIER();
  }
  }
}

extern "C" void kernel_launch(void* const* d_in, const int* in_sizes, int n_in, void* d_out, int out_size, void* d_ws,
                              size_t ws_size, hipStream_t stream) {
  fused_block<<<NB * NH, 768, 0, stream>>>((const unsigned char*)d_in[3], (const unsigned char*)d_in[1], (const unsigned char*)d_in[4],
                                      (const unsigned char*)d_in[2], (const unsigned short*)d_in[0], (const unsigned char*)d_in[6],
                                      (const unsigned char*)d_in[7], (const unsigned char*)d_in[8], (const unsigned short*)d_in[9],
                                      (unsigned short*)d_out, (unsigned char*)d_ws);
}
```

```cpp
#include <hip/hip_runtime.h>
#include <hip/hip_bf16.h>
#include <stdint.h>

#define STAMPS 0
#define PFCODE 0
#define WN 3
#define BWN 3
#define QB0 4
#define QRDY 9
#define QSTEP 28
#define OB0 14
#define MB0 35
#define QBS 8
#define OBS 12
#define MBS 12
#define NG 8
#define OWD 5
#define INB 8192
#define SENTH 0
#define SENTP 1
#define CHKS 61
#define PEND 1850
#define PTAU 24
#define PBUR 90
#define PBUM 110
#define DITH 32
#define PSH 1
#define DITG 0
#define TSTEP 14
#define PRIO 0
#define UD1 10
#define UD2 1
#define W2B 1
#define QOWN 2
#define HDLY 0
#define SDLY 0
#define QNT false
#define PADS 1
#define MSTEP 6
#define MWAVE 3
#define NREP 32
#define TOFF 215
#define OTOUCH 1
#define COLD 0
#define TFRAC 4
#define TCOM 1
#define SKEW 0
#define PFROM 10
#define PTO 60
#define PEV 2
#define SENTE 1
#define LOSE -1
#define EARLYST 0
#define TOUCH 0
#define SENTW 0
#define TAILST 0
#define HST 1
#define EXPM 0
#define DLY0 17
#define DLY1 11
#define DLY2 18

typedef unsigned u32x4 __attribute__((ext_vector_type(4)));
typedef unsigned u32x2 __attribute__((ext_vector_type(2)));
typedef float f32x2 __attribute__((ext_vector_type(2)));
typedef float f32x4 __attribute__((ext_vector_type(4)));
typedef __bf16 bf16x2 __attribute__((ext_vector_type(2)));
typedef short s16x4 __attribute__((ext_vector_type(4)));
typedef unsigned u32x8 __attribute__((ext_vector_type(8)));
typedef __bf16 bf16x8 __attribute__((ext_vector_type(8)));
typedef __bf16 bf16x16 __attribute__((ext_vector_type(16)));
typedef __amdgpu_buffer_rsrc_t rsrc_t;
#define LAS __attribute__((address_space(3)))
#define DI __device__ __forceinline__

constexpr int NB = 8, D = 512, NH = 32, SK = 4096, DH = 128, WIN = 1024, F = 1376;
constexpr int FS = 48;
constexpr int AUX_NT = 2, AUX_SC1 = 16;
constexpr int OOB = 0x7ffffff0;
constexpr unsigned SENT = 0xffffffffu;
constexpr unsigned SPIN_CAP = 1u << 17;
constexpr int IN_P1 = 0;
constexpr int IN_P2 = 3072;
constexpr int IN_H = 2048;
constexpr int IN_BYTES = INB;
constexpr int WS_T = 256 * IN_BYTES;
constexpr int WS_BYTES = WS_T + 128;
constexpr int WS_STAMP = 4 * 1024 * 1024;
constexpr int L_RED = 0;
constexpr int L_RED2 = L_RED + 2048;
constexpr int L_EX = L_RED2 + 4096;
constexpr int L_EX2 = L_EX + 1024;
constexpr int L_ML = L_EX2 + 1024;
constexpr int L_ST = L_ML + 64;
constexpr int L_T = L_ST + 16;
constexpr int L_Y = 9216;
constexpr int L_DUMP = L_Y + 2048;
constexpr int L_W = L_DUMP + 1024;
constexpr int L_BYTES = L_W + 128 * 1024;

DI float dot2(unsigned a, unsigned b, float acc) {
  return __builtin_amdgcn_fdot2_f32_bf16(__builtin_bit_cast(bf16x2, a), __builtin_bit_cast(bf16x2, b), acc, false);
}
DI unsigned fbits(float f) { return __builtin_bit_cast(unsigned, f); }
DI float asf(unsigned u) { return __builtin_bit_cast(float, u); }
DI unsigned cvtpk(float lo, float hi) {
  bf16x2 r = {(__bf16)lo, (__bf16)hi};
  return __builtin_bit_cast(unsigned, r);
}
DI float rbf(float f) { return asf((unsigned)__builtin_bit_cast(unsigned short, (__bf16)f) << 16); }
template <int CTRL> DI unsigned dppu(unsigned x) { return __builtin_amdgcn_mov_dpp(x, CTRL, 0xf, 0xf, true); }
template <int CTRL> DI float dppf(float x) { return asf(dppu<CTRL>(fbits(x))); }
constexpr int XOR1 = 0xB1, XOR2 = 0x4E, HMIR = 0x141, ROR4 = 0x124, ROR8 = 0x128, BCAST = 0x150;
DI float tsum32(float a, float b) {
  auto s = __builtin_amdgcn_permlane32_swap(fbits(a), fbits(b), false, false);
  return asf(s[0]) + asf(s[1]);
}
DI float tsum16(float a, float b) {
  auto s = __builtin_amdgcn_permlane16_swap(fbits(a), fbits(b), false, false);
  return asf(s[0]) + asf(s[1]);
}
DI float row_sum(float x) { x += dppf<XOR1>(x); x += dppf<XOR2>(x); x += dppf<ROR4>(x); x += dppf<ROR8>(x); return x; }
DI float row_max(float x) { x = fmaxf(x, dppf<XOR1>(x)); x = fmaxf(x, dppf<XOR2>(x)); x = fmaxf(x, dppf<ROR4>(x)); x = fmaxf(x, dppf<ROR8>(x)); return x; }
DI float wave_sum(float x) { x = row_sum(x); x = tsum16(x, x); return tsum32(x, x); }
DI float wave_max(float x) {
  x = row_max(x);
  auto s = __builtin_amdgcn_permlane16_swap(fbits(x), fbits(x), false, false);
  x = fmaxf(asf(s[0]), asf(s[1]));
  auto t = __builtin_amdgcn_permlane32_swap(fbits(x), fbits(x), false, false);
  return fmaxf(asf(t[0]), asf(t[1]));
}
DI void rows_bcast(unsigned x, unsigned (&o)[4]) {
  auto s = __builtin_amdgcn_permlane16_swap(x, x, false, false);
  auto a = __builtin_amdgcn_permlane32_swap(s[0], s[0], false, false);
  auto b = __builtin_amdgcn_permlane32_swap(s[1], s[1], false, false);
  o[0] = a[0]; o[2] = a[1]; o[1] = b[0]; o[3] = b[1];
}
DI f32x4 smf(u32x4 a, u32x8 b, f32x4 c, int idx) {
  return __builtin_amdgcn_smfmac_f32_16x16x64_bf16(__builtin_bit_cast(bf16x8, a), __builtin_bit_cast(bf16x16, b), c, idx, 0, 0);
}
DI f32x4 mfma(u32x4 a, u32x4 b, f32x4 c) {
  return __builtin_amdgcn_mfma_f32_16x16x32_bf16(__builtin_bit_cast(bf16x8, a), __builtin_bit_cast(bf16x8, b), c, 0, 0, 0);
}
constexpr int SHL = 0x100;
template <class T> DI void pin_(T& x) { asm volatile("" : "+v"(x)); }
#define PIN(x) pin_(x)
template <class T> DI void keep_(const T& x) { asm volatile("" :: "v"(x)); }
#define KEEP(x) keep_(x)
template <class T> DI void pins_(T& x) { asm volatile("" : "+s"(x)); }
#define PINS(x) pins_(x)
#define SB() __builtin_amdgcn_sched_barrier(0)
#define NEW_PAGE() do { if (PADS) { SB(); asm volatile("s_branch 1f\n\t.p2align 12\n1:"); SB(); } } while (0)
#define PAD(id, next) do { if (PADS) { SB(); asm volatile("PAD" #id ":\n\ts_cbranch_execz PAD" #next); SB(); } } while (0)
#define VISIT(u, p0, p1, p2, p3, ret) do { if (PADS) { SB(); asm volatile( \
    "s_mov_b64 exec, 0\n\ts_cmp_eq_u32 %0, 0\n\ts_cbranch_scc1 PAD" #p0 "\n\ts_cmp_eq_u32 %0, 1\n\ts_cbranch_scc1 PAD" #p1 "\n\t" \
    "s_cmp_eq_u32 %0, 2\n\ts_cbranch_scc1 PAD" #p2 "\n\ts_branch PAD" #p3 "\nPAD" #ret ":\n\ts_mov_b64 exec, -1" :: "s"(u) : "scc"); SB(); } } while (0)
#define PACEN(n) do { asm volatile("s_waitcnt vmcnt(%0)" :: "i"(n) : "memory"); SB(); } while (0)
#define PACE() PACEN(WN)
#define BARRIER() do { asm volatile("s_waitcnt lgkmcnt(0)\n\ts_barrier" ::: "memory"); SB(); } while (0)
DI rsrc_t mk_rsrc(const void* p, int bytes) { return __builtin_amdgcn_make_buffer_rsrc((void*)p, 0, bytes, 0x00020000); }

template <int G, int N, int CNT = N> DI void burst(int grp, rsrc_t r, int voff, int stride, int lds, int j0) {
  int keep, j, cnt, t;
  asm volatile(
      "s_cmp_lg_u32 %4, %10\n\t"
      "s_cbranch_scc1 BE%=\n\t"
      "s_mov_b32 %0, m0\n\t"
      "s_mov_b32 %1, %9\n\t"
      "s_mov_b32 %2, %12\n"
      "BL%=:\n\t"
      "s_lshl_b32 %3, %1, 10\n\t"
      "s_add_u32 m0, %3, %8\n\t"
      "s_mul_i32 %3, %1, %7\n\t"
      "s_add_u32 %1, %1, 1\n\t"
      "s_waitcnt vmcnt(%13)\n\t"
      "buffer_load_dwordx4 %6, %5, %3 offen lds\n\t"
      "s_cmp_eq_u32 %1, %11\n\t"
      "s_cselect_b32 %1, 0, %1\n\t"
      "s_sub_u32 %2, %2, 1\n\t"
      "s_cmp_lg_u32 %2, 0\n\t"
      "s_cbranch_scc1 BL%=\n\t"
      "s_mov_b32 m0, %0\n"
      "BE%=:"
      : "=&s"(keep), "=&s"(j), "=&s"(cnt), "=&s"(t)
      : "s"(grp), "s"(r), "v"(voff), "s"(stride), "s"(lds), "s"(j0), "i"(G), "i"(N), "i"(CNT), "i"(BWN)
      : "memory", "scc");
  SB();
}
DI void ald128nt(u32x4& d, rsrc_t r, int voff, int soff) { asm volatile("buffer_load_dwordx4 %0, %1, %2, %3 offen nt" : "=v"(d) : "v"(voff), "s"(r), "s"(soff) : "memory"); SB(); }
DI void ald128(u32x4& d, rsrc_t r, int voff, int soff) { asm volatile("buffer_load_dwordx4 %0, %1, %2, %3 offen" : "=v"(d) : "v"(voff), "s"(r), "s"(soff) : "memory"); SB(); }
DI void ald64sc(u32x2& d, rsrc_t r, int voff, int soff) { asm volatile("buffer_load_dwordx2 %0, %1, %2, %3 offen sc1" : "=v"(d) : "v"(voff), "s"(r), "s"(soff) : "memory"); SB(); }
DI void ald32sc(unsigned& d, rsrc_t r, int voff) { asm volatile("buffer_load_dword %0, %1, %2, 0 offen sc1" : "=v"(d) : "v"(voff), "s"(r) : "memory"); SB(); }
DI void ald32(unsigned& d, rsrc_t r, int voff) { asm volatile("buffer_load_dword %0, %1, %2, 0 offen" : "=v"(d) : "v"(voff), "s"(r) : "memory"); SB(); }
DI void ald16(unsigned& d, rsrc_t r, int voff, int soff) { asm volatile("buffer_load_ushort %0, %1, %2, %3 offen" : "=v"(d) : "v"(voff), "s"(r), "s"(soff) : "memory"); SB(); }
template <bool NT = false> DI void dma1(rsrc_t r, int voff, int soff, int lds) {
  int keep;
  if constexpr (NT)
    asm volatile("s_mov_b32 %0, m0\n\ts_mov_b32 m0, %4\n\ts_nop 0\n\tbuffer_load_dwordx4 %1, %2, %3 offen nt lds\n\ts_mov_b32 m0, %0"
                 : "=&s"(keep) : "v"(voff), "s"(r), "s"(soff), "s"(lds) : "memory");
  else
    asm volatile("s_mov_b32 %0, m0\n\ts_mov_b32 m0, %4\n\ts_nop 0\n\tbuffer_load_dwordx4 %1, %2, %3 offen lds\n\ts_mov_b32 m0, %0"
                 : "=&s"(keep) : "v"(voff), "s"(r), "s"(soff), "s"(lds) : "memory");
  SB();
}
template <int G> DI void cond_barrier(int grp) {
  asm volatile("s_cmp_lg_u32 %0, %1\n\ts_cbranch_scc1 CB%=\n\ts_barrier\nCB%=:" :: "s"(grp), "i"(G) : "memory", "scc");
  SB();
}
DI float gather16(rsrc_t r, int voff);
DI u32x2 tr_read(LAS unsigned char* p) {
  return __builtin_bit_cast(u32x2, __builtin_amdgcn_ds_read_tr16_b64_v4i16((LAS s16x4*)p));
}
template <int M1, int M2> DI u32x8 tr_read8(LAS unsigned char* p) {
  const u32x2 a = tr_read(p), b = tr_read(p + M1), c = tr_read(p + M2), d = tr_read(p + M1 + M2);
  return u32x8{a[0], a[1], b[0], b[1], c[0], c[1], d[0], d[1]};
}
DI f32x2 fold_rows8(const float (&acc)[8]) {
  const float t0 = tsum32(acc[0], acc[4]), t1 = tsum32(acc[1], acc[5]), t2 = tsum32(acc[2], acc[6]), t3 = tsum32(acc[3], acc[7]);
  return f32x2{tsum16(t0, t2), tsum16(t1, t3)};
}
DI float fold_rows4(float a0, float a1, float a2, float a3) { return tsum16(tsum32(a0, a2), tsum32(a1, a3)); }
DI unsigned max4(u32x4 v) { return max(max(v[0], v[1]), max(v[2], v[3])); }

DI bool any_nan(float t) { return __builtin_amdgcn_ballot_w64(t != t) != 0; }
DI bool any_sent(unsigned m) { return __builtin_amdgcn_ballot_w64(m == SENT) != 0; }
DI float gather16(rsrc_t r, int voff) {
  u32x4 A, Bv;
  auto fetch = [&]() {
    asm volatile("" ::: "memory");
    A = __builtin_amdgcn_raw_buffer_load_b128(r, voff, 0, AUX_SC1);
    Bv = __builtin_amdgcn_raw_buffer_load_b128(r, voff + 1024, 0, AUX_SC1);
  };
  auto total = [&]() {
    const f32x4 s = __builtin_bit_cast(f32x4, A) + __builtin_bit_cast(f32x4, Bv);
    float t = fold_rows4(s[0], s[1], s[2], s[3]);
    t += dppf<ROR4>(t); t += dppf<ROR8>(t);
    return t;
  };
  fetch();
  float t = total();
  if (any_nan(t)) {
#pragma unroll 1
    for (unsigned spins = 0; spins < SPIN_CAP; ++spins) {
      fetch();
      if (!any_sent(max(max4(A), max4(Bv)))) break;
    }
    t = total();
  }
  return t;
}

template <int I> DI void pv_step(float (&acc)[8], unsigned plo, unsigned phi, u32x4 v) {
  const unsigned lo = dppu<BCAST + I>(plo), hi = dppu<BCAST + I>(phi);
#pragma unroll
  for (int k = 0; k < 4; ++k) { acc[2 * k] = dot2(lo, v[k], acc[2 * k]); acc[2 * k + 1] = dot2(hi, v[k], acc[2 * k + 1]); }
}
DI void pin8(float (&a)[8]) {
  asm volatile("" : "+v"(a[0]), "+v"(a[1]), "+v"(a[2]), "+v"(a[3]), "+v"(a[4]), "+v"(a[5]), "+v"(a[6]), "+v"(a[7]));
}
template <int... Is> struct iseq {};
template <int N, int... Is> struct mkseq : mkseq<N - 1, N - 1, Is...> {};
template <int... Is> struct mkseq<0, Is...> { typedef iseq<Is...> type; };
template <class Fn, int... Is> DI void sfor_(Fn&& f, iseq<Is...>) { (f(std::integral_constant<int, Is>{}), ...); }
template <int N, class Fn> DI void sfor(Fn&& f) { sfor_(f, typename mkseq<N>::type{}); }

DI float transpose_reduce16(float (&x)[16], int c) {
#define TR_STEP(HALF, CTRL, BIT) _Pragma("unroll") for (int h_ = 0; h_ < (HALF); ++h_) { \
    const float lo_ = x[h_], hi_ = x[h_ + (HALF)]; const float t1_ = lo_ + dppf<CTRL>(lo_), t2_ = hi_ + dppf<CTRL>(hi_); \
    x[h_] = (c & (BIT)) ? t2_ : t1_; }
  TR_STEP(8, ROR8, 8) TR_STEP(4, HMIR, 4) TR_STEP(2, XOR2, 2) TR_STEP(1, XOR1, 1)
#undef TR_STEP
  return x[0];
}

#define STAMP(i) do {} while (0)
#define ESTAMP(i) do {} while (0)
#define TSTAMP(i) do {} while (0)

extern "C" __global__ void __launch_bounds__(768, 3) __attribute__((aligned(4096)))
fused_block(const unsigned char* __restrict__ wq, const unsigned char* __restrict__ kc, const unsigned char* __restrict__ wo,
            const unsigned char* __restrict__ vc, const unsigned short* __restrict__ x, const unsigned char* __restrict__ w1,
            const unsigned char* __restrict__ b1, const unsigned char* __restrict__ w2, const unsigned short* __restrict__ b2,
            unsigned short* __restrict__ out, unsigned char* ws) {
  __shared__ __attribute__((aligned(16))) unsigned char smem[L_BYTES];
  LAS unsigned char* L = (LAS unsigned char*)smem;
  if (SKEW && (blockIdx.x & 7) == 5) __builtin_amdgcn_s_sleep(SKEW);
  STAMP(0);
  const int blk = blockIdx.x, b = blk >> 5, h = blk & 31;
  const int tid = threadIdx.x, lane = tid & 63, r = lane >> 4, c = lane & 15;
  const int w = __builtin_amdgcn_readfirstlane(tid >> 6);
  const int slice = (h & 7) * 4 + (h >> 3), col0 = slice * FS;
#define IC(n) std::integral_constant<int, (n)>{}
  if (LOSE >= 0 && (blk == LOSE || blk == LOSE + 5)) for (int i = 0; i < (blk == LOSE ? 12 : 30); ++i) __builtin_amdgcn_s_sleep(127);
  {
    int w2 = w;
    PINS(w2);
    if (w2 >= (SENTP ? 9 : 8)) {
      if (COLD || SDLY) { asm volatile("s_waitcnt lgkmcnt(0)"); __builtin_amdgcn_s_sleep(COLD + SDLY); }
      const rsrc_t r0 = mk_rsrc(ws, WS_BYTES);
      u32x4 s4 = {SENT, SENT, SENT, SENT};
      PIN(s4);
      if (!(EXPM & 1)) __builtin_amdgcn_raw_buffer_store_b128(s4, r0, lane * 16, blk * IN_BYTES + (w2 - (SENTP ? 9 : 8)) * 1024, AUX_SC1);
      if (!SENTP) __builtin_amdgcn_raw_buffer_store_b128(s4, r0, w2 == 8 ? lane * 16 : OOB, blk * IN_BYTES + 4096, AUX_SC1);
      SB(); KEEP(s4);
    }
    SB();
  }
  if (w >= 8) {
  const int u = w - 8;
  if (HDLY) { __builtin_amdgcn_s_sleep(HDLY); SB(); }
  const rsrc_t rws = mk_rsrc(ws, WS_BYTES);
  u32x4 xrow; unsigned xr0, xr1, b1r;
  {
    const rsrc_t rx = mk_rsrc(x + b * D, D * 2);
    ald128(xrow, rx, lane * 16, 0);
    ald16(xr0, rx, (4 * c + r) * 2, u * 256);
    ald16(xr1, rx, (4 * c + r) * 2, u * 256 + 128);
    ald16(b1r, mk_rsrc(b1, F * 2), lane < FS ? (col0 + lane) * 2 : OOB, 0);
  }
  if (PFCODE && (blk >> 3) == PFCODE) {
    unsigned long long pc; int o0, o1;
    asm volatile("s_getpc_b64 %0\nKPC0:\n\ts_mov_b32 %1, KPC0-KPCA\n\ts_mov_b32 %2, KPCB-KPCA" : "=s"(pc), "=s"(o0), "=s"(o1));
    const rsrc_t rc = mk_rsrc((const void*)(pc - (long long)o0), o1);
    unsigned p0 = __builtin_amdgcn_raw_buffer_load_b32(rc, lane * 128, u * 8192, 0);
    PIN(p0);
  }
  VISIT(u, 10, 11, 12, 13, R1);
  PACEN(0);
  PIN(xrow); PIN(xr0); PIN(xr1); PIN(b1r);
  const float xe0 = asf(xr0 << 16), xe1 = asf(xr1 << 16);
  float b1v = asf(b1r << 16);
  {
    float xf[8];
#pragma unroll
    for (int k = 0; k < 4; ++k) { xf[2 * k] = asf(xrow[k] << 16); xf[2 * k + 1] = asf(xrow[k] & 0xffff0000u); }
    const float mean = wave_sum(((xf[0] + xf[1]) + (xf[2] + xf[3])) + ((xf[4] + xf[5]) + (xf[6] + xf[7]))) * (1.f / D);
    float vs = 0.f;
#pragma unroll
    for (int k = 0; k < 8; ++k) { const float d = xf[k] - mean; vs = fmaf(d, d, vs); }
    const float var = wave_sum(vs) * (1.f / D);
    const float den = rbf(sqrtf(rbf(rbf(var) + 1e-5f))), mb = rbf(mean);
    const unsigned xp = cvtpk(rbf(xe0 - mb) / den, rbf(xe1 - mb) / den);
    unsigned xlo[2] = {xp & 0xffffu, xp >> 16}, xhi[2] = {xp << 16, xp & 0xffff0000u};
    PIN(xlo[0]); PIN(xlo[1]); PIN(xhi[0]); PIN(xhi[1]); PIN(b1v);
    ESTAMP(5);
    NEW_PAGE(); PAD(12, 14);
    BARRIER();
    ESTAMP(6);
    float acc[8] = {0.f, 0.f, 0.f, 0.f, 0.f, 0.f, 0.f, 0.f};
    sfor<32>([&](auto I) { pv_step<I & 15>(acc, xlo[I >> 4], xhi[I >> 4], *(const LAS u32x4*)(L + L_W + u * 32768 + I * 1024 + lane * 16)); if constexpr (I == 15) PAD(13, 20); });
    *(LAS f32x2*)(L + L_RED + (u >> 1) * 1024 + lane * 16 + (u & 1) * 8) = fold_rows8(acc);
  }
  STAMP(11);
  BARRIER();
  BARRIER();
  u32x8 WO[16];
  const int q4 = (lane >> 2) & 3;
  LAS unsigned char* lw = L + L_W + u * 32768 + (lane & 3) * 8;
  {
    LAS unsigned char* p = lw + (32 * (r >> 1) + 4 * (r & 1) + (q4 >> 1) + 8 * (q4 & 1)) * 128;
    sfor<16>([&](auto J) { WO[J] = tr_read8<2 * 128, 64 * 128>(p + (J >> 3) * 16384 + ((J >> 1) & 3) * 32 + (J & 1) * 16 * 128); });
    sfor<16>([&](auto J) { PIN(WO[J]); });
  }
  VISIT(u, 30, 31, 32, 33, R2);
  NEW_PAGE();
  BARRIER();
  BARRIER();
  int vp = lane < 16 ? (b * 32 + 8 * u + (lane >> 2)) * IN_BYTES + (lane & 3) * 16 : OOB;
  int lrd = lane * 16;
  int idx = (lane & 1) ? 0xEEEEEEEE : 0x44444444;
  PIN(vp); PIN(lrd); PIN(idx);
  const f32x4 zero4 = {0.f, 0.f, 0.f, 0.f};
  f32x4 d[8];
  d[0] = zero4; d[1] = zero4; PIN(d[0]); PIN(d[1]);
  int so1 = IN_P1 + h * 64, so2 = IN_P1 + h * 64 + 4 * IN_BYTES;
  PINS(so1); PINS(so2);
  if (PRIO) __builtin_amdgcn_s_setprio(2);
  BARRIER();
  TSTAMP(2);
  {
    f32x4 s4 = *(const LAS f32x4*)(L + L_RED2 + lrd) + *(const LAS f32x4*)(L + L_RED2 + 1024 + lrd);
    s4 += *(const LAS f32x4*)(L + L_RED2 + 2048 + lrd) + *(const LAS f32x4*)(L + L_RED2 + 3072 + lrd);
    const f32x2 o2 = f32x2{s4[0], s4[1]} + f32x2{s4[2], s4[3]};
    const unsigned op = cvtpk(o2[0], o2[1]);
    const u32x4 a0 = {op, dppu<SHL + 2>(op), dppu<SHL + 4>(op), dppu<SHL + 6>(op)};
    u32x4 a1;
    a1[0] = dppu<SHL + 8>(op); a1[1] = dppu<SHL + 10>(op);
    SB();
    d[0] = smf(a0, WO[0], d[0], idx);
    SB();
    a1[2] = dppu<SHL + 12>(op); a1[3] = dppu<SHL + 14>(op);
    SB();
    d[1] = smf(a0, WO[2], d[1], idx);
    SB();
    d[2] = zero4; PIN(d[2]);
    SB();
    d[0] = smf(a1, WO[1], d[0], idx);
    SB();
    d[3] = zero4; PIN(d[3]);
    SB();
    d[1] = smf(a1, WO[3], d[1], idx);
    SB();
    float y[8];
    auto fin = [&](auto T) { y[T] = d[T][0] + d[T][1]; PIN(y[T]); KEEP(d[T]); };
    sfor<6>([&](auto J) {
      constexpr int T = J + 2;
      d[T] = smf(a0, WO[2 * T], d[T], idx);
      SB();
      if constexpr (T + 2 < 8) { d[T + 2] = zero4; PIN(d[T + 2]); }
      if constexpr (T == 6)
        __builtin_amdgcn_raw_buffer_store_b128(u32x4{fbits(y[0]), fbits(y[1]), fbits(y[2]), fbits(y[3])}, rws, (EXPM & 8) ? OOB : vp, so1, AUX_SC1);
      SB();
      d[T] = smf(a1, WO[2 * T + 1], d[T], idx);
      SB();
      fin(IC(T - 2));
      SB();
    });
    fin(IC(6)); fin(IC(7));
    if (TAILST == 3) TSTAMP(4);
    __builtin_amdgcn_raw_buffer_store_b128(u32x4{fbits(y[4]), fbits(y[5]), fbits(y[6]), fbits(y[7])}, rws, (EXPM & 8) ? OOB : vp, so2, AUX_SC1);
    SB();
    if (TAILST == 3) TSTAMP(5); else TSTAMP(3);
    if (lrd < 256) {
      *(LAS f32x4*)(L + L_Y + u * 512 + lrd) = f32x4{y[0], y[1], y[2], y[3]};
      *(LAS f32x4*)(L + L_Y + u * 512 + 256 + lrd) = f32x4{y[4], y[5], y[6], y[7]};
    }
  }
  SB();
  STAMP(5);
  u32x8 W2[8];
  if (W2B) BARRIER();
  {
    const int ln = __builtin_amdgcn_mbcnt_hi(~0u, __builtin_amdgcn_mbcnt_lo(~0u, 0u)), rr = ln >> 4, qq = (ln >> 2) & 3;
    LAS unsigned char* p2 = L + L_W + u * 32768 + (ln & 3) * 8 + 8192 + (16 * (rr >> 1) + 8 * (rr & 1) + (qq >> 1) + 2 * (qq & 1)) * 128;
    LAS unsigned char* p3 = p2 + (32 - 16 * (rr >> 1)) * 128;
    sfor<8>([&](auto J) {
      constexpr int o = (J >> 2) * 16384 + (J & 3) * 32;
      const u32x2 t0 = tr_read(p2 + o), t1 = tr_read(p2 + o + 4 * 128), t2 = tr_read(p3 + o), t3 = tr_read(p3 + o + 4 * 128);
      W2[J] = u32x8{t0[0], t0[1], t1[0], t1[1], t2[0], t2[1], t3[0], t3[1]};
    });
    sfor<8>([&](auto J) { PIN(W2[J]); d[J] = zero4; PIN(d[J]); });
  }
  PAD(35, R2);
  so1 = IN_P2 + slice * 64; so2 = IN_P2 + slice * 64 + 4 * IN_BYTES;
  int lst = L_ST;
  float gk = -2.f * 1.4426950408889634f * 0.7978845608028654f;
  unsigned gmask = lrd < FS * 16 ? ~0u : 0u;
  PINS(so1); PINS(so2); PIN(lst); PIN(gk); PIN(gmask);
  SB();
  STAMP(6);
  if (!W2B) BARRIER();
  float c1;
  { const f32x4 t = *(const LAS f32x4*)(L + L_EX + lrd); c1 = (t[0] + t[1]) + (t[2] + t[3]); }
  PIN(c1);
  BARRIER();
  if (TAILST == 2) TSTAMP(8);
  {
    const f32x4 t4 = *(const LAS f32x4*)(L + L_EX2 + lrd);
    const f32x2 ms = *(const LAS f32x2*)(L + lst);
    const float tot = (t4[0] + t4[1]) + (t4[2] + t4[3]);
    const float uu = rbf(rbf(fmaf(-ms[0], c1, tot) * ms[1]) + b1v);
    const float z = uu * fmaf(uu * uu, -2.f * 1.4426950408889634f * 0.044715f * 0.7978845608028654f, gk);
    const float g = asf(fbits(uu) & gmask) * __builtin_amdgcn_rcpf(1.f + __builtin_amdgcn_exp2f(z));
    const unsigned gp = cvtpk(g, dppf<XOR2>(g));
    const u32x4 ag = {gp, dppu<SHL + 4>(gp), dppu<SHL + 8>(gp), dppu<SHL + 12>(gp)};
    if (TAILST == 2) { KEEP(ag); TSTAMP(9); }
    float y[8];
    auto fin = [&](auto T) { y[T] = d[T][0] + d[T][1]; PIN(y[T]); KEEP(d[T]); SB(); };
    sfor<8>([&](auto T) { d[T] = smf(ag, W2[T], d[T], idx); SB(); });
    fin(IC(0)); fin(IC(1)); fin(IC(2)); fin(IC(3));
    __builtin_amdgcn_raw_buffer_store_b128(u32x4{fbits(y[0]), fbits(y[1]), fbits(y[2]), fbits(y[3])}, rws, vp, so1, AUX_SC1);
    SB();
    fin(IC(4)); fin(IC(5)); fin(IC(6)); fin(IC(7));
    KEEP(y[0]); KEEP(y[1]); KEEP(y[2]); KEEP(y[3]);
    __builtin_amdgcn_raw_buffer_store_b128(u32x4{fbits(y[4]), fbits(y[5]), fbits(y[6]), fbits(y[7])}, rws, vp, so2, AUX_SC1);
  }
  asm volatile("KPCB:");
  SB();
  STAMP(9); TSTAMP(10);
  } else {
  const int grp = NG == 8 ? (b + 2 * (h >> 3)) & 7 : (NG == 16 ? (2 * b + 4 * (h >> 4) + ((h >> 3) & 1)) & 15 : 4 * b + (h >> 3));
  const int pq = QB0 + ((grp * QBS) >> 2), po = OB0 + ((grp * OBS) >> 2), pm = MB0 + ((grp * MBS) >> 2);
  const size_t kvoff = ((size_t)blk * SK + (SK - WIN)) * (DH * 2);
  const rsrc_t rk = mk_rsrc(kc + kvoff, WIN * DH * 2), rv = mk_rsrc(vc + kvoff, WIN * DH * 2);
  const int vkv = tid * 16;
  const int lds0 = (int)(unsigned)(size_t)L, ldsw = lds0 + L_W + w * 16384, ldsd = lds0 + L_DUMP;

  u32x4 K[32], V[32];
  auto ldk = [&](auto I) { ald128nt(K[I], rk, vkv, I * 8192); };
  auto ldv = [&](auto I) { ald128nt(V[I], rv, vkv, I * 8192); };
  ldk(IC(0)); ldk(IC(1));
  if (QOWN > 2) { ldk(IC(2)); ldk(IC(3)); }
  const unsigned long long tclk = __builtin_amdgcn_s_memrealtime();
  SB();
  asm volatile("KPCA:");
  const rsrc_t rq = mk_rsrc(wq + h * (DH * 2), D * NH * DH * 2 - h * (DH * 2));
  const int vq = (64 * w + r) * (NH * DH * 2) + c * 16;
  if (QOWN == 2) {
    dma1<QNT>(rq, vq, 2 * b * (4 * NH * DH * 2), ldsw + 2 * b * 1024);
    dma1<QNT>(rq, vq, (2 * b + 1) * (4 * NH * DH * 2), ldsw + (2 * b + 1) * 1024);
  }
  unsigned tch = 0;
  if (TOUCH) {
    const unsigned char* p = w == 0 ? vc + kvoff : w == 1 ? wo + (size_t)h * (DH * D * 2) : w == 2 ? w1 : w == 3 ? w2 + (size_t)min(col0, F - 1) * (D * 2)
                           : w == 4 ? w1 + (D - 1) * F * 2 : w == 5 ? (const unsigned char*)x : w == 6 ? (const unsigned char*)b2 : b1;
    ald32(tch, mk_rsrc(p, 4), lane == 0 ? 0 : OOB);
  }
  const rsrc_t ro = mk_rsrc(wo + (size_t)h * (DH * D * 2), DH * D * 2);
  const int vo = (lane >> 3) * (D * 2) + w * 128 + (lane & 7) * 16;
  const rsrc_t r1 = mk_rsrc(w1, D * F * 2);
  const int v1 = ((lane & 7) < 6 && col0 + (lane & 7) * 8 < F) ? (64 * w + (lane >> 3)) * (F * 2) + col0 * 2 + (lane & 7) * 16 : OOB;
  const int c2 = min(col0, F);
  const rsrc_t r2 = mk_rsrc(w2 + (size_t)c2 * (D * 2), (F - c2) * (D * 2));
  const int b6 = b < 6 ? b : b - 6;
  const int dith = (((w - 4) * DITH) >> 3) + ((((blk >> 3) - 16) * DITG) >> 5);
  unsigned tst = PEND ? (unsigned)tclk + TOFF + dith : 0u;
  auto hook = [&](auto I) {
    constexpr int i = I;
    if constexpr (i == 32) PAD(11, 16);
    if constexpr (i == 35) PAD(14, R1);
    if constexpr (i == 38) PAD(15, R1);
    if constexpr (i == 42) { NEW_PAGE(); PAD(20, R1); }
    if constexpr (i == 49) PAD(16, R1);
    if constexpr (i == 56) PAD(36, 35);
    if constexpr (i == 61) { NEW_PAGE(); PAD(30, 36); }
    if constexpr (PEND && i >= PFROM && i <= PTO && i % PEV == 0) {
      PINS(tst);
      const int pend = (i <= pq ? PBUR : 0) + (i <= po ? PBUR : 0) + (i <= pm ? PBUM : 0);
      constexpr int extra = (i <= OB0 - OWD ? 2 : 0) + (i <= MB0 - OWD ? 2 : 0);
      const unsigned tgt = tst + PEND - (67 - i + extra) * PTAU - pend;
      int tmp;
      asm volatile("s_memrealtime vcc\n\ts_waitcnt lgkmcnt(0)\n\ts_sub_i32 %0, %1, vcc_lo\n\ts_ashr_i32 %0, %0, %2\n\ts_cmp_lt_i32 %0, 3\n\ts_cbranch_scc1 PE%=\n\t"
                   "s_add_i32 %0, %0, vcc_lo\n"
                   "PC%=:\n\ts_memrealtime vcc\n\ts_waitcnt lgkmcnt(0)\n\ts_sub_i32 vcc_lo, vcc_lo, %0\n\ts_cmp_lt_i32 vcc_lo, 0\n\ts_cbranch_scc1 PC%=\nPE%=:"
                   : "=&s"(tmp) : "s"(tgt), "i"(PSH) : "scc", "vcc", "memory");
      SB();
    }
    constexpr auto site = [](int i_, int p0, int sp) { for (int g = 0; g < NG; ++g) if (p0 + ((g * sp) >> 2) == i_) return true; return false; };
    if constexpr (site(i - 4, QB0, QBS)) cond_barrier<i - 4>(pq);
    if constexpr (site(i - QRDY, QB0, QBS)) cond_barrier<i - QRDY>(pq);
    if constexpr (site(i - 4, OB0, OBS)) cond_barrier<i - 4>(po);
    if constexpr (site(i, MB0, MBS)) cond_barrier<i>(pm);
    if constexpr (QOWN > 2 && i == QOWN) { PACE(); dma1<QNT>(rq, vq, 2 * b * (4 * NH * DH * 2), ldsw + 2 * b * 1024); PACE(); dma1<QNT>(rq, vq, (2 * b + 1) * (4 * NH * DH * 2), ldsw + (2 * b + 1) * 1024); }
    if constexpr (site(i, QB0, QBS)) burst<i, 16, 14>(pq, rq, vq, 4 * NH * DH * 2, ldsw, (2 * b + 2) & 15);
    if constexpr (i == OB0 - OWD) { PACE(); dma1(ro, vo, 2 * b * 8192, ldsd); PACE(); dma1(ro, vo, (2 * b + 1) * 8192, ldsd); }
    if constexpr (site(i, OB0, OBS)) burst<i, 16>(po, ro, vo, 8 * D * 2, ldsw, 2 * b);
    if constexpr (i == MB0 - OWD) { PACE(); dma1(r1, v1, b * (8 * F * 2), ldsd); PACE(); dma1(r2, vo, b * (8 * D * 2), ldsd); }
    if constexpr (site(i, MB0, MBS)) {
      burst<i, 8>(pm, r1, v1, 8 * F * 2, ldsw, b);
      burst<i, 6>(pm, r2, vo, 8 * D * 2, ldsw + 8192, b6);
    }
  };
  static_assert(QRDY > 4 && QB0 + (NG - 1) * QBS / 4 + QRDY < QSTEP && OBS >= QBS && OB0 >= QB0 + QRDY && MB0 > OB0 + 4 &&
                MB0 + (NG - 1) * MBS / 4 > OB0 + (NG - 1) * OBS / 4 + 5 && MB0 + (NG - 1) * MBS / 4 <= CHKS && CHKS >= 52 && CHKS <= 63, "barrier order, staging area reuse");
  unsigned tx;
  constexpr int S0 = QOWN > 2 ? 4 : 2;
  sfor<QSTEP - S0>([&](auto I) { constexpr int i = I + S0; if constexpr (i == 11) ESTAMP(7); if constexpr (i == 9) ESTAMP(8); if constexpr (i == 3) ESTAMP(9); hook(IC(i));
    PACEN(WN + (TOUCH && i < 6 ? 1 : 0) + (TCOM && ((i > TSTEP && i < TSTEP + 4) || (i > MSTEP && i < MSTEP + 4)) ? 1 : 0)); ldk(IC(i));
    if constexpr (TCOM && i == MSTEP) {
      __builtin_amdgcn_raw_buffer_store_b32((unsigned)__builtin_amdgcn_s_memrealtime(), mk_rsrc(ws + WS_T, NREP * 4),
                                            (tid == 64 * MWAVE && (blk & 0xd0) == 0x80) ? (((blk >> 1) & 16) | (blk & 15)) * 4 : OOB, 0, AUX_SC1);
      SB();
    }
    if constexpr (TCOM && i == TSTEP) ald32sc(tx, mk_rsrc(ws + WS_T, NREP * 4), (lane & 31) * 4);
    if constexpr (TCOM && i == TSTEP + 4) {
      PIN(tx);
      const int dt = (int)(tx - (unsigned)tclk) - TOFF;
      const bool ok = dt > -400 && dt < 400;
      float sm = ok ? (lane < 32 ? (float)dt : 1.f) : 0.f;
      sm = row_sum(tsum16(sm, sm));
      const float cnt = asf(__builtin_amdgcn_readlane(fbits(sm), 32));
      int adj = (int)(sm * __builtin_amdgcn_rcpf(fmaxf(cnt, 1.f)));
      PIN(adj);
      tst += __builtin_amdgcn_readfirstlane(adj);
      SB();
    }
  });
  PIN(tch);
  NEW_PAGE(); PAD(10, 15);
  STAMP(1);
  unsigned qp[4];
  {
    const f32x4 s4 = *(const LAS f32x4*)(L + L_RED + lane * 16) + *(const LAS f32x4*)(L + L_RED + 1024 + lane * 16);
    rows_bcast(cvtpk(s4[0] + s4[2], s4[1] + s4[3]), qp);
  }
  SB();
  STAMP(2);
  float sc[2], xs[16];
  auto qk = [&](u32x4& kv) { PIN(kv); return dot2(kv[3], qp[3], dot2(kv[2], qp[2], dot2(kv[1], qp[1], dot2(kv[0], qp[0], 0.f)))); };
  static_assert(QSTEP <= 28, "");
  sfor<36 - QSTEP>([&](auto I) {
    constexpr int i = I + QSTEP;
    hook(IC(i));
    PACE();
    if constexpr (i < 32) ldk(IC(i)); else ldv(IC(i - 32));
    if constexpr (I < 3) sfor<(I < 2 ? 6 : 4)>([&](auto J) { xs[6 * I + J] = qk(K[6 * I + J]); PIN(xs[6 * I + J]); });
    if constexpr (I == 3) { sc[0] = transpose_reduce16(xs, c); PIN(sc[0]); }
    if constexpr (I >= 4) {
      constexpr int lo = (I == 4) ? 16 : (16 + 6 * (I - 4) < i - 4 ? 16 + 6 * (I - 4) : i - 4);
      constexpr int hi = (16 + 6 * (I - 3) < i - 3) ? 16 + 6 * (I - 3) : i - 3;
      sfor<hi - lo>([&](auto J) { xs[lo - 16 + J] = qk(K[lo + J]); PIN(xs[lo - 16 + J]); });
    }
    SB();
  });
  sc[1] = transpose_reduce16(xs, c);
  SB();
  hook(IC(36)); PACE(); ldv(IC(4));
  unsigned pp[2];
  float mw, e1;
  {
    const float s0 = rbf(rbf(sc[0]) * 0.08838834764831845f), s1 = rbf(rbf(sc[1]) * 0.08838834764831845f);
    mw = wave_max(fmaxf(s0, s1));
    const float e0 = __builtin_amdgcn_exp2f((s0 - mw) * 1.4426950408889634f);
    e1 = __builtin_amdgcn_exp2f((s1 - mw) * 1.4426950408889634f);
    pp[0] = cvtpk(e0, e0); pp[1] = cvtpk(e1, e1);
    SB();
    hook(IC(37)); PACE(); ldv(IC(5));
    *(LAS f32x2*)(L + L_ML + w * 8) = f32x2{mw, wave_sum(e0 + e1)};
  }
  SB();
  STAMP(3);
  f32x4 acc = {0.f, 0.f, 0.f, 0.f};
  u32x4 sel;
#pragma unroll
  for (int k = 0; k < 4; ++k) sel[k] = ((c & 7) >> 1) == k ? ((c & 1) ? 0xffff0000u : 0xffffu) : 0u;
  auto pv = [&](auto I) { PIN(V[I]); const unsigned pb = dppu<BCAST + (I & 15)>(pp[I >> 4]); acc = mfma(V[I], sel & pb, acc); PIN(acc); };
  const int ocol = 64 * (h >> 2) + 16 * r + 4 * (h & 3) + (c & 3);
  unsigned b2v, xo;
  int lw2 = (w >> 1) * 1024 + lane * 16 + (w & 1) * 8;
  u32x4 blast;
  sfor<29>([&](auto I) {
    constexpr int i = I + 6;
    hook(IC(i + 32));
    if constexpr (i < 32) { PACE(); ldv(IC(i)); }
    else PACEN(35 - i);
    if constexpr (i == 24) { ald16(b2v, mk_rsrc(b2, D * 2), w == 0 ? ocol * 2 : OOB, 0); ald16(xo, mk_rsrc(x, NB * D * 2), w == 0 ? ocol * 2 : OOB, b * (D * 2)); PIN(lw2); }
    if constexpr (OTOUCH && i == 8) { __builtin_amdgcn_raw_buffer_store_b16((unsigned short)0, mk_rsrc(out, NB * D * 2), (w == 0 && c < 4) ? (b * D + ocol) * 2 : OOB, 0, 0); SB(); }
    if constexpr (i == 6) { pv(IC(0)); pv(IC(1)); }
    if constexpr (i + 32 == CHKS) {
      BARRIER();
      const f32x2 ml = *(const LAS f32x2*)(L + L_ML + (lane & 7) * 8);
      float M = ml[0];
      M = fmaxf(M, dppf<XOR1>(M)); M = fmaxf(M, dppf<XOR2>(M)); M = fmaxf(M, dppf<HMIR>(M));
      float Ls = ml[1] * __builtin_amdgcn_exp2f((ml[0] - M) * 1.4426950408889634f);
      Ls += dppf<XOR1>(Ls); Ls += dppf<XOR2>(Ls); Ls += dppf<HMIR>(Ls);
      const float scale = __builtin_amdgcn_exp2f((mw - M) * 1.4426950408889634f) / Ls;
      acc *= scale;
      pp[1] = cvtpk(e1 * scale, e1 * scale);
    }
    pv(IC(i - 4));
    SB();
  });
  blast = sel & dppu<BCAST + 15>(pp[1]);
  PIN(blast);
  PACEN(0);
  STAMP(4); TSTAMP(1);
  PIN(V[31]);
  acc = mfma(V[31], blast, acc);
  *(LAS f32x2*)(L + L_RED2 + lw2) = (c & 8) ? f32x2{acc[2], acc[3]} : f32x2{acc[0], acc[1]};
  BARRIER();
  PIN(b2v); PIN(xo);
  if (w == 0) {
    PAD(32, R2);
    const rsrc_t rws = mk_rsrc(ws, WS_BYTES);
    int vin = blk * IN_BYTES + lane * 16;
    auto init_p2 = [&]() {
      u32x4 sent4 = {SENT, SENT, SENT, SENT};
      PIN(sent4);
      __builtin_amdgcn_raw_buffer_store_b128(sent4, rws, vin, IN_P2, AUX_SC1);
      __builtin_amdgcn_raw_buffer_store_b128(sent4, rws, vin, IN_P2 + 1024, AUX_SC1);
      asm volatile("s_nop 1" :: "v"(sent4) : "memory");
    };
    if (SENTP == 1) init_p2();
    int vhs = HST == 0 ? (b * 32 + c) * IN_BYTES + IN_H + h * 32 + r * 8 : (b * 32 + (lane >> 1)) * IN_BYTES + IN_H + h * 32 + (lane & 1) * 16;
    int vout = c < 4 ? (b * D + ocol) * 2 : OOB;
    b2v <<= 16; xo <<= 16;
    PIN(vin); PIN(vhs); PIN(vout); PIN(b2v); PIN(xo);
    if (SENTP >= 2) { __builtin_amdgcn_s_sleep(SENTP); init_p2(); __builtin_amdgcn_s_sleep(DLY0 - SENTP); }
    else if (DLY0) __builtin_amdgcn_s_sleep(DLY0);
    if (PRIO) __builtin_amdgcn_s_setprio(3);
    float g1 = gather16(rws, vin + IN_P1);
    if (TAILST && TAILST != 3) { PIN(g1); TSTAMP(4); }
    PACEN(0);
    const float hf = asf(xo) + rbf(g1);
    const unsigned hq = cvtpk(hf, dppf<XOR1>(hf));
    unsigned hb[4];
    rows_bcast(dppu<0x88>(hq), hb);
    __builtin_amdgcn_raw_buffer_store_b128(u32x4{hb[0], hb[1], hb[2], hb[3]}, rws, vhs, 0, AUX_SC1);
    SB();
    STAMP(7); if (TAILST != 3) TSTAMP(5);
    BARRIER();
    PAD(33, R2);
    float hres = asf(hq << 16);
    PIN(hres);
    if (DLY1) __builtin_amdgcn_s_sleep(DLY1);
    int lst = L_ST;
    PIN(lst);
#pragma unroll 1
    for (unsigned spins = 0;; ++spins) {
      asm volatile("" ::: "memory");
      const u32x4 hr = __builtin_amdgcn_raw_buffer_load_b128(rws, vin + IN_H, 0, AUX_SC1);
      float s1 = 0.f, s2 = 0.f;
      PIN(s1); PIN(s2);
#pragma unroll
      for (int k = 0; k < 4; ++k) { s1 = dot2(hr[k], 0x3b003b00u, s1); s2 = dot2(hr[k], hr[k], s2); }
      float t = tsum32(s1, s2);
      t = row_sum(tsum16(t, t));
      const auto ms = __builtin_amdgcn_permlane32_swap(fbits(t), fbits(t), false, false);
      const float mean = asf(ms[0]);
      const float var = fmaxf(fmaf(asf(ms[1]), 1.f / D, -(mean * mean)), 0.f);
      const float den = rbf(__builtin_amdgcn_sqrtf(rbf(rbf(var) + 1e-5f)));
      *(LAS f32x2*)(L + lst) = f32x2{rbf(mean), __builtin_amdgcn_rcpf(den)};
      if (__builtin_expect(!any_nan(t), 1)) break;
      if (!any_sent(max4(hr)) || spins >= SPIN_CAP) break;
      if ((spins & 63u) == 63u) {
        const int vr = (b * 32 + (lane >> 2)) * IN_BYTES + IN_P1 + h * 64 + (lane & 3) * 16;
        __builtin_amdgcn_raw_buffer_store_b128(*(const LAS u32x4*)(L + L_Y + lane * 16), rws, vr, 0, AUX_SC1);
        __builtin_amdgcn_raw_buffer_store_b128(*(const LAS u32x4*)(L + L_Y + 1024 + lane * 16), rws, vr, 16 * IN_BYTES, AUX_SC1);
      }
    }
    if (TAILST == 1) TSTAMP(9);
    BARRIER();
    if (DLY2) __builtin_amdgcn_s_sleep(DLY2);
    const float res = hres + rbf(rbf(gather16(rws, vin + IN_P2)) + asf(b2v));
    __builtin_amdgcn_raw_buffer_store_b16(__builtin_bit_cast(unsigned short, (__bf16)res), mk_rsrc(out, NB * D * 2), vout, 0, 0);
    STAMP(10); TSTAMP(11);
  } else if (w >= 4) {
    PAD(31, R2);
    const int u = w - 4, q4 = (lane >> 2) & 3;
    const rsrc_t rws = mk_rsrc(ws, WS_BYTES);
    __builtin_amdgcn_s_sleep(UD1); SB();
    u32x8 W1[6];
    LAS unsigned char* p1 = L + L_W + u * 32768 + (lane & 3) * 8 + (4 * (r >> 1) + 32 * (r & 1) + q4) * 128;
    sfor<6>([&](auto J) { W1[J] = tr_read8<16 * 128, 8 * 128>(p1 + (J & 1) * 16384 + (J >> 1) * 32); });
    sfor<6>([&](auto J) { PIN(W1[J]); });
    SB(); __builtin_amdgcn_s_sleep(UD2); SB();
    int vhl = blk * IN_BYTES + IN_H + u * 256 + r * 32 + (lane & 1) * 16, lwr = lane * 16 + u * 4;
    int idx = (lane & 1) ? 0xEEEEEEEE : 0x44444444;
    const f32x4 zero4 = {0.f, 0.f, 0.f, 0.f};
    f32x4 e[3];
    auto project = [&](u32x4 h0, u32x4 h1) {
      sfor<3>([&](auto T) { e[T] = smf(h0, W1[2 * T], e[T], idx); SB(); });
      sfor<3>([&](auto T) { e[T] = smf(h1, W1[2 * T + 1], e[T], idx); SB(); });
      float a0 = e[0][0] + e[0][1], a1 = e[1][0] + e[1][1];
      a0 = r == 0 ? a0 : a1;
      PIN(a0);
      const float a2 = e[2][0] + e[2][1];
      KEEP(e[0]); KEEP(e[1]); KEEP(e[2]);
      return r >= 2 ? a2 : a0;
    };
    const u32x4 ones = {0x3f803f80u, 0x3f803f80u, 0x3f803f80u, 0x3f803f80u};
    sfor<3>([&](auto T) { e[T] = zero4; });
    *(LAS float*)(L + L_EX + lwr) = project(ones, ones);
    sfor<3>([&](auto T) { e[T] = zero4; PIN(e[T]); });
    PIN(vhl); PIN(lwr); PIN(idx);
    STAMP(6);
    BARRIER();
    if (DLY1) __builtin_amdgcn_s_sleep(DLY1);
#pragma unroll 1
    for (unsigned spins = 0;; ++spins) {
      asm volatile("" ::: "memory");
      const u32x4 h0 = __builtin_amdgcn_raw_buffer_load_b128(rws, vhl, 0, AUX_SC1);
      const u32x4 h1 = __builtin_amdgcn_raw_buffer_load_b128(rws, vhl + 128, 0, AUX_SC1);
      const float a = project(h0, h1);
      *(LAS float*)(L + L_EX2 + lwr) = a;
      if (__builtin_expect(!any_nan(a), 1)) break;
      if (!any_sent(max(max4(h0), max4(h1))) || spins >= SPIN_CAP) break;
      sfor<3>([&](auto T) { e[T] = zero4; PIN(e[T]); });
    }
    if (TAILST == 1) TSTAMP(8);
    BARRIER();
  }
  }
}

extern "C" void kernel_launch(void* const* d_in, const int* in_sizes, int n_in, void* d_out, int out_size, void* d_ws,
                              size_t ws_size, hipStream_t stream) {
  fused_block<<<NB * NH, 768, 0, stream>>>((const unsigned char*)d_in[3], (const unsigned char*)d_in[1], (const unsigned char*)d_in[4],
                                      (const unsigned char*)d_in[2], (const unsigned short*)d_in[0], (const unsigned char*)d_in[6],
                                      (const unsigned char*)d_in[7], (const unsigned char*)d_in[8], (const unsigned short*)d_in[9],
                                      (unsigned short*)d_out, (unsigned char*)d_ws);
}
```
